```python
import torch

def _softmax(x, axis=-1):
    return torch.softmax(x, dim=axis)

def _gelu(x):
    return torch.nn.functional.gelu(x, approximate='tanh')

def _silu(x):
    return torch.nn.functional.silu(x)

def _sigmoid(x):
    return torch.sigmoid(x)

def _layernorm(x, axis=-1, eps=1e-05):
    m = x.mean(dim=axis, keepdim=True)
    v = x.var(dim=axis, keepdim=True, unbiased=False)
    return (x - m) / torch.sqrt(v + eps)

def _scatter_add(tgt, idx, src, axis=0):
    return tgt.clone().index_add_(axis, idx, src)

def _segment_sum(src, seg, n):
    out = torch.zeros((n,) + src.shape[1:], dtype=src.dtype)
    return out.index_add_(0, seg, src)

def _pad(x, pw):
    flat = tuple((v for pair in reversed(pw) for v in pair))
    return torch.nn.functional.pad(x, flat)
kernel_name = 'interleave_layers__memory_aug_recurrent_mem_tr__swiglu_ffn__prompt'

def setup_inputs():

    _w = lambda a: (lambda t: t.to(torch.bfloat16) if t.is_floating_point() else t)(torch.as_tensor(a))

    def _A_setup():
        B, S, Dm = (1, 2048, 512)
        H, Dh = (32, 128)
        M, seg = (8, 32)
        seg = min(seg, S)
        S = S // seg * seg
        return {'x': _w((lambda *s: torch.randn(*s, dtype=torch.bfloat16) * 0.05)(B, S, Dm)), 'mem0': _w((lambda *s: torch.randn(*s, dtype=torch.bfloat16) * 0.8)(B, M, Dm)), 'Wq': _w((lambda *s: torch.randn(*s, dtype=torch.bfloat16) * 0.8)(Dm, H, Dh) * Dm ** (-0.5)), 'Wk': _w((lambda *s: torch.randn(*s, dtype=torch.bfloat16) * 0.8)(Dm, H, Dh) * Dm ** (-0.5)), 'Wv': _w((lambda *s: torch.randn(*s, dtype=torch.bfloat16) * 0.8)(Dm, H, Dh) * Dm ** (-0.5)), 'Wo': _w((lambda *s: torch.randn(*s, dtype=torch.bfloat16) * 0.8)(H, Dh, Dm) * (H * Dh) ** (-0.5)), 'seg_len': seg}
    _A_in = _A_setup()

    def _B_setup():
        B, S, D, F = (1, 2048, 512, 1376)
        return {'x': _w((lambda *s: torch.randn(*s, dtype=torch.bfloat16) * 0.05)(B, S, D)), 'w_gate': _w((lambda *s: torch.randn(*s, dtype=torch.bfloat16) * 0.05)(D, F)), 'w_up': _w((lambda *s: torch.randn(*s, dtype=torch.bfloat16) * 0.05)(D, F)), 'w_down': _w((lambda *s: torch.randn(*s, dtype=torch.bfloat16) * 0.05)(F, D))}
    _B_in = _B_setup()
    return {'A_x': _A_in['x'], 'A_mem0': _A_in['mem0'], 'A_Wq': _A_in['Wq'], 'A_Wk': _A_in['Wk'], 'A_Wv': _A_in['Wv'], 'A_Wo': _A_in['Wo'], 'A_seg_len': _A_in['seg_len'], 'B_x': _B_in['x'], 'B_w_gate': _B_in['w_gate'], 'B_w_up': _B_in['w_up'], 'B_w_down': _B_in['w_down']}

def reference(A_x, A_mem0, A_Wq, A_Wk, A_Wv, A_Wo, A_seg_len, B_x, B_w_gate, B_w_up, B_w_down):

    def _A_ref(x, mem0, Wq, Wk, Wv, Wo, seg_len):
        B, S, Dm = x.shape
        H, Dh = (Wq.shape[1], Wq.shape[2])
        M = mem0.shape[1]
        scale = Dh ** (-0.5)
        n_seg = S // seg_len
        L = M + seg_len
        idx = torch.arange(L)
        mask = (idx[:, None] < M) | (idx[:, None] >= idx[None, :])
        mem = mem0
        outs = []
        for s in range(n_seg):
            xs = x[:, s * seg_len:(s + 1) * seg_len, :].clone()
            h = torch.concatenate([mem, xs], axis=1)
            q = torch.einsum('btd,dhe->bhte', h, Wq)
            k = torch.einsum('btd,dhe->bhte', h, Wk)
            v = torch.einsum('btd,dhe->bhte', h, Wv)
            scores = torch.einsum('bhqe,bhke->bhqk', q, k) * scale
            scores = torch.where(mask[None, None], scores, -1000000000.0)
            p = _softmax(scores, axis=-1)
            o = torch.einsum('bhqk,bhke->bhqe', p, v)
            o = torch.einsum('bhte,hed->btd', o, Wo)
            mem = o[:, :M, :].clone()
            outs.append(o[:, M:, :])
        return torch.concatenate(outs, axis=1)

    def _B_ref(x, w_gate, w_up, w_down):
        g = _silu(x @ w_gate)
        u = x @ w_up
        return g * u @ w_down
    x = A_x
    x = x + _A_ref(_layernorm(x, axis=-1), A_mem0, A_Wq, A_Wk, A_Wv, A_Wo, A_seg_len)
    x = x + _B_ref(_layernorm(x, axis=-1), B_w_gate, B_w_up, B_w_down)
    x = x + _A_ref(_layernorm(x, axis=-1), A_mem0, A_Wq, A_Wk, A_Wv, A_Wo, A_seg_len)
    x = x + _B_ref(_layernorm(x, axis=-1), B_w_gate, B_w_up, B_w_down)
    return x
```

```cpp
#define PD1 13
#define PD2 7
#define PD3 2307
#define PLEAD 12
#define SLP 3
#define PDL 6156
#define PWARM 515
#define HAL 1
#define E1 3
#define TCAL 650
#define FK 2
#define E4 1
#define W4 3
#define F1 1
#define TS 58
#define SPK 31
#define RS 2
#include <hip/hip_runtime.h>
#include <stdint.h>
typedef unsigned u4 __attribute__((ext_vector_type(4)));
typedef unsigned u2 __attribute__((ext_vector_type(2)));
typedef unsigned u8v __attribute__((ext_vector_type(8)));
typedef float f4 __attribute__((ext_vector_type(4)));
typedef float f2 __attribute__((ext_vector_type(2)));
typedef __bf16 bf2 __attribute__((ext_vector_type(2)));
typedef __bf16 bf8 __attribute__((ext_vector_type(8)));
typedef __bf16 bf16v __attribute__((ext_vector_type(16)));
typedef short s4 __attribute__((ext_vector_type(4)));
typedef __amdgpu_buffer_rsrc_t rsrc_t;
#define DEVI __device__ __forceinline__
#define SB() __builtin_amdgcn_sched_barrier(0)
#define MEMB() asm volatile("" ::: "memory")
constexpr int SC1 = 16;
constexpr unsigned SENT = 0xFFFFFFFFu;
constexpr unsigned SPIN_CAP = 1u << 17;

constexpr int REP_OFF = 64 * 16384 + 4096;
constexpr int FLAG_OFF = 72 * 16384;
constexpr int CTL_OFF = FLAG_OFF + 10 * 16384;
constexpr int INIT_BYTES = CTL_OFF + 32 * 128;
constexpr unsigned NCLR = 46;
constexpr int ARR_OFF = 31 * 65536;
constexpr int PAGE = 2 << 20, BLK = 16384;
constexpr int A_QKV = 0, A_O = 96 * BLK;
constexpr int B_WP = 0, B_A = 32 * BLK, B_X1 = 38 * BLK, B_XN1 = 48 * BLK, B_XN = 52 * BLK, B_X2 = 56 * BLK, B_XN0 = 60 * BLK, AH = 64;
constexpr int ST0_OFF = 97 * PAGE;
constexpr int END_OFF = 127 * PAGE;
#define FS 128
constexpr int OFS = 9 * 16384;
enum { F_O, F_WP, F_X1, F_A, F_X2, F_XN };

DEVI rsrc_t mkrs(const void* p, unsigned bytes) { return __builtin_amdgcn_make_buffer_rsrc((void*)p, 0, (int)bytes, 0x00020000); }
DEVI u4 ld16(rsrc_t r, int v, int s) { return __builtin_amdgcn_raw_buffer_load_b128(r, v, s, 0); }
DEVI u4 ld16c(rsrc_t r, int v, int s) { return __builtin_amdgcn_raw_buffer_load_b128(r, v, s, SC1); }
DEVI unsigned ld4(rsrc_t r, int v, int s) { return __builtin_amdgcn_raw_buffer_load_b32(r, v, s, 0); }
DEVI unsigned ld4c(rsrc_t r, int v, int s) { return __builtin_amdgcn_raw_buffer_load_b32(r, v, s, SC1); }
DEVI void st16c(u4 x, rsrc_t r, int v, int s) { __builtin_amdgcn_raw_buffer_store_b128(x, r, v + s, 0, SC1); }
DEVI void st4c(unsigned x, rsrc_t r, int v, int s) { __builtin_amdgcn_raw_buffer_store_b32(x, r, v, s, SC1); }
DEVI void drain() { asm volatile("s_waitcnt vmcnt(0)" ::: "memory"); }
DEVI void wgbar() { asm volatile("s_waitcnt lgkmcnt(0)\n\ts_barrier" ::: "memory"); }
DEVI unsigned plo(unsigned a, unsigned b) { return __builtin_amdgcn_perm(b, a, 0x05040100u); }
DEVI unsigned phi(unsigned a, unsigned b) { return __builtin_amdgcn_perm(b, a, 0x07060302u); }
DEVI unsigned pk(float a, float b) { bf2 r = __builtin_convertvector(f2{a, b}, bf2); return __builtin_bit_cast(unsigned, r); }
DEVI float asf(unsigned x) { return __builtin_bit_cast(float, x); }
DEVI unsigned asu(float x) { return __builtin_bit_cast(unsigned, x); }
DEVI f4 mfma32(u4 a, u4 b, f4 c) { return __builtin_amdgcn_mfma_f32_16x16x32_bf16(__builtin_bit_cast(bf8, a), __builtin_bit_cast(bf8, b), c, 0, 0, 0); }
DEVI f4 mfma16(u2 a, u2 b, f4 c) { return __builtin_amdgcn_mfma_f32_16x16x16bf16_1k(__builtin_bit_cast(s4, a), __builtin_bit_cast(s4, b), c, 0, 0, 0); }
DEVI f4 smf(u4 a, u8v b, f4 c, int idx) { return __builtin_amdgcn_smfmac_f32_16x16x64_bf16(__builtin_bit_cast(bf8, a), __builtin_bit_cast(bf16v, b), c, idx, 0, 0); }
DEVI float fold32(float a, float b) { auto r = __builtin_amdgcn_permlane32_swap(asu(a), asu(b), false, false); return asf(r[0]) + asf(r[1]); }
DEVI float max32(float a) { auto r = __builtin_amdgcn_permlane32_swap(asu(a), asu(a), false, false); return fmaxf(asf(r[0]), asf(r[1])); }
DEVI float max16(float a) { auto r = __builtin_amdgcn_permlane16_swap(asu(a), asu(a), false, false); return fmaxf(asf(r[0]), asf(r[1])); }
DEVI float sum32(float a) { auto r = __builtin_amdgcn_permlane32_swap(asu(a), asu(a), false, false); return asf(r[0]) + asf(r[1]); }
DEVI float sum16(float a) { auto r = __builtin_amdgcn_permlane16_swap(asu(a), asu(a), false, false); return asf(r[0]) + asf(r[1]); }
DEVI float ex2(float x) { return __builtin_amdgcn_exp2f(x); }
DEVI unsigned umax4(u4 v) { return max(max(v.x, v.y), max(v.z, v.w)); }
DEVI bool anyl(bool b) { return __builtin_amdgcn_ballot_w64(b) != 0; }
DEVI unsigned xcc_id() { return (unsigned)__builtin_amdgcn_s_getreg(20 | (3 << 11)) & 0xfu; }
#define E2 0
#define E3 0
#define E5 0
#define RD 64
#define RDB 64
DEVI int item_a(int it, int par) { const int pg = (RD == 64 ? 1 : RD) + (it & (RD - 1)); return pg * PAGE + ((par ^ (it >> 6) ^ pg) & 1) * 8192; }
DEVI int item_q(int it, int par) { return item_a(E2 == 2 ? it ^ 64 : E2 ? it | 64 : it, par); }
DEVI int item_o(int it, int par) { return item_a(E3 == 2 ? it ^ 64 : E3 == 3 ? it | 64 : E3 ? it & 63 : it, par) + A_O; }
DEVI int item_b(int it, int par) { const int pg = 65 + ((it & (RDB - 1)) >> 1); return pg * PAGE + (it & 1) * (PAGE / 2) + ((par ^ (E1 & 1 ? 0 : it >> 6) ^ pg) & 1) * 8192; }
DEVI int item_c(int it, int par) { const int pg = 97 + ((it & (RDB - 1)) >> 2); return pg * PAGE + (it & 3) * (PAGE / 4) + (E1 > 1 ? (it >> 6) * 16 * BLK : 0) + ((par ^ (E1 ? (E5 & 2) && it >= 64 && it < 120 : it >> 6) ^ pg) & 1) * 8192; }
DEVI int item_w(int it, int par) {
  const int pg = 97 + (it >> 2);
  return E1 < 2 || it < 64 ? item_b(it, par) : it >= 120 ? item_b(it - 120, par) : pg * PAGE + (it & 3) * (PAGE / 4) + ((par ^ pg ^ E5) & 1) * 8192;
}
constexpr int XNH = E4 ? 0 : 8192;
DEVI int item_xn(int it, int par) { return it < 64 ? 65 * PAGE + it * (PAGE / 2) + B_XN0 : (item_b(it & 63, par) ^ XNH) + B_XN; }
DEVI int item_x2(int s, int par) { return FK > 1 ? item_xn(64 + s, par) : item_b(s, par) + B_X2; }
DEVI int rowoff(int r) { return (r >> 3) * BLK + (r & 7) * 1024; }
DEVI int flg(int kind, int layer, int par) { return kind == F_XN ? FLAG_OFF + (layer ? 5 * BLK + ((par ^ 1) & 1) * 8192 : 6 * BLK) : FLAG_OFF + kind * BLK + (par & 1) * 8192 + layer * 4096; }
DEVI int flg_qkv(int layer, int h, int par) { return FLAG_OFF + (7 + (h >> 4)) * BLK + ((par ^ layer) & 1) * 8192 + (h & 15) * 512; }
DEVI int topo(int x) { return (x & 1) << 14 | (x & 6) << 11; }
#define CHAIN_ASM \
  "s_nop 4\n" \
  "s_mov_b32 s83, 0x20000\n" \
  "s_mov_b32 s84, 0\n" \
  "s_mov_b32 s89, 40000\n" \
  "s_mov_b32 s90, -1\n" \
  "s_mov_b32 s91, 0x3e0293ee\n" \
  "s_mov_b32 s98, 0\n" \
  "s_mov_b32 s99, 0\n" \
  "s_mov_b32 s100, 0\n" \
  "s_mov_b32 s101, 0\n" \
  "s_mov_b32 s30, 0x3f803f80\n" \
  "s_mov_b32 s31, 0x3f803f80\n" \
  "s_mov_b64 s[80:81], s[48:49]\n" \
  "s_mov_b32 s82, 8192\n" \
  "s_lshl_b32 s92, s50, 11\n" \
  "s_add_u32 m0, s92, 16384\n" \
  "s_lshl_b32 s93, s50, 8\n" \
  "buffer_load_dwordx4 v20, s[80:83], s93 offen lds\n" \
  "s_add_u32 m0, m0, 1024\n" \
  "s_add_u32 s93, s93, 128\n" \
  "buffer_load_dwordx4 v20, s[80:83], s93 offen lds\n" \
  "s_lshl_b32 s94, s50, 12\n" \
  "s_add_u32 s94, s94, 32768\n" \
  "s_mov_b64 s[80:81], s[40:41]\n" \
  "s_mov_b32 s82, 4194304\n" \
  "s_add_u32 m0, s94, 0\n" \
  "s_mov_b32 s92, 2097152\n" \
  "buffer_load_dwordx4 v16, s[80:83], s92 offen offset:0 lds\n" \
  "s_mov_b32 s93, 2112512\n" \
  "buffer_load_dwordx4 v16, s[80:83], s93 offen offset:1024 lds\n" \
  "s_mov_b32 s92, 2160640\n" \
  "buffer_load_dwordx4 v16, s[80:83], s92 offen offset:2048 lds\n" \
  "s_mov_b32 s93, 2176000\n" \
  "buffer_load_dwordx4 v16, s[80:83], s93 offen offset:3072 lds\n" \
  "s_add_u32 m0, s94, 16384\n" \
  "s_mov_b32 s92, 2621440\n" \
  "buffer_load_dwordx4 v16, s[80:83], s92 offen offset:0 lds\n" \
  "s_mov_b32 s93, 2636800\n" \
  "buffer_load_dwordx4 v16, s[80:83], s93 offen offset:1024 lds\n" \
  "s_mov_b32 s92, 2684928\n" \
  "buffer_load_dwordx4 v16, s[80:83], s92 offen offset:2048 lds\n" \
  "s_mov_b32 s93, 2700288\n" \
  "buffer_load_dwordx4 v16, s[80:83], s93 offen offset:3072 lds\n" \
  "s_add_u32 m0, s94, 32768\n" \
  "s_mov_b32 s92, 3145728\n" \
  "buffer_load_dwordx4 v16, s[80:83], s92 offen offset:0 lds\n" \
  "s_mov_b32 s93, 3161088\n" \
  "buffer_load_dwordx4 v16, s[80:83], s93 offen offset:1024 lds\n" \
  "s_mov_b32 s92, 3209216\n" \
  "buffer_load_dwordx4 v16, s[80:83], s92 offen offset:2048 lds\n" \
  "s_mov_b32 s93, 3224576\n" \
  "buffer_load_dwordx4 v16, s[80:83], s93 offen offset:3072 lds\n" \
  "s_add_u32 m0, s94, 49152\n" \
  "s_mov_b32 s92, 3670016\n" \
  "buffer_load_dwordx4 v16, s[80:83], s92 offen offset:0 lds\n" \
  "s_mov_b32 s93, 3685376\n" \
  "buffer_load_dwordx4 v16, s[80:83], s93 offen offset:1024 lds\n" \
  "s_mov_b32 s92, 3733504\n" \
  "buffer_load_dwordx4 v16, s[80:83], s92 offen offset:2048 lds\n" \
  "s_mov_b32 s93, 3748864\n" \
  "buffer_load_dwordx4 v16, s[80:83], s93 offen offset:3072 lds\n" \
  "s_mov_b64 s[80:81], s[42:43]\n" \
  "s_mov_b32 s82, 4194304\n" \
  "s_add_u32 m0, s94, 65536\n" \
  "s_mov_b32 s92, 0\n" \
  "buffer_load_dwordx4 v16, s[80:83], s92 offen offset:0 lds\n" \
  "s_mov_b32 s93, 15360\n" \
  "buffer_load_dwordx4 v16, s[80:83], s93 offen offset:1024 lds\n" \
  "s_mov_b32 s92, 63488\n" \
  "buffer_load_dwordx4 v16, s[80:83], s92 offen offset:2048 lds\n" \
  "s_mov_b32 s93, 78848\n" \
  "buffer_load_dwordx4 v16, s[80:83], s93 offen offset:3072 lds\n" \
  "s_add_u32 m0, s94, 81920\n" \
  "s_mov_b32 s92, 524288\n" \
  "buffer_load_dwordx4 v16, s[80:83], s92 offen offset:0 lds\n" \
  "s_mov_b32 s93, 539648\n" \
  "buffer_load_dwordx4 v16, s[80:83], s93 offen offset:1024 lds\n" \
  "s_mov_b32 s92, 587776\n" \
  "buffer_load_dwordx4 v16, s[80:83], s92 offen offset:2048 lds\n" \
  "s_mov_b32 s93, 603136\n" \
  "buffer_load_dwordx4 v16, s[80:83], s93 offen offset:3072 lds\n" \
  "s_add_u32 m0, s94, 98304\n" \
  "s_mov_b32 s92, 1048576\n" \
  "buffer_load_dwordx4 v16, s[80:83], s92 offen offset:0 lds\n" \
  "s_mov_b32 s93, 1063936\n" \
  "buffer_load_dwordx4 v16, s[80:83], s93 offen offset:1024 lds\n" \
  "s_mov_b32 s92, 1112064\n" \
  "buffer_load_dwordx4 v16, s[80:83], s92 offen offset:2048 lds\n" \
  "s_mov_b32 s93, 1127424\n" \
  "buffer_load_dwordx4 v16, s[80:83], s93 offen offset:3072 lds\n" \
  "s_waitcnt vmcnt(24) lgkmcnt(0)\n" \
  "s_barrier\n" \
  "s_add_u32 m0, s94, 114688\n" \
  "s_mov_b32 s92, 1572864\n" \
  "buffer_load_dwordx4 v16, s[80:83], s92 offen offset:0 lds\n" \
  "s_mov_b32 s93, 1588224\n" \
  "buffer_load_dwordx4 v16, s[80:83], s93 offen offset:1024 lds\n" \
  "s_mov_b32 s92, 1636352\n" \
  "buffer_load_dwordx4 v16, s[80:83], s92 offen offset:2048 lds\n" \
  "s_mov_b32 s93, 1651712\n" \
  "buffer_load_dwordx4 v16, s[80:83], s93 offen offset:3072 lds\n" \
  "v_add_u32 v24, 0, v18\n" \
  "v_xor_b32 v25, 32, v24\n" \
  "ds_read_b64_tr_b16 a[0:1], v24 offset:0\n" \
  "ds_read_b64_tr_b16 a[2:3], v24 offset:1024\n" \
  "ds_read_b64_tr_b16 a[4:5], v24 offset:4096\n" \
  "ds_read_b64_tr_b16 a[6:7], v24 offset:5120\n" \
  "ds_read_b64_tr_b16 a[8:9], v25 offset:0\n" \
  "ds_read_b64_tr_b16 a[10:11], v25 offset:1024\n" \
  "ds_read_b64_tr_b16 a[12:13], v25 offset:4096\n" \
  "ds_read_b64_tr_b16 a[14:15], v25 offset:5120\n" \
  "s_waitcnt vmcnt(24) lgkmcnt(0)\n" \
  "s_barrier\n" \
  "s_add_u32 m0, s94, 0\n" \
  "s_mov_b32 s92, 2097152\n" \
  "buffer_load_dwordx4 v16, s[80:83], s92 offen offset:0 lds\n" \
  "s_mov_b32 s93, 2112512\n" \
  "buffer_load_dwordx4 v16, s[80:83], s93 offen offset:1024 lds\n" \
  "s_mov_b32 s92, 2160640\n" \
  "buffer_load_dwordx4 v16, s[80:83], s92 offen offset:2048 lds\n" \
  "s_mov_b32 s93, 2176000\n" \
  "buffer_load_dwordx4 v16, s[80:83], s93 offen offset:3072 lds\n" \
  "v_add_u32 v24, 16384, v18\n" \
  "v_xor_b32 v25, 32, v24\n" \
  "ds_read_b64_tr_b16 a[16:17], v24 offset:0\n" \
  "ds_read_b64_tr_b16 a[18:19], v24 offset:1024\n" \
  "ds_read_b64_tr_b16 a[20:21], v24 offset:4096\n" \
  "ds_read_b64_tr_b16 a[22:23], v24 offset:5120\n" \
  "ds_read_b64_tr_b16 a[24:25], v25 offset:0\n" \
  "ds_read_b64_tr_b16 a[26:27], v25 offset:1024\n" \
  "ds_read_b64_tr_b16 a[28:29], v25 offset:4096\n" \
  "ds_read_b64_tr_b16 a[30:31], v25 offset:5120\n" \
  "s_waitcnt vmcnt(24) lgkmcnt(0)\n" \
  "s_barrier\n" \
  "s_add_u32 m0, s94, 16384\n" \
  "s_mov_b32 s92, 2621440\n" \
  "buffer_load_dwordx4 v16, s[80:83], s92 offen offset:0 lds\n" \
  "s_mov_b32 s93, 2636800\n" \
  "buffer_load_dwordx4 v16, s[80:83], s93 offen offset:1024 lds\n" \
  "s_mov_b32 s92, 2684928\n" \
  "buffer_load_dwordx4 v16, s[80:83], s92 offen offset:2048 lds\n" \
  "s_mov_b32 s93, 2700288\n" \
  "buffer_load_dwordx4 v16, s[80:83], s93 offen offset:3072 lds\n" \
  "v_add_u32 v24, 32768, v18\n" \
  "v_xor_b32 v25, 32, v24\n" \
  "ds_read_b64_tr_b16 a[32:33], v24 offset:0\n" \
  "ds_read_b64_tr_b16 a[34:35], v24 offset:1024\n" \
  "ds_read_b64_tr_b16 a[36:37], v24 offset:4096\n" \
  "ds_read_b64_tr_b16 a[38:39], v24 offset:5120\n" \
  "ds_read_b64_tr_b16 a[40:41], v25 offset:0\n" \
  "ds_read_b64_tr_b16 a[42:43], v25 offset:1024\n" \
  "ds_read_b64_tr_b16 a[44:45], v25 offset:4096\n" \
  "ds_read_b64_tr_b16 a[46:47], v25 offset:5120\n" \
  "s_waitcnt vmcnt(24) lgkmcnt(0)\n" \
  "s_barrier\n" \
  "s_add_u32 m0, s94, 32768\n" \
  "s_mov_b32 s92, 3145728\n" \
  "buffer_load_dwordx4 v16, s[80:83], s92 offen offset:0 lds\n" \
  "s_mov_b32 s93, 3161088\n" \
  "buffer_load_dwordx4 v16, s[80:83], s93 offen offset:1024 lds\n" \
  "s_mov_b32 s92, 3209216\n" \
  "buffer_load_dwordx4 v16, s[80:83], s92 offen offset:2048 lds\n" \
  "s_mov_b32 s93, 3224576\n" \
  "buffer_load_dwordx4 v16, s[80:83], s93 offen offset:3072 lds\n" \
  "v_add_u32 v24, 49152, v18\n" \
  "v_xor_b32 v25, 32, v24\n" \
  "ds_read_b64_tr_b16 a[48:49], v24 offset:0\n" \
  "ds_read_b64_tr_b16 a[50:51], v24 offset:1024\n" \
  "ds_read_b64_tr_b16 a[52:53], v24 offset:4096\n" \
  "ds_read_b64_tr_b16 a[54:55], v24 offset:5120\n" \
  "ds_read_b64_tr_b16 a[56:57], v25 offset:0\n" \
  "ds_read_b64_tr_b16 a[58:59], v25 offset:1024\n" \
  "ds_read_b64_tr_b16 a[60:61], v25 offset:4096\n" \
  "ds_read_b64_tr_b16 a[62:63], v25 offset:5120\n" \
  "s_waitcnt vmcnt(24) lgkmcnt(0)\n" \
  "s_barrier\n" \
  "s_add_u32 m0, s94, 49152\n" \
  "s_mov_b32 s92, 3670016\n" \
  "buffer_load_dwordx4 v16, s[80:83], s92 offen offset:0 lds\n" \
  "s_mov_b32 s93, 3685376\n" \
  "buffer_load_dwordx4 v16, s[80:83], s93 offen offset:1024 lds\n" \
  "s_mov_b32 s92, 3733504\n" \
  "buffer_load_dwordx4 v16, s[80:83], s92 offen offset:2048 lds\n" \
  "s_mov_b32 s93, 3748864\n" \
  "buffer_load_dwordx4 v16, s[80:83], s93 offen offset:3072 lds\n" \
  "v_add_u32 v24, 65536, v18\n" \
  "v_xor_b32 v25, 32, v24\n" \
  "ds_read_b64_tr_b16 v[128:129], v24 offset:0\n" \
  "ds_read_b64_tr_b16 v[130:131], v24 offset:1024\n" \
  "ds_read_b64_tr_b16 v[132:133], v24 offset:4096\n" \
  "ds_read_b64_tr_b16 v[134:135], v24 offset:5120\n" \
  "ds_read_b64_tr_b16 v[136:137], v25 offset:0\n" \
  "ds_read_b64_tr_b16 v[138:139], v25 offset:1024\n" \
  "ds_read_b64_tr_b16 v[140:141], v25 offset:4096\n" \
  "ds_read_b64_tr_b16 v[142:143], v25 offset:5120\n" \
  "s_waitcnt vmcnt(24) lgkmcnt(0)\n" \
  "s_barrier\n" \
  "s_mov_b64 s[80:81], s[44:45]\n" \
  "s_mov_b32 s82, 4194304\n" \
  "s_add_u32 m0, s94, 65536\n" \
  "s_mov_b32 s92, 0\n" \
  "buffer_load_dwordx4 v16, s[80:83], s92 offen offset:0 lds\n" \
  "s_mov_b32 s93, 15360\n" \
  "buffer_load_dwordx4 v16, s[80:83], s93 offen offset:1024 lds\n" \
  "s_mov_b32 s92, 63488\n" \
  "buffer_load_dwordx4 v16, s[80:83], s92 offen offset:2048 lds\n" \
  "s_mov_b32 s93, 78848\n" \
  "buffer_load_dwordx4 v16, s[80:83], s93 offen offset:3072 lds\n" \
  "v_add_u32 v24, 81920, v18\n" \
  "v_xor_b32 v25, 32, v24\n" \
  "ds_read_b64_tr_b16 v[144:145], v24 offset:0\n" \
  "ds_read_b64_tr_b16 v[146:147], v24 offset:1024\n" \
  "ds_read_b64_tr_b16 v[148:149], v24 offset:4096\n" \
  "ds_read_b64_tr_b16 v[150:151], v24 offset:5120\n" \
  "ds_read_b64_tr_b16 v[152:153], v25 offset:0\n" \
  "ds_read_b64_tr_b16 v[154:155], v25 offset:1024\n" \
  "ds_read_b64_tr_b16 v[156:157], v25 offset:4096\n" \
  "ds_read_b64_tr_b16 v[158:159], v25 offset:5120\n" \
  "s_waitcnt vmcnt(24) lgkmcnt(0)\n" \
  "s_barrier\n" \
  "s_add_u32 m0, s94, 81920\n" \
  "s_mov_b32 s92, 524288\n" \
  "buffer_load_dwordx4 v16, s[80:83], s92 offen offset:0 lds\n" \
  "s_mov_b32 s93, 539648\n" \
  "buffer_load_dwordx4 v16, s[80:83], s93 offen offset:1024 lds\n" \
  "s_mov_b32 s92, 587776\n" \
  "buffer_load_dwordx4 v16, s[80:83], s92 offen offset:2048 lds\n" \
  "s_mov_b32 s93, 603136\n" \
  "buffer_load_dwordx4 v16, s[80:83], s93 offen offset:3072 lds\n" \
  "v_add_u32 v24, 98304, v18\n" \
  "v_xor_b32 v25, 32, v24\n" \
  "ds_read_b64_tr_b16 v[160:161], v24 offset:0\n" \
  "ds_read_b64_tr_b16 v[162:163], v24 offset:1024\n" \
  "ds_read_b64_tr_b16 v[164:165], v24 offset:4096\n" \
  "ds_read_b64_tr_b16 v[166:167], v24 offset:5120\n" \
  "ds_read_b64_tr_b16 v[168:169], v25 offset:0\n" \
  "ds_read_b64_tr_b16 v[170:171], v25 offset:1024\n" \
  "ds_read_b64_tr_b16 v[172:173], v25 offset:4096\n" \
  "ds_read_b64_tr_b16 v[174:175], v25 offset:5120\n" \
  "s_waitcnt vmcnt(24) lgkmcnt(0)\n" \
  "s_barrier\n" \
  "s_add_u32 m0, s94, 98304\n" \
  "s_mov_b32 s92, 1048576\n" \
  "buffer_load_dwordx4 v16, s[80:83], s92 offen offset:0 lds\n" \
  "s_mov_b32 s93, 1063936\n" \
  "buffer_load_dwordx4 v16, s[80:83], s93 offen offset:1024 lds\n" \
  "s_mov_b32 s92, 1112064\n" \
  "buffer_load_dwordx4 v16, s[80:83], s92 offen offset:2048 lds\n" \
  "s_mov_b32 s93, 1127424\n" \
  "buffer_load_dwordx4 v16, s[80:83], s93 offen offset:3072 lds\n" \
  "v_add_u32 v24, 114688, v18\n" \
  "v_xor_b32 v25, 32, v24\n" \
  "ds_read_b64_tr_b16 v[176:177], v24 offset:0\n" \
  "ds_read_b64_tr_b16 v[178:179], v24 offset:1024\n" \
  "ds_read_b64_tr_b16 v[180:181], v24 offset:4096\n" \
  "ds_read_b64_tr_b16 v[182:183], v24 offset:5120\n" \
  "ds_read_b64_tr_b16 v[184:185], v25 offset:0\n" \
  "ds_read_b64_tr_b16 v[186:187], v25 offset:1024\n" \
  "ds_read_b64_tr_b16 v[188:189], v25 offset:4096\n" \
  "ds_read_b64_tr_b16 v[190:191], v25 offset:5120\n" \
  "s_waitcnt vmcnt(24) lgkmcnt(0)\n" \
  "s_barrier\n" \
  "s_add_u32 m0, s94, 114688\n" \
  "s_mov_b32 s92, 1572864\n" \
  "buffer_load_dwordx4 v16, s[80:83], s92 offen offset:0 lds\n" \
  "s_mov_b32 s93, 1588224\n" \
  "buffer_load_dwordx4 v16, s[80:83], s93 offen offset:1024 lds\n" \
  "s_mov_b32 s92, 1636352\n" \
  "buffer_load_dwordx4 v16, s[80:83], s92 offen offset:2048 lds\n" \
  "s_mov_b32 s93, 1651712\n" \
  "buffer_load_dwordx4 v16, s[80:83], s93 offen offset:3072 lds\n" \
  "v_add_u32 v24, 0, v18\n" \
  "v_xor_b32 v25, 32, v24\n" \
  "ds_read_b64_tr_b16 a[64:65], v24 offset:0\n" \
  "ds_read_b64_tr_b16 a[66:67], v24 offset:1024\n" \
  "ds_read_b64_tr_b16 a[68:69], v24 offset:4096\n" \
  "ds_read_b64_tr_b16 a[70:71], v24 offset:5120\n" \
  "ds_read_b64_tr_b16 a[72:73], v25 offset:0\n" \
  "ds_read_b64_tr_b16 a[74:75], v25 offset:1024\n" \
  "ds_read_b64_tr_b16 a[76:77], v25 offset:4096\n" \
  "ds_read_b64_tr_b16 a[78:79], v25 offset:5120\n" \
  "s_waitcnt vmcnt(24) lgkmcnt(0)\n" \
  "s_barrier\n" \
  "s_add_u32 m0, s94, 0\n" \
  "s_mov_b32 s92, 2097152\n" \
  "buffer_load_dwordx4 v16, s[80:83], s92 offen offset:0 lds\n" \
  "s_mov_b32 s93, 2112512\n" \
  "buffer_load_dwordx4 v16, s[80:83], s93 offen offset:1024 lds\n" \
  "s_mov_b32 s92, 2160640\n" \
  "buffer_load_dwordx4 v16, s[80:83], s92 offen offset:2048 lds\n" \
  "s_mov_b32 s93, 2176000\n" \
  "buffer_load_dwordx4 v16, s[80:83], s93 offen offset:3072 lds\n" \
  "v_add_u32 v24, 16384, v18\n" \
  "v_xor_b32 v25, 32, v24\n" \
  "ds_read_b64_tr_b16 a[80:81], v24 offset:0\n" \
  "ds_read_b64_tr_b16 a[82:83], v24 offset:1024\n" \
  "ds_read_b64_tr_b16 a[84:85], v24 offset:4096\n" \
  "ds_read_b64_tr_b16 a[86:87], v24 offset:5120\n" \
  "ds_read_b64_tr_b16 a[88:89], v25 offset:0\n" \
  "ds_read_b64_tr_b16 a[90:91], v25 offset:1024\n" \
  "ds_read_b64_tr_b16 a[92:93], v25 offset:4096\n" \
  "ds_read_b64_tr_b16 a[94:95], v25 offset:5120\n" \
  "s_waitcnt vmcnt(24) lgkmcnt(0)\n" \
  "s_barrier\n" \
  "s_add_u32 m0, s94, 16384\n" \
  "s_mov_b32 s92, 2621440\n" \
  "buffer_load_dwordx4 v16, s[80:83], s92 offen offset:0 lds\n" \
  "s_mov_b32 s93, 2636800\n" \
  "buffer_load_dwordx4 v16, s[80:83], s93 offen offset:1024 lds\n" \
  "s_mov_b32 s92, 2684928\n" \
  "buffer_load_dwordx4 v16, s[80:83], s92 offen offset:2048 lds\n" \
  "s_mov_b32 s93, 2700288\n" \
  "buffer_load_dwordx4 v16, s[80:83], s93 offen offset:3072 lds\n" \
  "v_add_u32 v24, 32768, v18\n" \
  "v_xor_b32 v25, 32, v24\n" \
  "ds_read_b64_tr_b16 a[96:97], v24 offset:0\n" \
  "ds_read_b64_tr_b16 a[98:99], v24 offset:1024\n" \
  "ds_read_b64_tr_b16 a[100:101], v24 offset:4096\n" \
  "ds_read_b64_tr_b16 a[102:103], v24 offset:5120\n" \
  "ds_read_b64_tr_b16 a[104:105], v25 offset:0\n" \
  "ds_read_b64_tr_b16 a[106:107], v25 offset:1024\n" \
  "ds_read_b64_tr_b16 a[108:109], v25 offset:4096\n" \
  "ds_read_b64_tr_b16 a[110:111], v25 offset:5120\n" \
  "s_waitcnt vmcnt(24) lgkmcnt(0)\n" \
  "s_barrier\n" \
  "s_add_u32 m0, s94, 32768\n" \
  "s_mov_b32 s92, 3145728\n" \
  "buffer_load_dwordx4 v16, s[80:83], s92 offen offset:0 lds\n" \
  "s_mov_b32 s93, 3161088\n" \
  "buffer_load_dwordx4 v16, s[80:83], s93 offen offset:1024 lds\n" \
  "s_mov_b32 s92, 3209216\n" \
  "buffer_load_dwordx4 v16, s[80:83], s92 offen offset:2048 lds\n" \
  "s_mov_b32 s93, 3224576\n" \
  "buffer_load_dwordx4 v16, s[80:83], s93 offen offset:3072 lds\n" \
  "v_add_u32 v24, 49152, v18\n" \
  "v_xor_b32 v25, 32, v24\n" \
  "ds_read_b64_tr_b16 a[112:113], v24 offset:0\n" \
  "ds_read_b64_tr_b16 a[114:115], v24 offset:1024\n" \
  "ds_read_b64_tr_b16 a[116:117], v24 offset:4096\n" \
  "ds_read_b64_tr_b16 a[118:119], v24 offset:5120\n" \
  "ds_read_b64_tr_b16 a[120:121], v25 offset:0\n" \
  "ds_read_b64_tr_b16 a[122:123], v25 offset:1024\n" \
  "ds_read_b64_tr_b16 a[124:125], v25 offset:4096\n" \
  "ds_read_b64_tr_b16 a[126:127], v25 offset:5120\n" \
  "s_waitcnt vmcnt(24) lgkmcnt(0)\n" \
  "s_barrier\n" \
  "s_add_u32 m0, s94, 49152\n" \
  "s_mov_b32 s92, 3670016\n" \
  "buffer_load_dwordx4 v16, s[80:83], s92 offen offset:0 lds\n" \
  "s_mov_b32 s93, 3685376\n" \
  "buffer_load_dwordx4 v16, s[80:83], s93 offen offset:1024 lds\n" \
  "s_mov_b32 s92, 3733504\n" \
  "buffer_load_dwordx4 v16, s[80:83], s92 offen offset:2048 lds\n" \
  "s_mov_b32 s93, 3748864\n" \
  "buffer_load_dwordx4 v16, s[80:83], s93 offen offset:3072 lds\n" \
  "v_add_u32 v24, 65536, v18\n" \
  "v_xor_b32 v25, 32, v24\n" \
  "ds_read_b64_tr_b16 a[128:129], v24 offset:0\n" \
  "ds_read_b64_tr_b16 a[130:131], v24 offset:1024\n" \
  "ds_read_b64_tr_b16 a[132:133], v24 offset:4096\n" \
  "ds_read_b64_tr_b16 a[134:135], v24 offset:5120\n" \
  "ds_read_b64_tr_b16 a[136:137], v25 offset:0\n" \
  "ds_read_b64_tr_b16 a[138:139], v25 offset:1024\n" \
  "ds_read_b64_tr_b16 a[140:141], v25 offset:4096\n" \
  "ds_read_b64_tr_b16 a[142:143], v25 offset:5120\n" \
  "s_waitcnt vmcnt(24) lgkmcnt(0)\n" \
  "s_barrier\n" \
  "s_mov_b64 s[80:81], s[46:47]\n" \
  "s_mov_b32 s82, 4194304\n" \
  "s_add_u32 m0, s94, 65536\n" \
  "s_xor_b32 s95, s50, 0\n" \
  "s_lshl_b32 s95, s95, 15\n" \
  "s_add_u32 s92, s95, 0\n" \
  "buffer_load_dwordx4 v17, s[80:83], s92 offen offset:0 lds\n" \
  "s_add_u32 s93, s95, 0\n" \
  "buffer_load_dwordx4 v17, s[80:83], s93 offen offset:1024 lds\n" \
  "s_add_u32 s92, s95, 6144\n" \
  "buffer_load_dwordx4 v17, s[80:83], s92 offen offset:2048 lds\n" \
  "s_add_u32 s93, s95, 6144\n" \
  "buffer_load_dwordx4 v17, s[80:83], s93 offen offset:3072 lds\n" \
  "v_add_u32 v24, 81920, v18\n" \
  "v_xor_b32 v25, 32, v24\n" \
  "ds_read_b64_tr_b16 a[144:145], v24 offset:0\n" \
  "ds_read_b64_tr_b16 a[146:147], v24 offset:1024\n" \
  "ds_read_b64_tr_b16 a[148:149], v24 offset:4096\n" \
  "ds_read_b64_tr_b16 a[150:151], v24 offset:5120\n" \
  "ds_read_b64_tr_b16 a[152:153], v25 offset:0\n" \
  "ds_read_b64_tr_b16 a[154:155], v25 offset:1024\n" \
  "ds_read_b64_tr_b16 a[156:157], v25 offset:4096\n" \
  "ds_read_b64_tr_b16 a[158:159], v25 offset:5120\n" \
  "s_waitcnt vmcnt(24) lgkmcnt(0)\n" \
  "s_barrier\n" \
  "s_add_u32 m0, s94, 81920\n" \
  "s_xor_b32 s95, s50, 1\n" \
  "s_lshl_b32 s95, s95, 15\n" \
  "s_add_u32 s92, s95, 0\n" \
  "buffer_load_dwordx4 v17, s[80:83], s92 offen offset:0 lds\n" \
  "s_add_u32 s93, s95, 0\n" \
  "buffer_load_dwordx4 v17, s[80:83], s93 offen offset:1024 lds\n" \
  "s_add_u32 s92, s95, 6144\n" \
  "buffer_load_dwordx4 v17, s[80:83], s92 offen offset:2048 lds\n" \
  "s_add_u32 s93, s95, 6144\n" \
  "buffer_load_dwordx4 v17, s[80:83], s93 offen offset:3072 lds\n" \
  "v_add_u32 v24, 98304, v18\n" \
  "v_xor_b32 v25, 32, v24\n" \
  "ds_read_b64_tr_b16 a[160:161], v24 offset:0\n" \
  "ds_read_b64_tr_b16 a[162:163], v24 offset:1024\n" \
  "ds_read_b64_tr_b16 a[164:165], v24 offset:4096\n" \
  "ds_read_b64_tr_b16 a[166:167], v24 offset:5120\n" \
  "ds_read_b64_tr_b16 a[168:169], v25 offset:0\n" \
  "ds_read_b64_tr_b16 a[170:171], v25 offset:1024\n" \
  "ds_read_b64_tr_b16 a[172:173], v25 offset:4096\n" \
  "ds_read_b64_tr_b16 a[174:175], v25 offset:5120\n" \
  "s_waitcnt vmcnt(24) lgkmcnt(0)\n" \
  "s_barrier\n" \
  "s_add_u32 m0, s94, 98304\n" \
  "s_xor_b32 s95, s50, 2\n" \
  "s_lshl_b32 s95, s95, 15\n" \
  "s_add_u32 s92, s95, 0\n" \
  "buffer_load_dwordx4 v17, s[80:83], s92 offen offset:0 lds\n" \
  "s_add_u32 s93, s95, 0\n" \
  "buffer_load_dwordx4 v17, s[80:83], s93 offen offset:1024 lds\n" \
  "s_add_u32 s92, s95, 6144\n" \
  "buffer_load_dwordx4 v17, s[80:83], s92 offen offset:2048 lds\n" \
  "s_add_u32 s93, s95, 6144\n" \
  "buffer_load_dwordx4 v17, s[80:83], s93 offen offset:3072 lds\n" \
  "v_add_u32 v24, 114688, v18\n" \
  "v_xor_b32 v25, 32, v24\n" \
  "ds_read_b64_tr_b16 a[176:177], v24 offset:0\n" \
  "ds_read_b64_tr_b16 a[178:179], v24 offset:1024\n" \
  "ds_read_b64_tr_b16 a[180:181], v24 offset:4096\n" \
  "ds_read_b64_tr_b16 a[182:183], v24 offset:5120\n" \
  "ds_read_b64_tr_b16 a[184:185], v25 offset:0\n" \
  "ds_read_b64_tr_b16 a[186:187], v25 offset:1024\n" \
  "ds_read_b64_tr_b16 a[188:189], v25 offset:4096\n" \
  "ds_read_b64_tr_b16 a[190:191], v25 offset:5120\n" \
  "s_waitcnt vmcnt(24) lgkmcnt(0)\n" \
  "s_barrier\n" \
  "s_add_u32 m0, s94, 114688\n" \
  "s_xor_b32 s95, s50, 3\n" \
  "s_lshl_b32 s95, s95, 15\n" \
  "s_add_u32 s92, s95, 0\n" \
  "buffer_load_dwordx4 v17, s[80:83], s92 offen offset:0 lds\n" \
  "s_add_u32 s93, s95, 0\n" \
  "buffer_load_dwordx4 v17, s[80:83], s93 offen offset:1024 lds\n" \
  "s_add_u32 s92, s95, 6144\n" \
  "buffer_load_dwordx4 v17, s[80:83], s92 offen offset:2048 lds\n" \
  "s_add_u32 s93, s95, 6144\n" \
  "buffer_load_dwordx4 v17, s[80:83], s93 offen offset:3072 lds\n" \
  "v_add_u32 v24, 0, v18\n" \
  "v_xor_b32 v25, 32, v24\n" \
  "ds_read_b64_tr_b16 a[192:193], v24 offset:0\n" \
  "ds_read_b64_tr_b16 a[194:195], v24 offset:1024\n" \
  "ds_read_b64_tr_b16 a[196:197], v24 offset:4096\n" \
  "ds_read_b64_tr_b16 a[198:199], v24 offset:5120\n" \
  "ds_read_b64_tr_b16 a[200:201], v25 offset:0\n" \
  "ds_read_b64_tr_b16 a[202:203], v25 offset:1024\n" \
  "ds_read_b64_tr_b16 a[204:205], v25 offset:4096\n" \
  "ds_read_b64_tr_b16 a[206:207], v25 offset:5120\n" \
  "s_waitcnt vmcnt(24) lgkmcnt(0)\n" \
  "s_barrier\n" \
  "s_mov_b64 s[80:81], s[40:41]\n" \
  "s_mov_b32 s82, 4194304\n" \
  "s_add_u32 m0, s94, 0\n" \
  "s_mov_b32 s92, 0\n" \
  "buffer_load_dwordx4 v16, s[80:83], s92 offen offset:0 lds\n" \
  "s_mov_b32 s93, 15360\n" \
  "buffer_load_dwordx4 v16, s[80:83], s93 offen offset:1024 lds\n" \
  "s_mov_b32 s92, 63488\n" \
  "buffer_load_dwordx4 v16, s[80:83], s92 offen offset:2048 lds\n" \
  "s_mov_b32 s93, 78848\n" \
  "buffer_load_dwordx4 v16, s[80:83], s93 offen offset:3072 lds\n" \
  "v_add_u32 v24, 16384, v18\n" \
  "v_xor_b32 v25, 32, v24\n" \
  "ds_read_b64_tr_b16 a[208:209], v24 offset:0\n" \
  "ds_read_b64_tr_b16 a[210:211], v24 offset:1024\n" \
  "ds_read_b64_tr_b16 a[212:213], v24 offset:4096\n" \
  "ds_read_b64_tr_b16 a[214:215], v24 offset:5120\n" \
  "ds_read_b64_tr_b16 a[216:217], v25 offset:0\n" \
  "ds_read_b64_tr_b16 a[218:219], v25 offset:1024\n" \
  "ds_read_b64_tr_b16 a[220:221], v25 offset:4096\n" \
  "ds_read_b64_tr_b16 a[222:223], v25 offset:5120\n" \
  "s_waitcnt vmcnt(24) lgkmcnt(0)\n" \
  "s_barrier\n" \
  "s_add_u32 m0, s94, 16384\n" \
  "s_mov_b32 s92, 524288\n" \
  "buffer_load_dwordx4 v16, s[80:83], s92 offen offset:0 lds\n" \
  "s_mov_b32 s93, 539648\n" \
  "buffer_load_dwordx4 v16, s[80:83], s93 offen offset:1024 lds\n" \
  "s_mov_b32 s92, 587776\n" \
  "buffer_load_dwordx4 v16, s[80:83], s92 offen offset:2048 lds\n" \
  "s_mov_b32 s93, 603136\n" \
  "buffer_load_dwordx4 v16, s[80:83], s93 offen offset:3072 lds\n" \
  "v_add_u32 v24, 32768, v18\n" \
  "v_xor_b32 v25, 32, v24\n" \
  "ds_read_b64_tr_b16 a[224:225], v24 offset:0\n" \
  "ds_read_b64_tr_b16 a[226:227], v24 offset:1024\n" \
  "ds_read_b64_tr_b16 a[228:229], v24 offset:4096\n" \
  "ds_read_b64_tr_b16 a[230:231], v24 offset:5120\n" \
  "ds_read_b64_tr_b16 a[232:233], v25 offset:0\n" \
  "ds_read_b64_tr_b16 a[234:235], v25 offset:1024\n" \
  "ds_read_b64_tr_b16 a[236:237], v25 offset:4096\n" \
  "ds_read_b64_tr_b16 a[238:239], v25 offset:5120\n" \
  "s_waitcnt vmcnt(24) lgkmcnt(0)\n" \
  "s_barrier\n" \
  "s_add_u32 m0, s94, 32768\n" \
  "s_mov_b32 s92, 1048576\n" \
  "buffer_load_dwordx4 v16, s[80:83], s92 offen offset:0 lds\n" \
  "s_mov_b32 s93, 1063936\n" \
  "buffer_load_dwordx4 v16, s[80:83], s93 offen offset:1024 lds\n" \
  "s_mov_b32 s92, 1112064\n" \
  "buffer_load_dwordx4 v16, s[80:83], s92 offen offset:2048 lds\n" \
  "s_mov_b32 s93, 1127424\n" \
  "buffer_load_dwordx4 v16, s[80:83], s93 offen offset:3072 lds\n" \
  "v_add_u32 v24, 49152, v18\n" \
  "v_xor_b32 v25, 32, v24\n" \
  "ds_read_b64_tr_b16 a[240:241], v24 offset:0\n" \
  "ds_read_b64_tr_b16 a[242:243], v24 offset:1024\n" \
  "ds_read_b64_tr_b16 a[244:245], v24 offset:4096\n" \
  "ds_read_b64_tr_b16 a[246:247], v24 offset:5120\n" \
  "ds_read_b64_tr_b16 a[248:249], v25 offset:0\n" \
  "ds_read_b64_tr_b16 a[250:251], v25 offset:1024\n" \
  "ds_read_b64_tr_b16 a[252:253], v25 offset:4096\n" \
  "ds_read_b64_tr_b16 a[254:255], v25 offset:5120\n" \
  "s_waitcnt vmcnt(24) lgkmcnt(0)\n" \
  "s_barrier\n" \
  "s_add_u32 m0, s94, 49152\n" \
  "s_mov_b32 s92, 1572864\n" \
  "buffer_load_dwordx4 v16, s[80:83], s92 offen offset:0 lds\n" \
  "s_mov_b32 s93, 1588224\n" \
  "buffer_load_dwordx4 v16, s[80:83], s93 offen offset:1024 lds\n" \
  "s_mov_b32 s92, 1636352\n" \
  "buffer_load_dwordx4 v16, s[80:83], s92 offen offset:2048 lds\n" \
  "s_mov_b32 s93, 1651712\n" \
  "buffer_load_dwordx4 v16, s[80:83], s93 offen offset:3072 lds\n" \
  "v_add_u32 v24, 65536, v19\n" \
  "v_xor_b32 v25, 64, v24\n" \
  "ds_read_b64_tr_b16 v[192:193], v24 offset:0\n" \
  "ds_read_b64_tr_b16 v[194:195], v24 offset:1024\n" \
  "ds_read_b64_tr_b16 v[196:197], v24 offset:2048\n" \
  "ds_read_b64_tr_b16 v[198:199], v24 offset:3072\n" \
  "ds_read_b64_tr_b16 v[224:225], v25 offset:0\n" \
  "ds_read_b64_tr_b16 v[226:227], v25 offset:1024\n" \
  "ds_read_b64_tr_b16 v[228:229], v25 offset:2048\n" \
  "ds_read_b64_tr_b16 v[230:231], v25 offset:3072\n" \
  "s_waitcnt vmcnt(24) lgkmcnt(0)\n" \
  "s_barrier\n" \
  "s_mov_b64 s[80:81], s[46:47]\n" \
  "s_mov_b32 s82, 4194304\n" \
  "s_add_u32 m0, s94, 65536\n" \
  "s_xor_b32 s95, s50, 0\n" \
  "s_lshl_b32 s95, s95, 15\n" \
  "s_add_u32 s92, s95, 128\n" \
  "buffer_load_dwordx4 v17, s[80:83], s92 offen offset:0 lds\n" \
  "s_add_u32 s93, s95, 128\n" \
  "buffer_load_dwordx4 v17, s[80:83], s93 offen offset:1024 lds\n" \
  "s_add_u32 s92, s95, 6272\n" \
  "buffer_load_dwordx4 v17, s[80:83], s92 offen offset:2048 lds\n" \
  "s_add_u32 s93, s95, 6272\n" \
  "buffer_load_dwordx4 v17, s[80:83], s93 offen offset:3072 lds\n" \
  "v_add_u32 v24, 81920, v19\n" \
  "v_xor_b32 v25, 64, v24\n" \
  "ds_read_b64_tr_b16 v[200:201], v24 offset:0\n" \
  "ds_read_b64_tr_b16 v[202:203], v24 offset:1024\n" \
  "ds_read_b64_tr_b16 v[204:205], v24 offset:2048\n" \
  "ds_read_b64_tr_b16 v[206:207], v24 offset:3072\n" \
  "ds_read_b64_tr_b16 v[232:233], v25 offset:0\n" \
  "ds_read_b64_tr_b16 v[234:235], v25 offset:1024\n" \
  "ds_read_b64_tr_b16 v[236:237], v25 offset:2048\n" \
  "ds_read_b64_tr_b16 v[238:239], v25 offset:3072\n" \
  "s_waitcnt vmcnt(24) lgkmcnt(0)\n" \
  "s_barrier\n" \
  "s_add_u32 m0, s94, 81920\n" \
  "s_xor_b32 s95, s50, 1\n" \
  "s_lshl_b32 s95, s95, 15\n" \
  "s_add_u32 s92, s95, 128\n" \
  "buffer_load_dwordx4 v17, s[80:83], s92 offen offset:0 lds\n" \
  "s_add_u32 s93, s95, 128\n" \
  "buffer_load_dwordx4 v17, s[80:83], s93 offen offset:1024 lds\n" \
  "s_add_u32 s92, s95, 6272\n" \
  "buffer_load_dwordx4 v17, s[80:83], s92 offen offset:2048 lds\n" \
  "s_add_u32 s93, s95, 6272\n" \
  "buffer_load_dwordx4 v17, s[80:83], s93 offen offset:3072 lds\n" \
  "v_add_u32 v24, 98304, v19\n" \
  "v_xor_b32 v25, 64, v24\n" \
  "ds_read_b64_tr_b16 v[208:209], v24 offset:0\n" \
  "ds_read_b64_tr_b16 v[210:211], v24 offset:1024\n" \
  "ds_read_b64_tr_b16 v[212:213], v24 offset:2048\n" \
  "ds_read_b64_tr_b16 v[214:215], v24 offset:3072\n" \
  "ds_read_b64_tr_b16 v[240:241], v25 offset:0\n" \
  "ds_read_b64_tr_b16 v[242:243], v25 offset:1024\n" \
  "ds_read_b64_tr_b16 v[244:245], v25 offset:2048\n" \
  "ds_read_b64_tr_b16 v[246:247], v25 offset:3072\n" \
  "s_waitcnt vmcnt(24) lgkmcnt(0)\n" \
  "s_barrier\n" \
  "s_add_u32 m0, s94, 98304\n" \
  "s_xor_b32 s95, s50, 2\n" \
  "s_lshl_b32 s95, s95, 15\n" \
  "s_add_u32 s92, s95, 128\n" \
  "buffer_load_dwordx4 v17, s[80:83], s92 offen offset:0 lds\n" \
  "s_add_u32 s93, s95, 128\n" \
  "buffer_load_dwordx4 v17, s[80:83], s93 offen offset:1024 lds\n" \
  "s_add_u32 s92, s95, 6272\n" \
  "buffer_load_dwordx4 v17, s[80:83], s92 offen offset:2048 lds\n" \
  "s_add_u32 s93, s95, 6272\n" \
  "buffer_load_dwordx4 v17, s[80:83], s93 offen offset:3072 lds\n" \
  "v_add_u32 v24, 114688, v19\n" \
  "v_xor_b32 v25, 64, v24\n" \
  "ds_read_b64_tr_b16 v[216:217], v24 offset:0\n" \
  "ds_read_b64_tr_b16 v[218:219], v24 offset:1024\n" \
  "ds_read_b64_tr_b16 v[220:221], v24 offset:2048\n" \
  "ds_read_b64_tr_b16 v[222:223], v24 offset:3072\n" \
  "ds_read_b64_tr_b16 v[248:249], v25 offset:0\n" \
  "ds_read_b64_tr_b16 v[250:251], v25 offset:1024\n" \
  "ds_read_b64_tr_b16 v[252:253], v25 offset:2048\n" \
  "ds_read_b64_tr_b16 v[254:255], v25 offset:3072\n" \
  "s_waitcnt vmcnt(24) lgkmcnt(0)\n" \
  "s_barrier\n" \
  "s_add_u32 m0, s94, 114688\n" \
  "s_xor_b32 s95, s50, 3\n" \
  "s_lshl_b32 s95, s95, 15\n" \
  "s_add_u32 s92, s95, 128\n" \
  "buffer_load_dwordx4 v17, s[80:83], s92 offen offset:0 lds\n" \
  "s_add_u32 s93, s95, 128\n" \
  "buffer_load_dwordx4 v17, s[80:83], s93 offen offset:1024 lds\n" \
  "s_add_u32 s92, s95, 6272\n" \
  "buffer_load_dwordx4 v17, s[80:83], s92 offen offset:2048 lds\n" \
  "s_add_u32 s93, s95, 6272\n" \
  "buffer_load_dwordx4 v17, s[80:83], s93 offen offset:3072 lds\n" \
  "v_add_u32 v24, 0, v18\n" \
  "v_xor_b32 v25, 32, v24\n" \
  "ds_read_b64_tr_b16 v[32:33], v24 offset:0\n" \
  "ds_read_b64_tr_b16 v[34:35], v24 offset:1024\n" \
  "ds_read_b64_tr_b16 v[36:37], v24 offset:4096\n" \
  "ds_read_b64_tr_b16 v[38:39], v24 offset:5120\n" \
  "ds_read_b64_tr_b16 v[40:41], v25 offset:0\n" \
  "ds_read_b64_tr_b16 v[42:43], v25 offset:1024\n" \
  "ds_read_b64_tr_b16 v[44:45], v25 offset:4096\n" \
  "ds_read_b64_tr_b16 v[46:47], v25 offset:5120\n" \
  "s_waitcnt vmcnt(24) lgkmcnt(0)\n" \
  "s_barrier\n" \
  "v_add_u32 v24, 16384, v18\n" \
  "v_xor_b32 v25, 32, v24\n" \
  "ds_read_b64_tr_b16 v[48:49], v24 offset:0\n" \
  "ds_read_b64_tr_b16 v[50:51], v24 offset:1024\n" \
  "ds_read_b64_tr_b16 v[52:53], v24 offset:4096\n" \
  "ds_read_b64_tr_b16 v[54:55], v24 offset:5120\n" \
  "ds_read_b64_tr_b16 v[56:57], v25 offset:0\n" \
  "ds_read_b64_tr_b16 v[58:59], v25 offset:1024\n" \
  "ds_read_b64_tr_b16 v[60:61], v25 offset:4096\n" \
  "ds_read_b64_tr_b16 v[62:63], v25 offset:5120\n" \
  "ds_write_b128 v1, v[32:35] offset:0\n" \
  "ds_write_b128 v1, v[36:39] offset:1024\n" \
  "ds_write_b128 v1, v[40:43] offset:2048\n" \
  "ds_write_b128 v1, v[44:47] offset:3072\n" \
  "s_waitcnt vmcnt(20) lgkmcnt(0)\n" \
  "s_barrier\n" \
  "v_add_u32 v24, 32768, v18\n" \
  "v_xor_b32 v25, 32, v24\n" \
  "ds_read_b64_tr_b16 v[32:33], v24 offset:0\n" \
  "ds_read_b64_tr_b16 v[34:35], v24 offset:1024\n" \
  "ds_read_b64_tr_b16 v[36:37], v24 offset:4096\n" \
  "ds_read_b64_tr_b16 v[38:39], v24 offset:5120\n" \
  "ds_read_b64_tr_b16 v[40:41], v25 offset:0\n" \
  "ds_read_b64_tr_b16 v[42:43], v25 offset:1024\n" \
  "ds_read_b64_tr_b16 v[44:45], v25 offset:4096\n" \
  "ds_read_b64_tr_b16 v[46:47], v25 offset:5120\n" \
  "ds_write_b128 v1, v[48:51] offset:16384\n" \
  "ds_write_b128 v1, v[52:55] offset:17408\n" \
  "ds_write_b128 v1, v[56:59] offset:18432\n" \
  "ds_write_b128 v1, v[60:63] offset:19456\n" \
  "s_waitcnt vmcnt(16) lgkmcnt(0)\n" \
  "s_barrier\n" \
  "v_add_u32 v24, 49152, v18\n" \
  "v_xor_b32 v25, 32, v24\n" \
  "ds_read_b64_tr_b16 v[48:49], v24 offset:0\n" \
  "ds_read_b64_tr_b16 v[50:51], v24 offset:1024\n" \
  "ds_read_b64_tr_b16 v[52:53], v24 offset:4096\n" \
  "ds_read_b64_tr_b16 v[54:55], v24 offset:5120\n" \
  "ds_read_b64_tr_b16 v[56:57], v25 offset:0\n" \
  "ds_read_b64_tr_b16 v[58:59], v25 offset:1024\n" \
  "ds_read_b64_tr_b16 v[60:61], v25 offset:4096\n" \
  "ds_read_b64_tr_b16 v[62:63], v25 offset:5120\n" \
  "ds_write_b128 v1, v[32:35] offset:32768\n" \
  "ds_write_b128 v1, v[36:39] offset:33792\n" \
  "ds_write_b128 v1, v[40:43] offset:34816\n" \
  "ds_write_b128 v1, v[44:47] offset:35840\n" \
  "s_waitcnt vmcnt(12) lgkmcnt(0)\n" \
  "s_barrier\n" \
  "v_add_u32 v24, 65536, v19\n" \
  "v_xor_b32 v25, 64, v24\n" \
  "ds_read_b64_tr_b16 v[32:33], v24 offset:0\n" \
  "ds_read_b64_tr_b16 v[34:35], v24 offset:1024\n" \
  "ds_read_b64_tr_b16 v[36:37], v24 offset:2048\n" \
  "ds_read_b64_tr_b16 v[38:39], v24 offset:3072\n" \
  "ds_read_b64_tr_b16 v[40:41], v25 offset:0\n" \
  "ds_read_b64_tr_b16 v[42:43], v25 offset:1024\n" \
  "ds_read_b64_tr_b16 v[44:45], v25 offset:2048\n" \
  "ds_read_b64_tr_b16 v[46:47], v25 offset:3072\n" \
  "ds_write_b128 v1, v[48:51] offset:49152\n" \
  "ds_write_b128 v1, v[52:55] offset:50176\n" \
  "ds_write_b128 v1, v[56:59] offset:51200\n" \
  "ds_write_b128 v1, v[60:63] offset:52224\n" \
  "s_waitcnt vmcnt(8) lgkmcnt(0)\n" \
  "s_barrier\n" \
  "v_add_u32 v24, 81920, v19\n" \
  "v_xor_b32 v25, 64, v24\n" \
  "ds_read_b64_tr_b16 v[48:49], v24 offset:0\n" \
  "ds_read_b64_tr_b16 v[50:51], v24 offset:1024\n" \
  "ds_read_b64_tr_b16 v[52:53], v24 offset:2048\n" \
  "ds_read_b64_tr_b16 v[54:55], v24 offset:3072\n" \
  "ds_read_b64_tr_b16 v[56:57], v25 offset:0\n" \
  "ds_read_b64_tr_b16 v[58:59], v25 offset:1024\n" \
  "ds_read_b64_tr_b16 v[60:61], v25 offset:2048\n" \
  "ds_read_b64_tr_b16 v[62:63], v25 offset:3072\n" \
  "ds_write_b128 v2, v[32:35] offset:0\n" \
  "ds_write_b128 v2, v[36:39] offset:1024\n" \
  "ds_write_b128 v2, v[40:43] offset:2048\n" \
  "ds_write_b128 v2, v[44:47] offset:3072\n" \
  "s_waitcnt vmcnt(4) lgkmcnt(0)\n" \
  "s_barrier\n" \
  "v_add_u32 v24, 98304, v19\n" \
  "v_xor_b32 v25, 64, v24\n" \
  "ds_read_b64_tr_b16 v[32:33], v24 offset:0\n" \
  "ds_read_b64_tr_b16 v[34:35], v24 offset:1024\n" \
  "ds_read_b64_tr_b16 v[36:37], v24 offset:2048\n" \
  "ds_read_b64_tr_b16 v[38:39], v24 offset:3072\n" \
  "ds_read_b64_tr_b16 v[40:41], v25 offset:0\n" \
  "ds_read_b64_tr_b16 v[42:43], v25 offset:1024\n" \
  "ds_read_b64_tr_b16 v[44:45], v25 offset:2048\n" \
  "ds_read_b64_tr_b16 v[46:47], v25 offset:3072\n" \
  "ds_write_b128 v2, v[48:51] offset:16384\n" \
  "ds_write_b128 v2, v[52:55] offset:17408\n" \
  "ds_write_b128 v2, v[56:59] offset:18432\n" \
  "ds_write_b128 v2, v[60:63] offset:19456\n" \
  "s_waitcnt vmcnt(0) lgkmcnt(0)\n" \
  "s_barrier\n" \
  "v_add_u32 v24, 114688, v19\n" \
  "v_xor_b32 v25, 64, v24\n" \
  "ds_read_b64_tr_b16 v[48:49], v24 offset:0\n" \
  "ds_read_b64_tr_b16 v[50:51], v24 offset:1024\n" \
  "ds_read_b64_tr_b16 v[52:53], v24 offset:2048\n" \
  "ds_read_b64_tr_b16 v[54:55], v24 offset:3072\n" \
  "ds_read_b64_tr_b16 v[56:57], v25 offset:0\n" \
  "ds_read_b64_tr_b16 v[58:59], v25 offset:1024\n" \
  "ds_read_b64_tr_b16 v[60:61], v25 offset:2048\n" \
  "ds_read_b64_tr_b16 v[62:63], v25 offset:3072\n" \
  "ds_write_b128 v2, v[32:35] offset:32768\n" \
  "ds_write_b128 v2, v[36:39] offset:33792\n" \
  "ds_write_b128 v2, v[40:43] offset:34816\n" \
  "ds_write_b128 v2, v[44:47] offset:35840\n" \
  "s_waitcnt lgkmcnt(0)\n" \
  "s_barrier\n" \
  "ds_write_b128 v2, v[48:51] offset:49152\n" \
  "ds_write_b128 v2, v[52:55] offset:50176\n" \
  "ds_write_b128 v2, v[56:59] offset:51200\n" \
  "ds_write_b128 v2, v[60:63] offset:52224\n" \
  ".Lch_par:\n" \
  "buffer_load_dwordx2 v[24:25], off, s[36:39], s61 sc1\n" \
  "buffer_load_dwordx2 v[26:27], off, s[36:39], s61 offset:16 sc1\n" \
  "s_waitcnt vmcnt(1)\n" \
  "v_readfirstlane_b32 s94, v24\n" \
  "v_readfirstlane_b32 s95, v25\n" \
  "s_waitcnt vmcnt(0)\n" \
  "v_readfirstlane_b32 s92, v26\n" \
  "v_readfirstlane_b32 s93, v27\n" \
  "s_add_u32 s94, s94, 16\n" \
  "s_xor_b32 s94, s94, s92\n" \
  "s_xnor_b32 s95, s95, s93\n" \
  "s_lshr_b32 s94, s94, 4\n" \
  "s_or_b32 s94, s94, s95\n" \
  "s_cbranch_scc0 .Lch_par_ok\n" \
  "s_sleep 4\n" \
  "s_sub_u32 s89, s89, 1\n" \
  "s_cbranch_scc0 .Lch_par\n" \
  "s_mov_b32 s89, 0\n" \
  ".Lch_par_ok:\n" \
  "s_and_b32 s92, s92, 14\n" \
  "s_lshl_b32 s92, s92, 11\n" \
  "s_and_b32 s93, s92, 0x2000\n" \
  "s_xor_b32 s56, s56, s93\n" \
  "s_xor_b32 s57, s57, s93\n" \
  "s_xor_b32 s58, s58, s93\n" \
  "s_xor_b32 s59, s59, s93\n" \
  "s_xor_b32 s60, s60, s93\n" \
  "v_readfirstlane_b32 s80, v21\n" \
  "v_readfirstlane_b32 s81, v22\n" \
  "v_readfirstlane_b32 s86, v23\n" \
  "s_xor_b32 s80, s80, s93\n" \
  "s_xor_b32 s81, s81, s93\n" \
  "s_xor_b32 s23, s52, s92\n" \
  "s_xor_b32 s24, s53, s92\n" \
  "s_and_b32 s22, s51, 0x8f00\n" \
  "s_mov_b32 s85, 0\n" \
  "s_xor_b32 s87, s51, s92\n" \
  "s_add_u32 s88, s87, 0x10000\n" \
  "s_cmp_eq_u32 s50, 0\n" \
  "s_cselect_b32 s93, s92, 0\n" \
  "v_xor_b32 v12, s93, v12\n" \
  "s_lshr_b32 s47, s55, 8\n" \
  "s_and_b32 s55, s55, 0xff\n" \
  "s_mov_b32 s49, 1648\n" \
  "s_lshr_b32 s48, s63, 16\n" \
  "s_bfe_u32 s29, s63, 0x80008\n" \
  "s_and_b32 s63, s63, 0xff\n" \
  "s_mov_b32 s28, 0x40000000\n" \
  "s_mov_b32 s33, s28\n" \
  "s_xor_b32 s26, s23, 540672\n" \
  "s_lshl_b32 s93, s50, 15\n" \
  "s_add_u32 s26, s26, s93\n" \
  "s_cmp_gt_u32 s50, 1\n" \
  "s_cselect_b32 s26, s28, s26\n" \
  "v_mov_b64 v[64:65], -1\n" \
  "v_mov_b64 v[66:67], -1\n" \
  "v_mov_b64 v[68:69], -1\n" \
  "v_mov_b64 v[70:71], -1\n" \
  "v_mov_b64 v[72:73], -1\n" \
  "v_mov_b64 v[74:75], -1\n" \
  "v_mov_b64 v[76:77], -1\n" \
  "v_mov_b64 v[78:79], -1\n" \
  "s_waitcnt lgkmcnt(0)\n" \
  "ds_read_b128 v[16:19], v1 offset:0\n" \
  "ds_read_b128 v[20:23], v1 offset:1024\n" \
  "ds_read_b128 v[24:27], v1 offset:2048\n" \
  "ds_read_b128 v[28:31], v1 offset:3072\n" \
  "ds_read_b128 v[32:35], v1 offset:16384\n" \
  "ds_read_b128 v[36:39], v1 offset:17408\n" \
  "ds_read_b128 v[40:43], v1 offset:18432\n" \
  "ds_read_b128 v[44:47], v1 offset:19456\n" \
  "ds_read_b128 v[48:51], v1 offset:32768\n" \
  "ds_read_b128 v[52:55], v1 offset:33792\n" \
  "ds_read_b128 v[56:59], v1 offset:34816\n" \
  "ds_read_b128 v[60:63], v1 offset:35840\n" \
  "v_mov_b64 v[112:113], 0\n" \
  "v_mov_b64 v[114:115], 0\n" \
  "v_mov_b64 v[116:117], 0\n" \
  "v_mov_b64 v[118:119], 0\n" \
  "v_mov_b64 v[120:121], 0\n" \
  "v_mov_b64 v[122:123], 0\n" \
  "v_mov_b64 v[124:125], 0\n" \
  "v_mov_b64 v[126:127], 0\n" \
  "s_cmp_lg_u32 s50, 1\n" \
  "s_cbranch_scc1 .Lch_b0\n" \
  "s_mov_b32 s94, 0\n" \
  ".Lch_f0:\n" \
  "s_sleep 2\n" \
  "buffer_load_dword v13, v12, s[36:39], s58 offen sc1\n" \
  "s_waitcnt vmcnt(0)\n" \
  ".Lch_f0_chk:\n" \
  "v_cmp_gt_i32 vcc, s94, v13\n" \
  "s_and_b32 vcc_lo, vcc_lo, 7\n" \
  "s_cbranch_scc0 .Lch_f0_ok\n" \
  "s_sub_u32 s89, s89, 1\n" \
  "s_cbranch_scc0 .Lch_f0\n" \
  "s_mov_b32 s89, 0\n" \
  ".Lch_f0_ok:\n" \
  "s_mov_b32 m0, 0\n" \
  "s_nop 0\n" \
  "buffer_load_dwordx4 v0, s[36:39], s56 offen offset:0 sc1 lds\n" \
  "buffer_load_dwordx4 v0, s[36:39], s56 offen offset:1024 sc1 lds\n" \
  "buffer_load_dwordx4 v0, s[36:39], s56 offen offset:2048 sc1 lds\n" \
  "buffer_load_dwordx4 v0, s[36:39], s56 offen offset:3072 sc1 lds\n" \
  "s_mov_b32 m0, 4096\n" \
  "s_add_u32 s92, s56, 4096\n" \
  "buffer_load_dwordx4 v0, s[36:39], s92 offen offset:0 sc1 lds\n" \
  "buffer_load_dwordx4 v0, s[36:39], s92 offen offset:1024 sc1 lds\n" \
  "buffer_load_dwordx4 v0, s[36:39], s92 offen offset:2048 sc1 lds\n" \
  "buffer_load_dwordx4 v0, s[36:39], s92 offen offset:3072 sc1 lds\n" \
  "s_mov_b32 m0, 8192\n" \
  "s_add_u32 s92, s56, 524288\n" \
  "buffer_load_dwordx4 v0, s[36:39], s92 offen offset:0 sc1 lds\n" \
  "buffer_load_dwordx4 v0, s[36:39], s92 offen offset:1024 sc1 lds\n" \
  "buffer_load_dwordx4 v0, s[36:39], s92 offen offset:2048 sc1 lds\n" \
  "buffer_load_dwordx4 v0, s[36:39], s92 offen offset:3072 sc1 lds\n" \
  "s_mov_b32 m0, 12288\n" \
  "s_add_u32 s92, s56, 528384\n" \
  "buffer_load_dwordx4 v0, s[36:39], s92 offen offset:0 sc1 lds\n" \
  "buffer_load_dwordx4 v0, s[36:39], s92 offen offset:1024 sc1 lds\n" \
  "buffer_load_dwordx4 v0, s[36:39], s92 offen offset:2048 sc1 lds\n" \
  "buffer_load_dwordx4 v0, s[36:39], s92 offen offset:3072 sc1 lds\n" \
  "s_waitcnt vmcnt(0)\n" \
  "s_memrealtime s[82:83]\n" \
  "s_lshr_b32 s94, s49, 4\n" \
  ".Lch_b0:\n" \
  "s_barrier\n" \
  ".Lch_z:\n" \
  "ds_read_b128 v[80:83], v0 offset:16384\n" \
  "ds_read_b128 v[84:87], v0 offset:17408\n" \
  "ds_read_b128 v[88:91], v0 offset:18432\n" \
  "ds_read_b128 v[92:95], v0 offset:19456\n" \
  "buffer_store_dwordx4 v[64:67], v0, s[36:39], s26 offen offset:0 sc1\n" \
  "buffer_store_dwordx4 v[64:67], v12, s[36:39], s33 offen sc1\n" \
  "ds_read_b128 v[96:99], v0 offset:20480\n" \
  "ds_read_b128 v[100:103], v0 offset:21504\n" \
  "ds_read_b128 v[104:107], v0 offset:22528\n" \
  "ds_read_b128 v[108:111], v0 offset:23552\n" \
  "s_waitcnt lgkmcnt(7)\n" \
  "v_smfmac_f32_16x16x64_bf16 v[112:115], v[80:83], v[16:23], v9\n" \
  "s_mov_b32 s92, 0x00ff00ff\n" \
  "s_mov_b32 s93, 0x00ff00ff\n" \
  "v_smfmac_f32_16x16x64_bf16 v[116:119], v[80:83], v[24:31], v9\n" \
  "s_waitcnt lgkmcnt(5)\n" \
  "ds_read_b128 v[16:19], v1 offset:49152\n" \
  "v_smfmac_f32_16x16x64_bf16 v[112:115], v[84:87], v[32:39], v9\n" \
  "ds_read_b128 v[20:23], v1 offset:50176\n" \
  "v_smfmac_f32_16x16x64_bf16 v[116:119], v[84:87], v[40:47], v9\n" \
  "buffer_store_dwordx4 v[68:71], v0, s[36:39], s26 offen offset:1024 sc1\n" \
  "v_smfmac_f32_16x16x64_bf16 v[112:115], v[88:91], v[48:55], v9\n" \
  "ds_read_b128 v[24:27], v1 offset:51200\n" \
  "v_smfmac_f32_16x16x64_bf16 v[116:119], v[88:91], v[56:63], v9\n" \
  "s_waitcnt lgkmcnt(5)\n" \
  "ds_read_b128 v[28:31], v1 offset:52224\n" \
  "v_smfmac_f32_16x16x64_bf16 v[112:115], v[96:99], a[0:7], v9\n" \
  "v_smfmac_f32_16x16x64_bf16 v[116:119], v[96:99], a[8:15], v9\n" \
  "buffer_store_dwordx4 v[72:75], v0, s[36:39], s26 offen offset:2048 sc1\n" \
  "v_smfmac_f32_16x16x64_bf16 v[112:115], v[100:103], a[16:23], v9\n" \
  "s_waitcnt lgkmcnt(4)\n" \
  "v_smfmac_f32_16x16x64_bf16 v[112:115], v[104:107], a[32:39], v9\n" \
  "buffer_store_dwordx4 v[76:79], v0, s[36:39], s26 offen offset:3072 sc1\n" \
  "v_smfmac_f32_16x16x64_bf16 v[112:115], v[108:111], a[48:55], v9\n" \
  "s_waitcnt lgkmcnt(0)\n" \
  "v_smfmac_f32_16x16x64_bf16 v[112:115], v[92:95], v[16:23], v9\n" \
  "s_add_u32 s95, s94, s82\n" \
  "v_mov_b32 v13, s95\n" \
  "v_smfmac_f32_16x16x64_bf16 v[116:119], v[100:103], a[24:31], v9\n" \
  "buffer_store_dword v13, off, s[36:39], s81 sc1\n" \
  "v_smfmac_f32_16x16x64_bf16 v[116:119], v[104:107], a[40:47], v9\n" \
  "v_add_f32 v112, v112, v114\n" \
  "v_smfmac_f32_16x16x64_bf16 v[116:119], v[108:111], a[56:63], v9\n" \
  "ds_read_b128 v[32:35], v0 offset:4096\n" \
  "v_add_f32 v113, v113, v115\n" \
  "v_smfmac_f32_16x16x64_bf16 v[116:119], v[92:95], v[24:31], v9\n" \
  "ds_read_b128 v[36:39], v0 offset:5120\n" \
  "v_mul_f32 v112, s91, v112\n" \
  "v_smfmac_f32_16x16x64_bf16 v[120:123], v[80:83], v[128:135], v9\n" \
  "ds_read_b128 v[40:43], v0 offset:6144\n" \
  "v_mul_f32 v113, s91, v113\n" \
  "v_smfmac_f32_16x16x64_bf16 v[124:127], v[80:83], v[136:143], v9\n" \
  "ds_read_b128 v[44:47], v0 offset:7168\n" \
  "v_add_f32 v116, v116, v118\n" \
  "v_smfmac_f32_16x16x64_bf16 v[120:123], v[84:87], v[144:151], v9\n" \
  "ds_read_b128 v[48:51], v0 offset:0\n" \
  "v_add_f32 v117, v117, v119\n" \
  "v_smfmac_f32_16x16x64_bf16 v[124:127], v[84:87], v[152:159], v9\n" \
  "ds_read_b128 v[52:55], v0 offset:1024\n" \
  "v_mul_f32 v116, s91, v116\n" \
  "v_smfmac_f32_16x16x64_bf16 v[120:123], v[88:91], v[160:167], v9\n" \
  "ds_read_b128 v[56:59], v0 offset:2048\n" \
  "v_mul_f32 v117, s91, v117\n" \
  "v_smfmac_f32_16x16x64_bf16 v[124:127], v[88:91], v[168:175], v9\n" \
  "ds_read_b128 v[60:63], v0 offset:3072\n" \
  "v_cvt_pk_bf16_f32 v112, v112, v116\n" \
  "v_smfmac_f32_16x16x64_bf16 v[120:123], v[92:95], v[176:183], v9\n" \
  "v_cvt_pk_bf16_f32 v113, v113, v117\n" \
  "v_smfmac_f32_16x16x64_bf16 v[124:127], v[92:95], v[184:191], v9\n" \
  "ds_write2_b32 v4, v112, v113 offset0:0 offset1:136\n" \
  "v_cmp_u_f32 vcc, v116, v117\n" \
  "v_smfmac_f32_16x16x64_bf16 v[120:123], v[96:99], a[64:71], v9\n" \
  "s_cbranch_vccnz .Lch_gf\n" \
  ".Lch_gok:\n" \
  "v_mov_b64 v[112:113], 0\n" \
  "v_smfmac_f32_16x16x64_bf16 v[124:127], v[96:99], a[72:79], v9\n" \
  "v_mov_b64 v[114:115], 0\n" \
  "v_smfmac_f32_16x16x64_bf16 v[120:123], v[100:103], a[80:87], v9\n" \
  "s_waitcnt vmcnt(6) lgkmcnt(0)\n" \
  "s_barrier\n" \
  "v_mov_b64 v[116:117], 0\n" \
  "v_smfmac_f32_16x16x64_bf16 v[120:123], v[104:107], a[96:103], v9\n" \
  "ds_read_b128 v[16:19], v6 offset:0\n" \
  "v_mov_b64 v[118:119], 0\n" \
  "v_smfmac_f32_16x16x64_bf16 v[120:123], v[108:111], a[112:119], v9\n" \
  "ds_read_b128 v[20:23], v6 offset:64\n" \
  "v_smfmac_f32_16x16x64_bf16 v[124:127], v[100:103], a[88:95], v9\n" \
  "ds_read_b128 v[24:27], v6 offset:128\n" \
  "v_smfmac_f32_16x16x64_bf16 v[124:127], v[104:107], a[104:111], v9\n" \
  "ds_read_b128 v[28:31], v6 offset:192\n" \
  "v_add_f32 v120, v120, v122\n" \
  "v_smfmac_f32_16x16x64_bf16 v[124:127], v[108:111], a[120:127], v9\n" \
  "ds_read_b128 v[64:67], v5 offset:0\n" \
  "v_add_f32 v121, v121, v123\n" \
  "v_smfmac_f32_16x16x64_bf16 v[112:115], v[80:83], a[128:135], v9\n" \
  "ds_read_b128 v[68:71], v5 offset:64\n" \
  "v_smfmac_f32_16x16x64_bf16 v[116:119], v[80:83], a[136:143], v9\n" \
  "ds_read_b128 v[80:83], v11 offset:31232\n" \
  "v_add_f32 v124, v124, v126\n" \
  "v_smfmac_f32_16x16x64_bf16 v[112:115], v[84:87], a[144:151], v9\n" \
  "ds_read_b128 v[72:75], v5 offset:128\n" \
  "v_add_f32 v125, v125, v127\n" \
  "v_smfmac_f32_16x16x64_bf16 v[116:119], v[84:87], a[152:159], v9\n" \
  "ds_read_b128 v[76:79], v5 offset:192\n" \
  "v_cvt_pk_bf16_f32 v120, v120, v124\n" \
  "v_smfmac_f32_16x16x64_bf16 v[112:115], v[88:91], a[160:167], v9\n" \
  "v_cvt_pk_bf16_f32 v121, v121, v125\n" \
  "v_smfmac_f32_16x16x64_bf16 v[116:119], v[88:91], a[168:175], v9\n" \
  "s_waitcnt lgkmcnt(2)\n" \
  "ds_write2_b32 v4, v120, v121 offset0:64 offset1:200\n" \
  "v_mfma_f32_16x16x32_bf16 v[124:127], v[32:35], v[16:19], v[80:83]\n" \
  "ds_read_b128 v[84:87], v2 offset:2048\n" \
  "v_mov_b64 v[16:17], 0\n" \
  "v_mfma_f32_16x16x32_bf16 v[124:127], v[36:39], v[20:23], v[124:127]\n" \
  "ds_read_b128 v[88:91], v2 offset:3072\n" \
  "v_mov_b64 v[18:19], 0\n" \
  "v_mfma_f32_16x16x32_bf16 v[124:127], v[40:43], v[24:27], v[124:127]\n" \
  "s_waitcnt lgkmcnt(2)\n" \
  "s_barrier\n" \
  "v_mov_b64 v[20:21], 0\n" \
  "v_mfma_f32_16x16x32_bf16 v[124:127], v[44:47], v[28:31], v[124:127]\n" \
  "ds_read_b128 v[32:35], v7 offset:0\n" \
  "v_mov_b64 v[22:23], 0\n" \
  "v_mfma_f32_16x16x32_bf16 v[124:127], v[48:51], v[64:67], v[124:127]\n" \
  "ds_read_b128 v[36:39], v7 offset:64\n" \
  "v_mov_b64 v[24:25], 0\n" \
  "v_mfma_f32_16x16x32_bf16 v[124:127], v[52:55], v[68:71], v[124:127]\n" \
  "ds_read_b128 v[40:43], v7 offset:128\n" \
  "v_mov_b64 v[26:27], 0\n" \
  "v_mfma_f32_16x16x32_bf16 v[124:127], v[56:59], v[72:75], v[124:127]\n" \
  "ds_read_b128 v[44:47], v7 offset:192\n" \
  "v_mov_b64 v[28:29], 0\n" \
  "v_mfma_f32_16x16x32_bf16 v[124:127], v[60:63], v[76:79], v[124:127]\n" \
  "ds_read_b128 v[48:51], v3 offset:8192\n" \
  "v_mov_b64 v[30:31], 0\n" \
  "v_smfmac_f32_16x16x64_bf16 v[112:115], v[92:95], a[176:183], v9\n" \
  "ds_read_b128 v[52:55], v3 offset:12288\n" \
  "v_mov_b64 v[56:57], s[30:31]\n" \
  "v_smfmac_f32_16x16x64_bf16 v[116:119], v[92:95], a[184:191], v9\n" \
  "s_waitcnt lgkmcnt(2)\n" \
  "v_mov_b64 v[58:59], s[30:31]\n" \
  "v_mfma_f32_16x16x32_bf16 v[120:123], v[32:35], v[64:67], v[80:83]\n" \
  "v_exp_f32 v60, v124\n" \
  "v_mfma_f32_16x16x32_bf16 v[120:123], v[36:39], v[68:71], v[120:123]\n" \
  "v_exp_f32 v61, v125\n" \
  "v_mfma_f32_16x16x32_bf16 v[120:123], v[40:43], v[72:75], v[120:123]\n" \
  "v_exp_f32 v62, v126\n" \
  "v_mfma_f32_16x16x32_bf16 v[120:123], v[44:47], v[76:79], v[120:123]\n" \
  "v_exp_f32 v63, v127\n" \
  "v_smfmac_f32_16x16x64_bf16 v[112:115], v[96:99], a[192:199], v9\n" \
  "ds_read_b128 v[40:43], v2 offset:0\n" \
  "v_cvt_pk_bf16_f32 v60, v60, v61\n" \
  "v_smfmac_f32_16x16x64_bf16 v[116:119], v[96:99], a[200:207], v9\n" \
  "ds_read_b128 v[44:47], v2 offset:1024\n" \
  "v_cvt_pk_bf16_f32 v61, v62, v63\n" \
  "v_smfmac_f32_16x16x64_bf16 v[112:115], v[100:103], a[208:215], v9\n" \
  "ds_read_b128 v[92:95], v2 offset:16384\n" \
  "v_mov_b32_dpp v62, v60 row_ror:8 row_mask:0xf bank_mask:0xf\n" \
  "v_smfmac_f32_16x16x64_bf16 v[116:119], v[100:103], a[216:223], v9\n" \
  "ds_read_b128 v[96:99], v2 offset:17408\n" \
  "v_mov_b32_dpp v63, v61 row_ror:8 row_mask:0xf bank_mask:0xf\n" \
  "v_smfmac_f32_16x16x64_bf16 v[112:115], v[104:107], a[224:231], v9\n" \
  "s_waitcnt lgkmcnt(4)\n" \
  "v_mov_b64 v[76:77], 0x3f803f80\n" \
  "v_mfma_f32_16x16x32_bf16 v[72:75], v[56:59], v[60:63], 0\n" \
  "v_exp_f32 v122, v120\n" \
  "v_mfma_f32_16x16x32_bf16 v[64:67], v[48:51], v[60:63], 0\n" \
  "v_exp_f32 v123, v121\n" \
  "v_smfmac_f32_16x16x64_bf16 v[116:119], v[104:107], a[232:239], v9\n" \
  "ds_read_b128 v[100:103], v2 offset:32768\n" \
  "v_cvt_pk_bf16_f32 v14, v122, v123\n" \
  "v_smfmac_f32_16x16x64_bf16 v[112:115], v[108:111], a[240:247], v9\n" \
  "ds_read_b128 v[104:107], v2 offset:33792\n" \
  "v_cvt_pk_bf16_f32 v15, v122, v123\n" \
  "v_smfmac_f32_16x16x64_bf16 v[116:119], v[108:111], a[248:255], v9\n" \
  "buffer_load_dword v108, v0, s[36:39], s80 offen sc1\n" \
  "v_mfma_f32_16x16x32_bf16 v[68:71], v[52:55], v[60:63], 0\n" \
  "v_cvt_pk_bf16_f32 v32, v112, v113\n" \
  "v_cvt_pk_bf16_f32 v33, v114, v115\n" \
  "v_mfma_f32_16x16x16_bf16 v[72:75], v[76:77], v[14:15], v[72:75]\n" \
  "v_cvt_pk_bf16_f32 v34, v116, v117\n" \
  "v_cvt_pk_bf16_f32 v35, v118, v119\n" \
  "v_mfma_f32_16x16x16_bf16 v[64:67], v[32:33], v[14:15], v[64:67]\n" \
  "s_nop 0\n" \
  "v_mfma_f32_16x16x16_bf16 v[68:71], v[34:35], v[14:15], v[68:71]\n" \
  "s_mov_b64 exec, s[92:93]\n" \
  "v_rcp_f32 v13, v72\n" \
  ".Lch_bok:\n" \
  "s_nop 1\n" \
  "v_mul_f32 v64, v64, v13\n" \
  "v_mul_f32 v65, v65, v13\n" \
  "v_mul_f32 v66, v66, v13\n" \
  "v_mul_f32 v67, v67, v13\n" \
  "v_pk_mul_f32 v[68:69], v[68:69], v[12:13] op_sel:[0,1] op_sel_hi:[1,1]\n" \
  "v_pk_mul_f32 v[70:71], v[70:71], v[12:13] op_sel:[0,1] op_sel_hi:[1,1]\n" \
  "v_cvt_pk_bf16_f32 v36, v64, v68\n" \
  "v_cvt_pk_bf16_f32 v37, v65, v69\n" \
  "v_cvt_pk_bf16_f32 v38, v66, v70\n" \
  "v_cvt_pk_bf16_f32 v39, v67, v71\n" \
  "ds_write_b128 v10, v[36:39]\n" \
  "v_max_f32 v73, v72, v13\n" \
  "v_cmp_ngt_f32 vcc, 0x4e800000, v73\n" \
  "s_mov_b64 exec, -1\n" \
  "v_mov_b32_dpp v36, v36 row_ror:8 row_mask:0xf bank_mask:0xc\n" \
  "v_mov_b32_dpp v37, v37 row_ror:8 row_mask:0xf bank_mask:0xc\n" \
  "v_mov_b32_dpp v38, v38 row_ror:8 row_mask:0xf bank_mask:0xc\n" \
  "v_mov_b32_dpp v39, v39 row_ror:8 row_mask:0xf bank_mask:0xc\n" \
  "s_cbranch_vccnz .Lch_big\n" \
  "v_xor_b32 v13, 512, v10\n" \
  "v_xor_b32 v14, 1024, v10\n" \
  "v_xor_b32 v15, 1536, v10\n" \
  "v_smfmac_f32_16x16x64_bf16 v[28:31], v[36:39], v[84:91], v9 abid:1\n" \
  "s_waitcnt vmcnt(1) lgkmcnt(0)\n" \
  "s_barrier\n" \
  "ds_read_b128 v[64:67], v13 offset:0\n" \
  "ds_read_b128 v[68:71], v14 offset:0\n" \
  "ds_read_b128 v[72:75], v15 offset:0\n" \
  "v_smfmac_f32_16x16x64_bf16 v[24:27], v[36:39], v[40:47], v9 abid:1\n" \
  "ds_read_b128 v[48:51], v2 offset:49152\n" \
  "v_add_u32_e64 v15, s84, -1\n" \
  "v_smfmac_f32_16x16x64_bf16 v[20:23], v[36:39], v[224:231], v9 abid:1\n" \
  "ds_read_b128 v[52:55], v2 offset:50176\n" \
  "v_smfmac_f32_16x16x64_bf16 v[16:19], v[36:39], v[192:199], v9 abid:1\n" \
  "ds_read_b128 v[56:59], v2 offset:18432\n" \
  "buffer_load_dword v13, v12, s[36:39], s58 offen sc1\n" \
  "s_waitcnt lgkmcnt(5)\n" \
  "v_smfmac_f32_16x16x64_bf16 v[16:19], v[64:67], v[200:207], v9 abid:1\n" \
  "ds_read_b128 v[60:63], v2 offset:19456\n" \
  "s_waitcnt lgkmcnt(5)\n" \
  "v_smfmac_f32_16x16x64_bf16 v[16:19], v[68:71], v[208:215], v9 abid:1\n" \
  "ds_read_b128 v[112:115], v2 offset:34816\n" \
  "s_waitcnt lgkmcnt(5)\n" \
  "v_smfmac_f32_16x16x64_bf16 v[16:19], v[72:75], v[216:223], v9 abid:1\n" \
  "ds_read_b128 v[116:119], v2 offset:35840\n" \
  "v_smfmac_f32_16x16x64_bf16 v[20:23], v[64:67], v[232:239], v9 abid:1\n" \
  "ds_read_b128 v[120:123], v2 offset:51200\n" \
  "v_smfmac_f32_16x16x64_bf16 v[20:23], v[68:71], v[240:247], v9 abid:1\n" \
  "ds_read_b128 v[124:127], v2 offset:52224\n" \
  "v_cvt_pk_bf16_f32 v16, v16, v17\n" \
  "v_smfmac_f32_16x16x64_bf16 v[20:23], v[72:75], v[248:255], v9 abid:1\n" \
  "v_cvt_pk_bf16_f32 v17, v18, v19\n" \
  "v_smfmac_f32_16x16x64_bf16 v[24:27], v[64:67], v[92:99], v9 abid:1\n" \
  "s_waitcnt lgkmcnt(4)\n" \
  "v_smfmac_f32_16x16x64_bf16 v[24:27], v[68:71], v[100:107], v9 abid:1\n" \
  "v_cvt_pk_bf16_f32 v18, v20, v21\n" \
  "v_smfmac_f32_16x16x64_bf16 v[24:27], v[72:75], v[48:55], v9 abid:1\n" \
  "s_waitcnt lgkmcnt(0)\n" \
  "v_cvt_pk_bf16_f32 v19, v22, v23\n" \
  "v_smfmac_f32_16x16x64_bf16 v[28:31], v[64:67], v[56:63], v9 abid:1\n" \
  "buffer_store_dwordx4 v[16:19], v8, s[36:39], s87 offen sc1\n" \
  "v_smfmac_f32_16x16x64_bf16 v[28:31], v[68:71], v[112:119], v9 abid:1\n" \
  "v_cvt_pk_bf16_f32 v24, v24, v25\n" \
  "v_smfmac_f32_16x16x64_bf16 v[28:31], v[72:75], v[120:127], v9 abid:1\n" \
  "s_cmp_gt_u32 s50, 1\n" \
  "v_cvt_pk_bf16_f32 v25, v26, v27\n" \
  "s_nop 5\n" \
  "v_cvt_pk_bf16_f32 v26, v28, v29\n" \
  "v_cvt_pk_bf16_f32 v27, v30, v31\n" \
  "buffer_store_dwordx4 v[24:27], v8, s[36:39], s88 offen sc1\n" \
  "buffer_store_dword v15, v12, s[36:39], s59 offen sc1\n" \
  "ds_write_b128 v3, v[32:35] offset:16384\n" \
  "s_cbranch_scc1 .Lch_w23\n" \
  "s_waitcnt lgkmcnt(0)\n" \
  "s_barrier\n" \
  "s_cmp_eq_u32 s84, 63\n" \
  "s_cbranch_scc1 .Lch_end\n" \
  "v_and_b32 v14, 0x70, v12\n" \
  "s_add_u32 s33, s22, s85\n" \
  "ds_read_b128 v[80:83], v14 offset:30720\n" \
  "s_mov_b32 s32, s23\n" \
  "s_add_u32 s52, s32, 32768\n" \
  "s_add_u32 s84, s84, 1\n" \
  "s_xor_b32 s85, s85, 0x10000\n" \
  "s_xor_b32 s23, s23, 540672\n" \
  "s_xor_b32 s87, s87, 540672\n" \
  "s_xor_b32 s88, s88, 540672\n" \
  "v_mov_b32 v92, 30848\n" \
  "v_lshl_add_u32 v14, s50, 2, v92\n" \
  "v_mov_b32 v94, s84\n" \
  "ds_write_b32 v14, v94\n" \
  "s_cmp_eq_u32 s50, 1\n" \
  "s_cbranch_scc1 .Lch_w1\n" \
  "s_waitcnt vmcnt(4)\n" \
  "v_readlane_b32 s94, v108, s86\n" \
  "s_memrealtime s[96:97]\n" \
  "s_nop 0\n" \
  "v_subrev_u32 v108, s94, v108\n" \
  "v_med3_i32 v108, v108, 0, 40\n" \
  "s_nop 1\n" \
  "v_max_i32_dpp v108, v108, v108 row_ror:8 row_mask:0xf bank_mask:0xf\n" \
  "s_nop 1\n" \
  "v_max_i32_dpp v108, v108, v108 row_ror:4 row_mask:0xf bank_mask:0xf\n" \
  "s_nop 1\n" \
  "v_max_i32_dpp v108, v108, v108 row_ror:2 row_mask:0xf bank_mask:0xf\n" \
  "s_nop 1\n" \
  "v_max_i32_dpp v108, v108, v108 row_ror:1 row_mask:0xf bank_mask:0xf\n" \
  "s_add_u32 s94, s94, s54\n" \
  "v_readlane_b32 s92, v108, 0\n" \
  "v_readlane_b32 s93, v108, 16\n" \
  "s_max_i32 s92, s92, s93\n" \
  "s_add_u32 s94, s94, s92\n" \
  "s_waitcnt lgkmcnt(0)\n" \
  "s_sub_u32 s95, s94, s96\n" \
  "s_cmp_gt_u32 s95, 80\n" \
  "s_cselect_b32 s94, s96, s94\n" \
  ".p2align 4\n" \
  ".Lch_n1:\n" \
  "s_memrealtime s[96:97]\n" \
  "s_sub_u32 s95, s94, s96\n" \
  "s_cmp_gt_i32 s95, 3\n" \
  "s_cbranch_scc1 .Lch_n1\n" \
  ".Lch_n1b:\n" \
  "s_sub_u32 s95, s95, 1\n" \
  "s_cmp_ge_i32 s95, 0\n" \
  "s_cbranch_scc1 .Lch_n1b\n" \
  "buffer_load_dwordx4 v[96:99], v0, s[36:39], s32 offen offset:0 sc1\n" \
  "buffer_load_dwordx4 v[100:103], v0, s[36:39], s32 offen offset:1024 sc1\n" \
  "buffer_load_dwordx4 v[104:107], v0, s[36:39], s32 offen offset:2048 sc1\n" \
  "buffer_load_dwordx4 v[108:111], v0, s[36:39], s32 offen offset:3072 sc1\n" \
  "buffer_load_dwordx4 v[112:115], v0, s[36:39], s52 offen offset:0 sc1\n" \
  "buffer_load_dwordx4 v[116:119], v0, s[36:39], s52 offen offset:1024 sc1\n" \
  "buffer_load_dwordx4 v[120:123], v0, s[36:39], s52 offen offset:2048 sc1\n" \
  "buffer_load_dwordx4 v[124:127], v0, s[36:39], s52 offen offset:3072 sc1\n" \
  "ds_read_b128 v[16:19], v1 offset:0\n" \
  "ds_read_b128 v[20:23], v1 offset:1024\n" \
  "ds_read_b128 v[24:27], v1 offset:2048\n" \
  "ds_read_b128 v[28:31], v1 offset:3072\n" \
  "ds_read_b128 v[32:35], v1 offset:16384\n" \
  "ds_read_b128 v[36:39], v1 offset:17408\n" \
  "ds_read_b128 v[40:43], v1 offset:18432\n" \
  "ds_read_b128 v[44:47], v1 offset:19456\n" \
  "ds_read_b128 v[48:51], v1 offset:32768\n" \
  "ds_read_b128 v[52:55], v1 offset:33792\n" \
  "ds_read_b128 v[56:59], v1 offset:34816\n" \
  "ds_read_b128 v[60:63], v1 offset:35840\n" \
  "v_mov_b64 v[64:65], -1\n" \
  "v_mov_b64 v[66:67], -1\n" \
  "v_mov_b64 v[68:69], -1\n" \
  "v_mov_b64 v[70:71], -1\n" \
  "v_mov_b64 v[72:73], -1\n" \
  "v_mov_b64 v[74:75], -1\n" \
  "v_mov_b64 v[76:77], -1\n" \
  "v_mov_b64 v[78:79], -1\n" \
  "s_add_u32 s34, s22, s85\n" \
  "s_mov_b32 s93, 2\n" \
  "s_waitcnt lgkmcnt(0)\n" \
  ".Lch_p1:\n" \
  "s_waitcnt vmcnt(7)\n" \
  "v_mfma_f32_16x16x32_bf16 v[84:87], v[96:99], v[80:83], 0\n" \
  "s_waitcnt vmcnt(6)\n" \
  "v_mfma_f32_16x16x32_bf16 v[84:87], v[100:103], v[80:83], v[84:87]\n" \
  "s_waitcnt vmcnt(5)\n" \
  "v_mfma_f32_16x16x32_bf16 v[84:87], v[104:107], v[80:83], v[84:87]\n" \
  "s_waitcnt vmcnt(4)\n" \
  "v_mfma_f32_16x16x32_bf16 v[84:87], v[108:111], v[80:83], v[84:87]\n" \
  "s_waitcnt vmcnt(3)\n" \
  "v_mfma_f32_16x16x32_bf16 v[84:87], v[112:115], v[80:83], v[84:87]\n" \
  "s_waitcnt vmcnt(2)\n" \
  "v_mfma_f32_16x16x32_bf16 v[84:87], v[116:119], v[80:83], v[84:87]\n" \
  "s_waitcnt vmcnt(1)\n" \
  "v_mfma_f32_16x16x32_bf16 v[84:87], v[120:123], v[80:83], v[84:87]\n" \
  "s_waitcnt vmcnt(0)\n" \
  "v_mfma_f32_16x16x32_bf16 v[84:87], v[124:127], v[80:83], v[84:87]\n" \
  "s_nop 7\n" \
  "v_cmp_u_f32 vcc, v84, v85\n" \
  "v_cmp_u_f32_e64 s[96:97], v86, v87\n" \
  "v_cvt_pk_bf16_f32 v88, v84, v85\n" \
  "v_cvt_pk_bf16_f32 v90, v84, v85\n" \
  "v_cvt_pk_bf16_f32 v89, v86, v87\n" \
  "v_cvt_pk_bf16_f32 v91, v86, v87\n" \
  "s_or_b64 vcc, vcc, s[96:97]\n" \
  "v_permlane16_swap_b32 v88, v90\n" \
  "v_permlane16_swap_b32 v89, v91\n" \
  "s_cbranch_scc1 .Lch_nan\n" \
  ".Lch_p1_ok:\n" \
  "buffer_store_dwordx4 v[88:91], v12, s[36:39], s34 offen sc1\n" \
  "ds_write_b64 v92, v[94:95] offset:16\n" \
  "s_mov_b32 s26, s32\n" \
  "v_mov_b64 v[112:113], 0\n" \
  "v_mov_b64 v[114:115], 0\n" \
  "v_mov_b64 v[116:117], 0\n" \
  "v_mov_b64 v[118:119], 0\n" \
  "v_mov_b64 v[120:121], 0\n" \
  "v_mov_b64 v[122:123], 0\n" \
  "v_mov_b64 v[124:125], 0\n" \
  "v_mov_b64 v[126:127], 0\n" \
  "s_branch .Lch_b0\n" \
  ".Lch_w1:\n" \
  "s_memrealtime s[96:97]\n" \
  "s_waitcnt lgkmcnt(0)\n" \
  "s_sub_u32 s96, s96, s95\n" \
  "s_min_i32 s96, s96, 6\n" \
  "s_max_i32 s96, s96, -6\n" \
  "s_add_u32 s49, s49, s96\n" \
  "ds_read_b128 v[16:19], v1 offset:0\n" \
  "ds_read_b128 v[20:23], v1 offset:1024\n" \
  "ds_read_b128 v[24:27], v1 offset:2048\n" \
  "ds_read_b128 v[28:31], v1 offset:3072\n" \
  "ds_read_b128 v[32:35], v1 offset:16384\n" \
  "ds_read_b128 v[36:39], v1 offset:17408\n" \
  "ds_read_b128 v[40:43], v1 offset:18432\n" \
  "ds_read_b128 v[44:47], v1 offset:19456\n" \
  "ds_read_b128 v[48:51], v1 offset:32768\n" \
  "ds_read_b128 v[52:55], v1 offset:33792\n" \
  "ds_read_b128 v[56:59], v1 offset:34816\n" \
  "ds_read_b128 v[60:63], v1 offset:35840\n" \
  "v_mov_b64 v[64:65], -1\n" \
  "v_mov_b64 v[66:67], -1\n" \
  "v_mov_b64 v[68:69], -1\n" \
  "v_mov_b64 v[70:71], -1\n" \
  "v_mov_b64 v[72:73], -1\n" \
  "v_mov_b64 v[74:75], -1\n" \
  "v_mov_b64 v[76:77], -1\n" \
  "v_mov_b64 v[78:79], -1\n" \
  "ds_write_b128 v0, v[64:67] offset:20480\n" \
  "ds_write_b128 v0, v[64:67] offset:21504\n" \
  "ds_write_b128 v0, v[64:67] offset:22528\n" \
  "ds_write_b128 v0, v[64:67] offset:23552\n" \
  "s_mov_b32 s26, s52\n" \
  "s_mov_b32 s33, s28\n" \
  "s_add_u32 s90, s24, s85\n" \
  "s_add_u32 s25, s90, 32768\n" \
  "s_add_u32 s56, s56, 2097152\n" \
  "s_xor_b32 s56, s56, 8192\n" \
  "v_and_b32 v93, 0x1f0, v0\n" \
  "v_and_b32 v14, 0x200, v0\n" \
  "v_lshlrev_b32 v93, 3, v93\n" \
  "v_lshl_or_b32 v93, v14, 6, v93\n" \
  "s_mov_b32 s94, s84\n" \
  "s_mov_b32 s95, s29\n" \
  ".p2align 3\n" \
  ".Lch_n4:\n" \
  "s_sub_u32 s95, s95, 1\n" \
  "s_cbranch_scc0 .Lch_n4\n" \
  ".Lch_c3:\n" \
  "ds_read_b64 v[14:15], v92 offset:32\n" \
  "s_waitcnt lgkmcnt(0)\n" \
  "v_min_u32 v14, v14, v15\n" \
  "v_cmp_gt_u32 vcc, s84, v14\n" \
  "s_cbranch_vccnz .Lch_c3\n" \
  "s_branch .Lch_f1_chk\n" \
  ".Lch_f1:\n" \
  "s_sleep 2\n" \
  "s_waitcnt vmcnt(3)\n" \
  "buffer_load_dword v13, v12, s[36:39], s58 offen sc1\n" \
  "s_waitcnt vmcnt(0)\n" \
  ".Lch_f1_chk:\n" \
  "s_waitcnt vmcnt(3)\n" \
  "v_cmp_gt_i32 vcc, s94, v13\n" \
  "s_and_b32 vcc_lo, vcc_lo, 7\n" \
  "s_cbranch_scc0 .Lch_f1_ok\n" \
  "s_sub_u32 s89, s89, 1\n" \
  "s_cbranch_scc0 .Lch_f1\n" \
  "s_mov_b32 s89, 0\n" \
  ".Lch_f1_ok:\n" \
  "s_mov_b32 m0, 0\n" \
  "s_nop 0\n" \
  "buffer_load_dwordx4 v0, s[36:39], s56 offen offset:0 sc1 lds\n" \
  "buffer_load_dwordx4 v0, s[36:39], s56 offen offset:1024 sc1 lds\n" \
  "buffer_load_dwordx4 v0, s[36:39], s56 offen offset:2048 sc1 lds\n" \
  "buffer_load_dwordx4 v0, s[36:39], s56 offen offset:3072 sc1 lds\n" \
  "s_mov_b32 m0, 4096\n" \
  "s_add_u32 s92, s56, 4096\n" \
  "buffer_load_dwordx4 v0, s[36:39], s92 offen offset:0 sc1 lds\n" \
  "buffer_load_dwordx4 v0, s[36:39], s92 offen offset:1024 sc1 lds\n" \
  "buffer_load_dwordx4 v0, s[36:39], s92 offen offset:2048 sc1 lds\n" \
  "buffer_load_dwordx4 v0, s[36:39], s92 offen offset:3072 sc1 lds\n" \
  "s_mov_b32 m0, 8192\n" \
  "s_add_u32 s92, s56, 524288\n" \
  "buffer_load_dwordx4 v0, s[36:39], s92 offen offset:0 sc1 lds\n" \
  "buffer_load_dwordx4 v0, s[36:39], s92 offen offset:1024 sc1 lds\n" \
  "buffer_load_dwordx4 v0, s[36:39], s92 offen offset:2048 sc1 lds\n" \
  "buffer_load_dwordx4 v0, s[36:39], s92 offen offset:3072 sc1 lds\n" \
  "s_mov_b32 m0, 12288\n" \
  "s_add_u32 s92, s56, 528384\n" \
  "buffer_load_dwordx4 v0, s[36:39], s92 offen offset:0 sc1 lds\n" \
  "buffer_load_dwordx4 v0, s[36:39], s92 offen offset:1024 sc1 lds\n" \
  "buffer_load_dwordx4 v0, s[36:39], s92 offen offset:2048 sc1 lds\n" \
  "buffer_load_dwordx4 v0, s[36:39], s92 offen offset:3072 sc1 lds\n" \
  "v_mov_b64 v[112:113], 0\n" \
  "v_mov_b64 v[114:115], 0\n" \
  "v_mov_b64 v[116:117], 0\n" \
  "v_mov_b64 v[118:119], 0\n" \
  "v_mov_b64 v[120:121], 0\n" \
  "v_mov_b64 v[122:123], 0\n" \
  "v_mov_b64 v[124:125], 0\n" \
  "v_mov_b64 v[126:127], 0\n" \
  ".Lch_c2:\n" \
  "ds_read_b64 v[14:15], v92 offset:16\n" \
  "s_waitcnt lgkmcnt(0)\n" \
  "v_cmp_gt_u32 vcc, s84, v14\n" \
  "s_cbranch_vccnz .Lch_c2\n" \
  "s_mov_b32 s95, s55\n" \
  ".p2align 3\n" \
  ".Lch_n2:\n" \
  "s_sub_u32 s95, s95, 1\n" \
  "s_cbranch_scc0 .Lch_n2\n" \
  "buffer_load_dword v14, v93, s[36:39], s90 offen sc1\n" \
  "s_mov_b32 m0, 16384\n" \
  "s_nop 0\n" \
  "buffer_load_dwordx4 v0, s[36:39], s90 offen offset:0 sc1 lds\n" \
  "buffer_load_dwordx4 v0, s[36:39], s90 offen offset:1024 sc1 lds\n" \
  "buffer_load_dwordx4 v0, s[36:39], s90 offen offset:2048 sc1 lds\n" \
  "buffer_load_dwordx4 v0, s[36:39], s90 offen offset:3072 sc1 lds\n" \
  "s_mov_b32 m0, 20480\n" \
  "s_nop 0\n" \
  "buffer_load_dwordx4 v0, s[36:39], s25 offen offset:0 sc1 lds\n" \
  "buffer_load_dwordx4 v0, s[36:39], s25 offen offset:1024 sc1 lds\n" \
  "buffer_load_dwordx4 v0, s[36:39], s25 offen offset:2048 sc1 lds\n" \
  "buffer_load_dwordx4 v0, s[36:39], s25 offen offset:3072 sc1 lds\n" \
  "s_mov_b32 s95, s63\n" \
  ".p2align 3\n" \
  ".Lch_na0:\n" \
  "s_sub_u32 s95, s95, 1\n" \
  "s_cbranch_scc0 .Lch_na0\n" \
  "buffer_load_dword v15, v93, s[36:39], s90 offen sc1\n" \
  "s_mov_b32 m0, 16384\n" \
  "s_nop 0\n" \
  "buffer_load_dwordx4 v0, s[36:39], s90 offen offset:0 sc1 lds\n" \
  "buffer_load_dwordx4 v0, s[36:39], s90 offen offset:1024 sc1 lds\n" \
  "buffer_load_dwordx4 v0, s[36:39], s90 offen offset:2048 sc1 lds\n" \
  "buffer_load_dwordx4 v0, s[36:39], s90 offen offset:3072 sc1 lds\n" \
  "s_mov_b32 m0, 20480\n" \
  "s_nop 0\n" \
  "buffer_load_dwordx4 v0, s[36:39], s25 offen offset:0 sc1 lds\n" \
  "buffer_load_dwordx4 v0, s[36:39], s25 offen offset:1024 sc1 lds\n" \
  "buffer_load_dwordx4 v0, s[36:39], s25 offen offset:2048 sc1 lds\n" \
  "buffer_load_dwordx4 v0, s[36:39], s25 offen offset:3072 sc1 lds\n" \
  ".Lch_g:\n" \
  "s_waitcnt vmcnt(17) lgkmcnt(0)\n" \
  "v_cmp_eq_u32 vcc, -1, v14\n" \
  "s_cbranch_vccz .Lch_gd\n" \
  "buffer_load_dword v14, v93, s[36:39], s90 offen sc1\n" \
  "s_mov_b32 m0, 16384\n" \
  "s_nop 0\n" \
  "buffer_load_dwordx4 v0, s[36:39], s90 offen offset:0 sc1 lds\n" \
  "buffer_load_dwordx4 v0, s[36:39], s90 offen offset:1024 sc1 lds\n" \
  "buffer_load_dwordx4 v0, s[36:39], s90 offen offset:2048 sc1 lds\n" \
  "buffer_load_dwordx4 v0, s[36:39], s90 offen offset:3072 sc1 lds\n" \
  "s_mov_b32 m0, 20480\n" \
  "s_nop 0\n" \
  "buffer_load_dwordx4 v0, s[36:39], s25 offen offset:0 sc1 lds\n" \
  "buffer_load_dwordx4 v0, s[36:39], s25 offen offset:1024 sc1 lds\n" \
  "buffer_load_dwordx4 v0, s[36:39], s25 offen offset:2048 sc1 lds\n" \
  "buffer_load_dwordx4 v0, s[36:39], s25 offen offset:3072 sc1 lds\n" \
  "s_waitcnt vmcnt(17) lgkmcnt(0)\n" \
  "v_cmp_eq_u32 vcc, -1, v15\n" \
  "s_cbranch_vccz .Lch_gd\n" \
  "buffer_load_dword v15, v93, s[36:39], s90 offen sc1\n" \
  "s_mov_b32 m0, 16384\n" \
  "s_nop 0\n" \
  "buffer_load_dwordx4 v0, s[36:39], s90 offen offset:0 sc1 lds\n" \
  "buffer_load_dwordx4 v0, s[36:39], s90 offen offset:1024 sc1 lds\n" \
  "buffer_load_dwordx4 v0, s[36:39], s90 offen offset:2048 sc1 lds\n" \
  "buffer_load_dwordx4 v0, s[36:39], s90 offen offset:3072 sc1 lds\n" \
  "s_mov_b32 m0, 20480\n" \
  "s_nop 0\n" \
  "buffer_load_dwordx4 v0, s[36:39], s25 offen offset:0 sc1 lds\n" \
  "buffer_load_dwordx4 v0, s[36:39], s25 offen offset:1024 sc1 lds\n" \
  "buffer_load_dwordx4 v0, s[36:39], s25 offen offset:2048 sc1 lds\n" \
  "buffer_load_dwordx4 v0, s[36:39], s25 offen offset:3072 sc1 lds\n" \
  "s_sub_u32 s89, s89, 1\n" \
  "s_cbranch_scc0 .Lch_g\n" \
  "s_mov_b32 s89, 0\n" \
  ".Lch_gd:\n" \
  "s_memrealtime s[82:83]\n" \
  "s_lshr_b32 s94, s49, 4\n" \
  "s_waitcnt vmcnt(13)\n" \
  "s_branch .Lch_b0\n" \
  ".Lch_gf:\n" \
  "s_cmp_eq_u32 s84, 0\n" \
  "s_cbranch_scc1 .Lch_gok\n" \
  "v_max3_u32 v13, v80, v81, v82\n" \
  "v_max3_u32 v13, v13, v83, v84\n" \
  "v_max3_u32 v13, v13, v85, v86\n" \
  "v_max3_u32 v13, v13, v87, v88\n" \
  "v_max3_u32 v13, v13, v89, v90\n" \
  "v_max3_u32 v13, v13, v91, v92\n" \
  "v_max3_u32 v13, v13, v93, v94\n" \
  "v_max3_u32 v13, v13, v95, v96\n" \
  "v_max3_u32 v13, v13, v97, v98\n" \
  "v_max3_u32 v13, v13, v99, v100\n" \
  "v_max3_u32 v13, v13, v101, v102\n" \
  "v_max3_u32 v13, v13, v103, v104\n" \
  "v_max3_u32 v13, v13, v105, v106\n" \
  "v_max3_u32 v13, v13, v107, v108\n" \
  "v_max3_u32 v13, v13, v109, v110\n" \
  "v_max_u32 v13, v13, v111\n" \
  "v_cmp_eq_u32 vcc, -1, v13\n" \
  "s_cbranch_vccz .Lch_gok\n" \
  "s_sub_u32 s89, s89, 1\n" \
  "s_cselect_b32 s89, 0, s89\n" \
  "s_cbranch_scc1 .Lch_gok\n" \
  "ds_read_b128 v[16:19], v1 offset:0\n" \
  "ds_read_b128 v[20:23], v1 offset:1024\n" \
  "ds_read_b128 v[24:27], v1 offset:2048\n" \
  "ds_read_b128 v[28:31], v1 offset:3072\n" \
  "ds_read_b128 v[32:35], v1 offset:16384\n" \
  "ds_read_b128 v[36:39], v1 offset:17408\n" \
  "ds_read_b128 v[40:43], v1 offset:18432\n" \
  "ds_read_b128 v[44:47], v1 offset:19456\n" \
  "ds_read_b128 v[48:51], v1 offset:32768\n" \
  "ds_read_b128 v[52:55], v1 offset:33792\n" \
  "ds_read_b128 v[56:59], v1 offset:34816\n" \
  "ds_read_b128 v[60:63], v1 offset:35840\n" \
  "s_nop 7\n" \
  "s_nop 7\n" \
  "v_mov_b64 v[112:113], 0\n" \
  "v_mov_b64 v[114:115], 0\n" \
  "v_mov_b64 v[116:117], 0\n" \
  "v_mov_b64 v[118:119], 0\n" \
  "v_mov_b64 v[120:121], 0\n" \
  "v_mov_b64 v[122:123], 0\n" \
  "v_mov_b64 v[124:125], 0\n" \
  "v_mov_b64 v[126:127], 0\n" \
  "s_cmp_lg_u32 s50, 1\n" \
  "s_cbranch_scc1 .Lch_z\n" \
  "s_mov_b32 m0, 16384\n" \
  "s_nop 0\n" \
  "buffer_load_dwordx4 v0, s[36:39], s90 offen offset:0 sc1 lds\n" \
  "buffer_load_dwordx4 v0, s[36:39], s90 offen offset:1024 sc1 lds\n" \
  "buffer_load_dwordx4 v0, s[36:39], s90 offen offset:2048 sc1 lds\n" \
  "buffer_load_dwordx4 v0, s[36:39], s90 offen offset:3072 sc1 lds\n" \
  "s_mov_b32 m0, 20480\n" \
  "s_nop 0\n" \
  "buffer_load_dwordx4 v0, s[36:39], s25 offen offset:0 sc1 lds\n" \
  "buffer_load_dwordx4 v0, s[36:39], s25 offen offset:1024 sc1 lds\n" \
  "buffer_load_dwordx4 v0, s[36:39], s25 offen offset:2048 sc1 lds\n" \
  "buffer_load_dwordx4 v0, s[36:39], s25 offen offset:3072 sc1 lds\n" \
  "s_waitcnt vmcnt(0)\n" \
  "s_branch .Lch_z\n" \
  ".Lch_big:\n" \
  "s_nop 7\n" \
  "s_nop 7\n" \
  "v_max3_f32 v13, v124, v125, v126\n" \
  "v_max_f32 v13, v13, v127\n" \
  "s_nop 1\n" \
  "v_max_f32_dpp v13, v13, v13 row_ror:8 row_mask:0xf bank_mask:0xf\n" \
  "v_max3_f32 v13, v13, v120, v121\n" \
  "v_mov_b32 v14, v13\n" \
  "s_nop 1\n" \
  "v_permlane16_swap_b32 v13, v14\n" \
  "v_max_f32 v13, v13, v14\n" \
  "v_mov_b32 v14, v13\n" \
  "s_nop 1\n" \
  "v_permlane32_swap_b32 v13, v14\n" \
  "v_max_f32 v13, v13, v14\n" \
  "s_nop 1\n" \
  "v_mov_b32_dpp v13, v13 row_ror:8 row_mask:0xf bank_mask:0xc\n" \
  "v_sub_f32 v124, v124, v13\n" \
  "v_sub_f32 v125, v125, v13\n" \
  "v_sub_f32 v126, v126, v13\n" \
  "v_sub_f32 v127, v127, v13\n" \
  "v_sub_f32 v120, v120, v13\n" \
  "v_sub_f32 v121, v121, v13\n" \
  "v_min_f32 v72, 0x44800000, v120\n" \
  "v_max_f32 v72, 0xc4800000, v72\n" \
  "v_mov_b32 v73, v72\n" \
  "v_mov_b32 v74, v72\n" \
  "v_mov_b32 v75, v72\n" \
  "s_not_b64 exec, s[92:93]\n" \
  "ds_write_b128 v11, v[72:75] offset:31232\n" \
  "s_mov_b64 exec, -1\n" \
  "v_exp_f32 v60, v124\n" \
  "v_exp_f32 v61, v125\n" \
  "v_exp_f32 v62, v126\n" \
  "v_exp_f32 v63, v127\n" \
  "v_cvt_pk_bf16_f32 v60, v60, v61\n" \
  "v_cvt_pk_bf16_f32 v61, v62, v63\n" \
  "s_nop 0\n" \
  "v_mov_b32_dpp v62, v60 row_ror:8 row_mask:0xf bank_mask:0xf\n" \
  "v_mov_b32_dpp v63, v61 row_ror:8 row_mask:0xf bank_mask:0xf\n" \
  "v_exp_f32 v122, v120\n" \
  "v_exp_f32 v123, v121\n" \
  "s_nop 0\n" \
  "v_cvt_pk_bf16_f32 v14, v122, v123\n" \
  "v_cvt_pk_bf16_f32 v15, v122, v123\n" \
  "s_waitcnt lgkmcnt(0)\n" \
  "v_mfma_f32_16x16x32_bf16 v[72:75], v[56:59], v[60:63], 0\n" \
  "v_mfma_f32_16x16x32_bf16 v[64:67], v[48:51], v[60:63], 0\n" \
  "v_mfma_f32_16x16x32_bf16 v[68:71], v[52:55], v[60:63], 0\n" \
  "v_mfma_f32_16x16x16_bf16 v[72:75], v[76:77], v[14:15], v[72:75]\n" \
  "v_mfma_f32_16x16x16_bf16 v[64:67], v[32:33], v[14:15], v[64:67]\n" \
  "v_mfma_f32_16x16x16_bf16 v[68:71], v[34:35], v[14:15], v[68:71]\n" \
  "v_rcp_f32 v13, v72\n" \
  "v_mov_b32 v72, 1.0\n" \
  "s_mov_b64 exec, s[92:93]\n" \
  "s_nop 7\n" \
  "s_nop 7\n" \
  "s_branch .Lch_bok\n" \
  ".Lch_nan:\n" \
  "s_sub_u32 s93, s93, 1\n" \
  "s_cbranch_scc0 .Lch_nan2\n" \
  "s_mov_b32 s93, 0\n" \
  "v_max3_u32 v13, v96, v97, v98\n" \
  "v_max3_u32 v13, v13, v99, v100\n" \
  "v_max3_u32 v13, v13, v101, v102\n" \
  "v_max3_u32 v13, v13, v103, v104\n" \
  "v_max3_u32 v13, v13, v105, v106\n" \
  "v_max3_u32 v13, v13, v107, v108\n" \
  "v_max3_u32 v13, v13, v109, v110\n" \
  "v_max3_u32 v13, v13, v111, v112\n" \
  "v_max3_u32 v13, v13, v113, v114\n" \
  "v_max3_u32 v13, v13, v115, v116\n" \
  "v_max3_u32 v13, v13, v117, v118\n" \
  "v_max3_u32 v13, v13, v119, v120\n" \
  "v_max3_u32 v13, v13, v121, v122\n" \
  "v_max3_u32 v13, v13, v123, v124\n" \
  "v_max3_u32 v13, v13, v125, v126\n" \
  "v_max_u32 v13, v13, v127\n" \
  "v_cmp_eq_u32 vcc, -1, v13\n" \
  "s_cbranch_vccz .Lch_p1_ok\n" \
  ".Lch_nan2:\n" \
  "buffer_load_dwordx4 v[96:99], v0, s[36:39], s32 offen offset:0 sc1\n" \
  "buffer_load_dwordx4 v[100:103], v0, s[36:39], s32 offen offset:1024 sc1\n" \
  "buffer_load_dwordx4 v[104:107], v0, s[36:39], s32 offen offset:2048 sc1\n" \
  "buffer_load_dwordx4 v[108:111], v0, s[36:39], s32 offen offset:3072 sc1\n" \
  "buffer_load_dwordx4 v[112:115], v0, s[36:39], s52 offen offset:0 sc1\n" \
  "buffer_load_dwordx4 v[116:119], v0, s[36:39], s52 offen offset:1024 sc1\n" \
  "buffer_load_dwordx4 v[120:123], v0, s[36:39], s52 offen offset:2048 sc1\n" \
  "buffer_load_dwordx4 v[124:127], v0, s[36:39], s52 offen offset:3072 sc1\n" \
  "s_sub_u32 s89, s89, 1\n" \
  "s_cbranch_scc0 .Lch_p1\n" \
  "s_mov_b32 s89, 0\n" \
  "s_branch .Lch_p1_ok\n" \
  ".Lch_w23:\n" \
  "buffer_load_dwordx4 v[112:115], v0, s[36:39], s60 offen offset:0 sc1\n" \
  "buffer_load_dwordx4 v[116:119], v0, s[36:39], s60 offen offset:1024 sc1\n" \
  "buffer_load_dwordx4 v[120:123], v0, s[36:39], s60 offen offset:2048 sc1\n" \
  "buffer_load_dwordx4 v[124:127], v0, s[36:39], s60 offen offset:3072 sc1\n" \
  "s_waitcnt lgkmcnt(0)\n" \
  "s_barrier\n" \
  "ds_read_b128 v[16:19], v7 offset:0\n" \
  "ds_read_b128 v[20:23], v7 offset:64\n" \
  "ds_read_b128 v[24:27], v7 offset:128\n" \
  "ds_read_b128 v[28:31], v7 offset:192\n" \
  "ds_read_b128 v[32:35], v0 offset:0\n" \
  "ds_read_b128 v[36:39], v0 offset:1024\n" \
  "ds_read_b128 v[40:43], v0 offset:2048\n" \
  "ds_read_b128 v[44:47], v0 offset:3072\n" \
  "ds_read_b128 v[48:51], v0 offset:4096\n" \
  "ds_read_b128 v[52:55], v0 offset:5120\n" \
  "ds_read_b128 v[56:59], v0 offset:6144\n" \
  "ds_read_b128 v[60:63], v0 offset:7168\n" \
  "ds_read_b128 v[64:67], v0 offset:8192\n" \
  "ds_read_b128 v[68:71], v0 offset:9216\n" \
  "ds_read_b128 v[72:75], v0 offset:10240\n" \
  "ds_read_b128 v[76:79], v0 offset:11264\n" \
  "ds_read_b128 v[80:83], v0 offset:12288\n" \
  "ds_read_b128 v[84:87], v0 offset:13312\n" \
  "ds_read_b128 v[88:91], v0 offset:14336\n" \
  "ds_read_b128 v[92:95], v0 offset:15360\n" \
  "ds_read_b128 v[96:99], v0 offset:16384\n" \
  "ds_read_b128 v[100:103], v0 offset:17408\n" \
  "ds_read_b128 v[104:107], v0 offset:18432\n" \
  "s_waitcnt vmcnt(8)\n" \
  "ds_read_b128 v[108:111], v0 offset:19456\n" \
  "s_lshl_b32 s92, s50, 2\n" \
  "s_add_u32 s92, s92, 30848\n" \
  "s_add_u32 s93, s84, 1\n" \
  "v_mov_b32 v14, s92\n" \
  "v_mov_b32 v15, s93\n" \
  "s_waitcnt lgkmcnt(0)\n" \
  "ds_write_b32 v14, v15 offset:24\n" \
  "s_waitcnt vmcnt(3)\n" \
  "v_mfma_f32_16x16x32_bf16 v[16:19], v[16:19], v[112:115], 0\n" \
  "s_waitcnt vmcnt(2)\n" \
  "v_mfma_f32_16x16x32_bf16 v[16:19], v[20:23], v[116:119], v[16:19]\n" \
  "s_waitcnt vmcnt(1)\n" \
  "v_mfma_f32_16x16x32_bf16 v[16:19], v[24:27], v[120:123], v[16:19]\n" \
  "s_waitcnt vmcnt(0)\n" \
  "v_mfma_f32_16x16x32_bf16 v[16:19], v[28:31], v[124:127], v[16:19]\n" \
  "v_mfma_f32_16x16x32_bf16 v[32:35], v[32:35], v[112:115], 0\n" \
  "v_mov_b32 v20, 0xd028965f\n" \
  "v_mfma_f32_16x16x32_bf16 v[32:35], v[36:39], v[116:119], v[32:35]\n" \
  "v_mfma_f32_16x16x32_bf16 v[32:35], v[40:43], v[120:123], v[32:35]\n" \
  "v_mfma_f32_16x16x32_bf16 v[32:35], v[44:47], v[124:127], v[32:35]\n" \
  "v_mfma_f32_16x16x32_bf16 v[48:51], v[48:51], v[112:115], 0\n" \
  "v_mfma_f32_16x16x32_bf16 v[48:51], v[52:55], v[116:119], v[48:51]\n" \
  "v_cndmask_b32_e64 v32, v20, v32, s[64:65]\n" \
  "v_cndmask_b32_e64 v33, v20, v33, s[66:67]\n" \
  "v_mfma_f32_16x16x32_bf16 v[48:51], v[56:59], v[120:123], v[48:51]\n" \
  "v_cndmask_b32_e64 v34, v20, v34, s[68:69]\n" \
  "v_cndmask_b32_e64 v35, v20, v35, s[70:71]\n" \
  "v_mfma_f32_16x16x32_bf16 v[48:51], v[60:63], v[124:127], v[48:51]\n" \
  "ds_read_b128 v[112:115], v11 offset:29696\n" \
  "ds_read_b64 v[116:117], v11 offset:30208\n" \
  "v_max3_f32 v18, v16, v17, v32\n" \
  "v_max3_f32 v18, v18, v33, v34\n" \
  "s_nop 3\n" \
  "v_cndmask_b32_e64 v48, v20, v48, s[72:73]\n" \
  "v_cndmask_b32_e64 v49, v20, v49, s[74:75]\n" \
  "v_cndmask_b32_e64 v50, v20, v50, s[76:77]\n" \
  "v_cndmask_b32_e64 v51, v20, v51, s[78:79]\n" \
  "v_max3_f32 v125, v18, v35, v48\n" \
  "v_max3_f32 v125, v125, v49, v50\n" \
  "v_max_f32 v125, v125, v51\n" \
  "v_mov_b32 v124, v125\n" \
  "s_nop 1\n" \
  "v_permlane16_swap_b32 v125, v124\n" \
  "v_max_f32 v125, v125, v124\n" \
  "v_mov_b32 v124, v125\n" \
  "s_nop 1\n" \
  "v_permlane32_swap_b32 v125, v124\n" \
  "v_max_f32 v125, v125, v124\n" \
  "v_mul_f32 v125, s91, v125\n" \
  "v_fma_f32 v32, v32, s91, -v125\n" \
  "v_fma_f32 v33, v33, s91, -v125\n" \
  "v_fma_f32 v34, v34, s91, -v125\n" \
  "v_fma_f32 v35, v35, s91, -v125\n" \
  "v_fma_f32 v48, v48, s91, -v125\n" \
  "v_fma_f32 v49, v49, s91, -v125\n" \
  "v_fma_f32 v50, v50, s91, -v125\n" \
  "v_fma_f32 v51, v51, s91, -v125\n" \
  "v_fma_f32 v16, v16, s91, -v125\n" \
  "v_fma_f32 v17, v17, s91, -v125\n" \
  "v_exp_f32 v32, v32\n" \
  "v_exp_f32 v33, v33\n" \
  "v_exp_f32 v34, v34\n" \
  "v_exp_f32 v35, v35\n" \
  "v_exp_f32 v48, v48\n" \
  "v_exp_f32 v49, v49\n" \
  "v_exp_f32 v50, v50\n" \
  "v_exp_f32 v51, v51\n" \
  "v_exp_f32 v16, v16\n" \
  "v_cvt_pk_bf16_f32 v20, v32, v33\n" \
  "v_cvt_pk_bf16_f32 v21, v34, v35\n" \
  "v_cvt_pk_bf16_f32 v22, v48, v49\n" \
  "v_cvt_pk_bf16_f32 v23, v50, v51\n" \
  "v_exp_f32 v17, v17\n" \
  "s_waitcnt lgkmcnt(1)\n" \
  "v_mfma_f32_16x16x32_bf16 v[120:123], v[112:115], v[20:23], 0\n" \
  "v_cvt_pk_bf16_f32 v14, v16, v17\n" \
  "v_cvt_pk_bf16_f32 v15, v16, v17\n" \
  "v_mfma_f32_16x16x32_bf16 v[32:35], v[64:67], v[20:23], 0\n" \
  "v_mfma_f32_16x16x32_bf16 v[36:39], v[68:71], v[20:23], 0\n" \
  "v_mfma_f32_16x16x32_bf16 v[40:43], v[72:75], v[20:23], 0\n" \
  "v_mfma_f32_16x16x32_bf16 v[44:47], v[76:79], v[20:23], 0\n" \
  "v_mfma_f32_16x16x32_bf16 v[48:51], v[80:83], v[20:23], 0\n" \
  "v_mfma_f32_16x16x32_bf16 v[52:55], v[84:87], v[20:23], 0\n" \
  "v_mfma_f32_16x16x32_bf16 v[56:59], v[88:91], v[20:23], 0\n" \
  "v_mfma_f32_16x16x32_bf16 v[60:63], v[92:95], v[20:23], 0\n" \
  "s_waitcnt lgkmcnt(0)\n" \
  "v_mfma_f32_16x16x16_bf16 v[120:123], v[116:117], v[14:15], v[120:123]\n" \
  "v_mfma_f32_16x16x16_bf16 v[32:35], v[96:97], v[14:15], v[32:35]\n" \
  "v_mfma_f32_16x16x16_bf16 v[48:51], v[98:99], v[14:15], v[48:51]\n" \
  "v_rcp_f32 v125, v120\n" \
  "v_mfma_f32_16x16x16_bf16 v[36:39], v[100:101], v[14:15], v[36:39]\n" \
  "v_mfma_f32_16x16x16_bf16 v[52:55], v[102:103], v[14:15], v[52:55]\n" \
  "v_mul_f32 v32, v32, v125\n" \
  "v_mul_f32 v48, v48, v125\n" \
  "v_mfma_f32_16x16x16_bf16 v[40:43], v[104:105], v[14:15], v[40:43]\n" \
  "v_mul_f32 v33, v33, v125\n" \
  "v_mul_f32 v49, v49, v125\n" \
  "v_mfma_f32_16x16x16_bf16 v[56:59], v[106:107], v[14:15], v[56:59]\n" \
  "v_mul_f32 v34, v34, v125\n" \
  "v_mul_f32 v50, v50, v125\n" \
  "v_mfma_f32_16x16x16_bf16 v[44:47], v[108:109], v[14:15], v[44:47]\n" \
  "v_mul_f32 v35, v35, v125\n" \
  "v_mul_f32 v51, v51, v125\n" \
  "v_mfma_f32_16x16x16_bf16 v[60:63], v[110:111], v[14:15], v[60:63]\n" \
  "ds_read_b128 v[16:19], v1 offset:0\n" \
  "ds_read_b128 v[20:23], v1 offset:1024\n" \
  "ds_read_b128 v[24:27], v1 offset:2048\n" \
  "ds_read_b128 v[28:31], v1 offset:3072\n" \
  "v_cvt_pk_bf16_f32 v64, v32, v48\n" \
  "v_cvt_pk_bf16_f32 v65, v33, v49\n" \
  "v_cvt_pk_bf16_f32 v66, v34, v50\n" \
  "v_cvt_pk_bf16_f32 v67, v35, v51\n" \
  "v_pk_mul_f32 v[36:37], v[36:37], v[124:125] op_sel:[0,1] op_sel_hi:[1,1]\n" \
  "v_pk_mul_f32 v[38:39], v[38:39], v[124:125] op_sel:[0,1] op_sel_hi:[1,1]\n" \
  "v_pk_mul_f32 v[52:53], v[52:53], v[124:125] op_sel:[0,1] op_sel_hi:[1,1]\n" \
  "v_pk_mul_f32 v[54:55], v[54:55], v[124:125] op_sel:[0,1] op_sel_hi:[1,1]\n" \
  "v_cvt_pk_bf16_f32 v68, v36, v52\n" \
  "v_cvt_pk_bf16_f32 v69, v37, v53\n" \
  "v_cvt_pk_bf16_f32 v70, v38, v54\n" \
  "v_cvt_pk_bf16_f32 v71, v39, v55\n" \
  "v_pk_mul_f32 v[40:41], v[40:41], v[124:125] op_sel:[0,1] op_sel_hi:[1,1]\n" \
  "v_pk_mul_f32 v[42:43], v[42:43], v[124:125] op_sel:[0,1] op_sel_hi:[1,1]\n" \
  "v_pk_mul_f32 v[56:57], v[56:57], v[124:125] op_sel:[0,1] op_sel_hi:[1,1]\n" \
  "v_pk_mul_f32 v[58:59], v[58:59], v[124:125] op_sel:[0,1] op_sel_hi:[1,1]\n" \
  "v_cvt_pk_bf16_f32 v72, v40, v56\n" \
  "v_cvt_pk_bf16_f32 v73, v41, v57\n" \
  "v_cvt_pk_bf16_f32 v74, v42, v58\n" \
  "v_cvt_pk_bf16_f32 v75, v43, v59\n" \
  "v_pk_mul_f32 v[44:45], v[44:45], v[124:125] op_sel:[0,1] op_sel_hi:[1,1]\n" \
  "v_pk_mul_f32 v[46:47], v[46:47], v[124:125] op_sel:[0,1] op_sel_hi:[1,1]\n" \
  "v_pk_mul_f32 v[60:61], v[60:61], v[124:125] op_sel:[0,1] op_sel_hi:[1,1]\n" \
  "v_pk_mul_f32 v[62:63], v[62:63], v[124:125] op_sel:[0,1] op_sel_hi:[1,1]\n" \
  "v_cvt_pk_bf16_f32 v76, v44, v60\n" \
  "v_cvt_pk_bf16_f32 v77, v45, v61\n" \
  "v_cvt_pk_bf16_f32 v78, v46, v62\n" \
  "v_cvt_pk_bf16_f32 v79, v47, v63\n" \
  "ds_read_b128 v[32:35], v1 offset:16384\n" \
  "ds_read_b128 v[36:39], v1 offset:17408\n" \
  "ds_read_b128 v[40:43], v1 offset:18432\n" \
  "ds_read_b128 v[44:47], v1 offset:19456\n" \
  "ds_read_b128 v[48:51], v1 offset:32768\n" \
  "ds_read_b128 v[52:55], v1 offset:33792\n" \
  "ds_read_b128 v[56:59], v1 offset:34816\n" \
  "ds_read_b128 v[60:63], v1 offset:35840\n" \
  "v_mov_b32 v15, s84\n" \
  "s_mov_b32 s26, s57\n" \
  "v_mov_b64 v[112:113], 0\n" \
  "v_mov_b64 v[114:115], 0\n" \
  "v_mov_b64 v[116:117], 0\n" \
  "v_mov_b64 v[118:119], 0\n" \
  "v_mov_b64 v[120:121], 0\n" \
  "v_mov_b64 v[122:123], 0\n" \
  "v_mov_b64 v[124:125], 0\n" \
  "v_mov_b64 v[126:127], 0\n" \
  "s_mov_b32 s32, s23\n" \
  "s_add_u32 s52, s32, 32768\n" \
  "s_add_u32 s84, s84, 1\n" \
  "s_xor_b32 s85, s85, 0x10000\n" \
  "s_xor_b32 s23, s23, 540672\n" \
  "s_xor_b32 s87, s87, 540672\n" \
  "s_xor_b32 s88, s88, 540672\n" \
  "s_add_u32 s60, s60, 2097152\n" \
  "s_xor_b32 s60, s60, 8192\n" \
  "s_add_u32 s57, s57, 2097152\n" \
  "s_xor_b32 s57, s57, 8192\n" \
  "s_cmp_le_u32 s84, s48\n" \
  "s_cbranch_scc1 .Lch_b0\n" \
  "buffer_store_dwordx4 v[64:67], v0, s[36:39], s26 offen offset:0 sc1\n" \
  "buffer_store_dwordx4 v[68:71], v0, s[36:39], s26 offen offset:1024 sc1\n" \
  "buffer_store_dwordx4 v[72:75], v0, s[36:39], s26 offen offset:2048 sc1\n" \
  "buffer_store_dwordx4 v[76:79], v0, s[36:39], s26 offen offset:3072 sc1\n" \
  "s_mov_b32 s26, s28\n" \
  "s_waitcnt vmcnt(0)\n" \
  "buffer_store_dword v15, v12, s[36:39], s59 offen sc1\n" \
  "s_cmp_lg_u32 s84, 64\n" \
  "s_cbranch_scc1 .Lch_b0\n" \
  ".Lch_end:\n" \
  "s_waitcnt vmcnt(0) lgkmcnt(0)\n" \
  "s_endpgm\n"
typedef unsigned u16v __attribute__((ext_vector_type(16)));
constexpr int L_KX = 0, L_VX = 8192, L_MS = 16384, L_OI = L_MS + 4096, L_VM = L_MS, L_QI = 24576, L_KI = 26880;
constexpr int L_ZERO = 29184, L_ONE = 29696, L_HALF = 30208, L_SEL = 30720, L_CNT = 30848, L_REF = 31232, L_WX = 32768;
#define SSH 0
constexpr int LDS_BYTES = 163840;
constexpr unsigned OOB = 0x40000000u;
constexpr int CAL2_OFF = ARR_OFF + 32768 + 0x5000;
constexpr int DBG_OFF = INIT_BYTES + 4096;
#define LDSP(T, off) ((__attribute__((address_space(3))) T*)(uintptr_t)(unsigned)(off))
struct Params {
  const void* x; const void* mem0; const void* wq; const void* wk; const void* wv; const void* wo;
  const void* wg; const void* wu; const void* wd; void* out; unsigned char* ws;
  int d1; int d2; int only_layer; int dbg; int lead; int lag; int pairs; int pad;
};
DEVI unsigned lo32(const void* p) { return (unsigned)(uintptr_t)p; }
DEVI unsigned hi32(const void* p) { return (unsigned)((uintptr_t)p >> 32); }

DEVI void chain_role(const Params& p, const int layer, const int h, const int rep) {
  const int tid = threadIdx.x, l = tid & 63, w = __builtin_amdgcn_readfirstlane(tid >> 6);
  const int c = l & 15, g = l >> 4, nb = (c >> 1) & 1, r = 2 * (c >> 2) + (c & 1);
  if (tid < 128) { *LDSP(unsigned, L_ZERO + 4 * tid) = 0; *LDSP(unsigned, L_ONE + 4 * tid) = 0x3f803f80u; *LDSP(unsigned, L_HALF + 4 * tid) = 0x3f003f00u; *LDSP(unsigned, L_REF + 4 * tid) = 0; }
  if (tid < 32) *LDSP(unsigned, L_SEL + 4 * tid) = (((tid >> 2) & 2) + (tid >> 4) == (tid & 3)) ? (0x3f80u << (((tid >> 2) & 1) * 16)) : 0u;
  if (tid < 16) *LDSP(unsigned, L_CNT + 4 * tid) = 0;
  u16v pv; u8v qv;
  pv[0] = l * 16; pv[1] = L_WX + w * 4096 + l * 16; pv[2] = pv[1] + 65536; pv[3] = l * 16 + w * 1024;
  pv[4] = L_QI + 2 * g * 544 + 64 * w + 4 * c;
  pv[5] = c < 8 ? L_QI + c * 544 + 16 * g : L_ZERO; pv[6] = c >= 8 ? L_QI + (c & 7) * 544 + 16 * g : L_ZERO;
  pv[7] = L_QI + 256 + r * 544 + 16 * g;
  pv[8] = (2 * (g >> 1) + (c >> 3)) * 0x20000 + (8 * (g & 1) + (c & 7)) * 16;
  pv[9] = (nb ? 0xEEEEu : 0x4444u) | (c >> 3 ? 0xEEEE0000u : 0x44440000u);
  pv[10] = L_OI + w * 512 + g * 128 + (c & 7) * 16; pv[11] = g * 128 + (c & 7) * 16;
  pv[12] = w == 0 ? (REP_OFF ^ topo(4 * layer + 2 * (g & 1) + (c >> 3))) + (8 * (g >> 1) + (c & 7)) * 16 : l < (w == 1 ? 3 : 1) ? l * FS : OOB;
  pv[13] = 0; pv[14] = 0; pv[15] = 0;
  const int u = (c >> 2) & 1, b = c >> 3, cq = c & 3, r2 = u + 2 * (g & 1);
  qv[0] = (16 * w + 4 * (g >> 1) + (g & 1)) * 8192 + h * 256 + (c ^ ((g + 4 * (w >> 1)) << 1)) * 16;
  qv[1] = (h * 128 + 16 * (c >> 3) + 2 * g) * 1024 + w * 256 + ((c & 7) ^ ((g & 1) << 2)) * 16;
  qv[2] = L_WX + b * 8192 + (g >> 1) * 2048 + r2 * 256 + ((2 * w) ^ (r2 + 4 * b)) * 32 + 8 * cq;
  qv[3] = L_WX + w * 4096 + g * 256 + u * 128 + (g & 1) * 64 + b * 32 + 8 * cq;
  qv[4] = r * 1024 + (32 * nb + 8 * g) * 2;
  const unsigned sts = FLAG_OFF + 5 * BLK + 4096 + layer * 8192;
  qv[5] = w == 0 ? sts : OOB; qv[6] = w == 1 ? sts + (h * 16 << SSH) : OOB; qv[7] = h;
  u16v sa, sm; u8v sb; u4 sc;
  sa[0] = lo32(p.ws); sa[1] = hi32(p.ws); sa[2] = END_OFF; sa[3] = 0x20000;
  sa[4] = lo32(p.wq); sa[5] = hi32(p.wq); sa[6] = lo32(p.wk); sa[7] = hi32(p.wk); sa[8] = lo32(p.wv); sa[9] = hi32(p.wv);
  sa[10] = lo32(p.wo); sa[11] = hi32(p.wo); sa[12] = lo32(p.mem0); sa[13] = hi32(p.mem0); sa[14] = w; sa[15] = (h >> 4) * 32768 + (h & 15) * 256 + topo(4 * layer + w);
  const unsigned qkv0 = item_q(layer * 64, 0) + h * BLK;
  sb[0] = (h & 3) * 0x20000 + (h & 4) * 0x4000 + topo(4 * layer + (h >> 3)); sb[1] = REP_OFF ^ topo(4 * layer + rep); sb[2] = layer && p.d1 >= 1000 ? p.d1 / 1000 : p.d1 % 1000; sb[3] = p.d2 % 100 | p.d2 / 100 << 8; sb[4] = qkv0 + 32 * BLK;
  sb[5] = item_o(layer * 64, 0) + h * BLK + (w & 1) * 4096;
  sb[6] = w == 1 ? flg_qkv(layer, h, 0) : OOB; sb[7] = w >= 2 ? flg(F_O, layer, 0) + h * FS + (w & 1) * OFS : OOB;
  sc[0] = w >= 2 ? qkv0 + (w & 1) * 4096 : OOB; sc[1] = CTL_OFF; sc[2] = DBG_OFF + ((layer * 32 + h) * 4 + w) * 128; sc[3] = (p.lag & 0xffff) | (63 - (p.lag >> (16 + 8 * layer) & 63)) << 16;
  const int tk = 16 * (w & 1) + c - 4 * g;
#pragma unroll
  for (int i = 0; i < 8; ++i) {
    const unsigned long long m = __builtin_amdgcn_ballot_w64((i & 3) + 16 * (i >> 2) <= tk);
    sm[2 * i] = (unsigned)m; sm[2 * i + 1] = (unsigned)(m >> 32);
  }
  asm volatile(CHAIN_ASM : : "{v[0:15]}"(pv), "{v[16:23]}"(qv), "{s[36:51]}"(sa), "{s[52:59]}"(sb), "{s[60:63]}"(sc), "{s[64:79]}"(sm)
               : "memory", "v255", "a255", "s101");
  __builtin_unreachable();
}

#define ASM_QK \
  "s_nop 4\n" \
  "s_mov_b32 s58, 0\n" \
  "s_mov_b32 s59, 0\n" \
  "s_mov_b32 s70, 100000\n" \
  "s_mov_b32 s69, 0x40000000\n" \
  "s_mov_b32 s67, 0x40000000\n" \
  "s_mov_b32 s68, 0x40000000\n" \
  "v_mov_b32 v60, 0\n" \
  "v_mov_b32 v61, 0\n" \
  "v_cmp_eq_u32 vcc, v15, v28\n" \
  "s_mov_b32 s29, vcc_lo\n" \
  "s_mov_b32 s73, 0x3f803f80\n" \
  "v_mov_b32 v234, 0x3727c5ac\n" \
  "s_mov_b32 s35, 0x20000\n" \
  "v_mov_b32 v236, s73\n" \
  "v_mov_b32 v237, s73\n" \
  "v_mov_b32 v238, s73\n" \
  "v_mov_b32 v239, s73\n" \
  "ds_read_b64_tr_b16 a[0:1], v20 offset:0\n" \
  "ds_read_b64_tr_b16 a[2:3], v21 offset:0\n" \
  "ds_read_b64_tr_b16 a[4:5], v22 offset:0\n" \
  "ds_read_b64_tr_b16 a[6:7], v23 offset:0\n" \
  "ds_read_b64_tr_b16 a[8:9], v24 offset:0\n" \
  "ds_read_b64_tr_b16 a[10:11], v25 offset:0\n" \
  "ds_read_b64_tr_b16 a[12:13], v26 offset:0\n" \
  "ds_read_b64_tr_b16 a[14:15], v27 offset:0\n" \
  "ds_read_b64_tr_b16 a[16:17], v20 offset:8192\n" \
  "ds_read_b64_tr_b16 a[18:19], v21 offset:8192\n" \
  "ds_read_b64_tr_b16 a[20:21], v22 offset:8192\n" \
  "ds_read_b64_tr_b16 a[22:23], v23 offset:8192\n" \
  "ds_read_b64_tr_b16 a[24:25], v24 offset:8192\n" \
  "ds_read_b64_tr_b16 a[26:27], v25 offset:8192\n" \
  "s_waitcnt lgkmcnt(12)\n" \
  "v_mfma_f32_16x16x32_bf16 v[192:195], a[0:3], v[236:239], 0\n" \
  "ds_read_b64_tr_b16 a[28:29], v26 offset:8192\n" \
  "ds_read_b64_tr_b16 a[30:31], v27 offset:8192\n" \
  "s_waitcnt lgkmcnt(12)\n" \
  "v_mfma_f32_16x16x32_bf16 v[196:199], a[4:7], v[236:239], 0\n" \
  "ds_read_b64_tr_b16 a[32:33], v20 offset:16384\n" \
  "ds_read_b64_tr_b16 a[34:35], v21 offset:16384\n" \
  "s_waitcnt lgkmcnt(12)\n" \
  "v_mfma_f32_16x16x32_bf16 v[200:203], a[8:11], v[236:239], 0\n" \
  "ds_read_b64_tr_b16 a[36:37], v22 offset:16384\n" \
  "ds_read_b64_tr_b16 a[38:39], v23 offset:16384\n" \
  "s_waitcnt lgkmcnt(12)\n" \
  "v_mfma_f32_16x16x32_bf16 v[204:207], a[12:15], v[236:239], 0\n" \
  "ds_read_b64_tr_b16 a[40:41], v24 offset:16384\n" \
  "ds_read_b64_tr_b16 a[42:43], v25 offset:16384\n" \
  "s_waitcnt lgkmcnt(12)\n" \
  "v_mfma_f32_16x16x32_bf16 v[192:195], a[16:19], v[236:239], v[192:195]\n" \
  "ds_read_b64_tr_b16 a[44:45], v26 offset:16384\n" \
  "ds_read_b64_tr_b16 a[46:47], v27 offset:16384\n" \
  "s_waitcnt lgkmcnt(12)\n" \
  "v_mfma_f32_16x16x32_bf16 v[196:199], a[20:23], v[236:239], v[196:199]\n" \
  "ds_read_b64_tr_b16 a[48:49], v20 offset:24576\n" \
  "ds_read_b64_tr_b16 a[50:51], v21 offset:24576\n" \
  "s_waitcnt lgkmcnt(12)\n" \
  "v_mfma_f32_16x16x32_bf16 v[200:203], a[24:27], v[236:239], v[200:203]\n" \
  "ds_read_b64_tr_b16 a[52:53], v22 offset:24576\n" \
  "ds_read_b64_tr_b16 a[54:55], v23 offset:24576\n" \
  "s_waitcnt lgkmcnt(12)\n" \
  "v_mfma_f32_16x16x32_bf16 v[204:207], a[28:31], v[236:239], v[204:207]\n" \
  "ds_read_b64_tr_b16 a[56:57], v24 offset:24576\n" \
  "ds_read_b64_tr_b16 a[58:59], v25 offset:24576\n" \
  "s_waitcnt lgkmcnt(12)\n" \
  "v_mfma_f32_16x16x32_bf16 v[192:195], a[32:35], v[236:239], v[192:195]\n" \
  "ds_read_b64_tr_b16 a[60:61], v26 offset:24576\n" \
  "ds_read_b64_tr_b16 a[62:63], v27 offset:24576\n" \
  "s_waitcnt lgkmcnt(12)\n" \
  "v_mfma_f32_16x16x32_bf16 v[196:199], a[36:39], v[236:239], v[196:199]\n" \
  "ds_read_b64_tr_b16 a[64:65], v20 offset:32768\n" \
  "ds_read_b64_tr_b16 a[66:67], v21 offset:32768\n" \
  "s_waitcnt lgkmcnt(12)\n" \
  "v_mfma_f32_16x16x32_bf16 v[200:203], a[40:43], v[236:239], v[200:203]\n" \
  "ds_read_b64_tr_b16 a[68:69], v22 offset:32768\n" \
  "ds_read_b64_tr_b16 a[70:71], v23 offset:32768\n" \
  "s_waitcnt lgkmcnt(12)\n" \
  "v_mfma_f32_16x16x32_bf16 v[204:207], a[44:47], v[236:239], v[204:207]\n" \
  "ds_read_b64_tr_b16 a[72:73], v24 offset:32768\n" \
  "ds_read_b64_tr_b16 a[74:75], v25 offset:32768\n" \
  "s_waitcnt lgkmcnt(12)\n" \
  "v_mfma_f32_16x16x32_bf16 v[192:195], a[48:51], v[236:239], v[192:195]\n" \
  "ds_read_b64_tr_b16 a[76:77], v26 offset:32768\n" \
  "ds_read_b64_tr_b16 a[78:79], v27 offset:32768\n" \
  "s_waitcnt lgkmcnt(12)\n" \
  "v_mfma_f32_16x16x32_bf16 v[196:199], a[52:55], v[236:239], v[196:199]\n" \
  "ds_read_b64_tr_b16 a[80:81], v20 offset:40960\n" \
  "ds_read_b64_tr_b16 a[82:83], v21 offset:40960\n" \
  "s_waitcnt lgkmcnt(12)\n" \
  "v_mfma_f32_16x16x32_bf16 v[200:203], a[56:59], v[236:239], v[200:203]\n" \
  "ds_read_b64_tr_b16 a[84:85], v22 offset:40960\n" \
  "ds_read_b64_tr_b16 a[86:87], v23 offset:40960\n" \
  "s_waitcnt lgkmcnt(12)\n" \
  "v_mfma_f32_16x16x32_bf16 v[204:207], a[60:63], v[236:239], v[204:207]\n" \
  "ds_read_b64_tr_b16 a[88:89], v24 offset:40960\n" \
  "ds_read_b64_tr_b16 a[90:91], v25 offset:40960\n" \
  "s_waitcnt lgkmcnt(12)\n" \
  "v_mfma_f32_16x16x32_bf16 v[192:195], a[64:67], v[236:239], v[192:195]\n" \
  "ds_read_b64_tr_b16 a[92:93], v26 offset:40960\n" \
  "ds_read_b64_tr_b16 a[94:95], v27 offset:40960\n" \
  "s_waitcnt lgkmcnt(12)\n" \
  "v_mfma_f32_16x16x32_bf16 v[196:199], a[68:71], v[236:239], v[196:199]\n" \
  "ds_read_b64_tr_b16 a[96:97], v20 offset:49152\n" \
  "ds_read_b64_tr_b16 a[98:99], v21 offset:49152\n" \
  "s_waitcnt lgkmcnt(12)\n" \
  "v_mfma_f32_16x16x32_bf16 v[200:203], a[72:75], v[236:239], v[200:203]\n" \
  "ds_read_b64_tr_b16 a[100:101], v22 offset:49152\n" \
  "ds_read_b64_tr_b16 a[102:103], v23 offset:49152\n" \
  "s_waitcnt lgkmcnt(12)\n" \
  "v_mfma_f32_16x16x32_bf16 v[204:207], a[76:79], v[236:239], v[204:207]\n" \
  "ds_read_b64_tr_b16 a[104:105], v24 offset:49152\n" \
  "ds_read_b64_tr_b16 a[106:107], v25 offset:49152\n" \
  "s_waitcnt lgkmcnt(12)\n" \
  "v_mfma_f32_16x16x32_bf16 v[192:195], a[80:83], v[236:239], v[192:195]\n" \
  "ds_read_b64_tr_b16 a[108:109], v26 offset:49152\n" \
  "ds_read_b64_tr_b16 a[110:111], v27 offset:49152\n" \
  "s_waitcnt lgkmcnt(12)\n" \
  "v_mfma_f32_16x16x32_bf16 v[196:199], a[84:87], v[236:239], v[196:199]\n" \
  "ds_read_b64_tr_b16 a[112:113], v20 offset:57344\n" \
  "ds_read_b64_tr_b16 a[114:115], v21 offset:57344\n" \
  "s_waitcnt lgkmcnt(12)\n" \
  "v_mfma_f32_16x16x32_bf16 v[200:203], a[88:91], v[236:239], v[200:203]\n" \
  "ds_read_b64_tr_b16 a[116:117], v22 offset:57344\n" \
  "ds_read_b64_tr_b16 a[118:119], v23 offset:57344\n" \
  "s_waitcnt lgkmcnt(12)\n" \
  "v_mfma_f32_16x16x32_bf16 v[204:207], a[92:95], v[236:239], v[204:207]\n" \
  "ds_read_b64_tr_b16 a[120:121], v24 offset:57344\n" \
  "ds_read_b64_tr_b16 a[122:123], v25 offset:57344\n" \
  "s_waitcnt lgkmcnt(12)\n" \
  "v_mfma_f32_16x16x32_bf16 v[192:195], a[96:99], v[236:239], v[192:195]\n" \
  "ds_read_b64_tr_b16 a[124:125], v26 offset:57344\n" \
  "ds_read_b64_tr_b16 a[126:127], v27 offset:57344\n" \
  "s_waitcnt lgkmcnt(12)\n" \
  "v_mfma_f32_16x16x32_bf16 v[196:199], a[100:103], v[236:239], v[196:199]\n" \
  "v_xor_b32 v20, 0x10000, v20\n" \
  "v_xor_b32 v21, 0x10000, v21\n" \
  "v_xor_b32 v22, 0x10000, v22\n" \
  "v_xor_b32 v23, 0x10000, v23\n" \
  "v_xor_b32 v24, 0x10000, v24\n" \
  "v_xor_b32 v25, 0x10000, v25\n" \
  "v_xor_b32 v26, 0x10000, v26\n" \
  "v_xor_b32 v27, 0x10000, v27\n" \
  "ds_read_b64_tr_b16 a[128:129], v20 offset:0\n" \
  "ds_read_b64_tr_b16 a[130:131], v21 offset:0\n" \
  "s_waitcnt lgkmcnt(12)\n" \
  "v_mfma_f32_16x16x32_bf16 v[200:203], a[104:107], v[236:239], v[200:203]\n" \
  "ds_read_b64_tr_b16 a[132:133], v22 offset:0\n" \
  "ds_read_b64_tr_b16 a[134:135], v23 offset:0\n" \
  "s_waitcnt lgkmcnt(12)\n" \
  "v_mfma_f32_16x16x32_bf16 v[204:207], a[108:111], v[236:239], v[204:207]\n" \
  "ds_read_b64_tr_b16 a[136:137], v24 offset:0\n" \
  "ds_read_b64_tr_b16 a[138:139], v25 offset:0\n" \
  "s_waitcnt lgkmcnt(12)\n" \
  "v_mfma_f32_16x16x32_bf16 v[192:195], a[112:115], v[236:239], v[192:195]\n" \
  "ds_read_b64_tr_b16 a[140:141], v26 offset:0\n" \
  "ds_read_b64_tr_b16 a[142:143], v27 offset:0\n" \
  "s_waitcnt lgkmcnt(12)\n" \
  "v_mfma_f32_16x16x32_bf16 v[196:199], a[116:119], v[236:239], v[196:199]\n" \
  "ds_read_b64_tr_b16 a[144:145], v20 offset:8192\n" \
  "ds_read_b64_tr_b16 a[146:147], v21 offset:8192\n" \
  "s_waitcnt lgkmcnt(12)\n" \
  "v_mfma_f32_16x16x32_bf16 v[200:203], a[120:123], v[236:239], v[200:203]\n" \
  "ds_read_b64_tr_b16 a[148:149], v22 offset:8192\n" \
  "ds_read_b64_tr_b16 a[150:151], v23 offset:8192\n" \
  "s_waitcnt lgkmcnt(12)\n" \
  "v_mfma_f32_16x16x32_bf16 v[204:207], a[124:127], v[236:239], v[204:207]\n" \
  "ds_read_b64_tr_b16 a[152:153], v24 offset:8192\n" \
  "ds_read_b64_tr_b16 a[154:155], v25 offset:8192\n" \
  "s_waitcnt lgkmcnt(12)\n" \
  "v_mfma_f32_16x16x32_bf16 v[192:195], a[128:131], v[236:239], v[192:195]\n" \
  "ds_read_b64_tr_b16 a[156:157], v26 offset:8192\n" \
  "ds_read_b64_tr_b16 a[158:159], v27 offset:8192\n" \
  "s_waitcnt lgkmcnt(12)\n" \
  "v_mfma_f32_16x16x32_bf16 v[196:199], a[132:135], v[236:239], v[196:199]\n" \
  "ds_read_b64_tr_b16 a[160:161], v20 offset:16384\n" \
  "ds_read_b64_tr_b16 a[162:163], v21 offset:16384\n" \
  "s_waitcnt lgkmcnt(12)\n" \
  "v_mfma_f32_16x16x32_bf16 v[200:203], a[136:139], v[236:239], v[200:203]\n" \
  "ds_read_b64_tr_b16 a[164:165], v22 offset:16384\n" \
  "ds_read_b64_tr_b16 a[166:167], v23 offset:16384\n" \
  "s_waitcnt lgkmcnt(12)\n" \
  "v_mfma_f32_16x16x32_bf16 v[204:207], a[140:143], v[236:239], v[204:207]\n" \
  "ds_read_b64_tr_b16 a[168:169], v24 offset:16384\n" \
  "ds_read_b64_tr_b16 a[170:171], v25 offset:16384\n" \
  "s_waitcnt lgkmcnt(12)\n" \
  "v_mfma_f32_16x16x32_bf16 v[192:195], a[144:147], v[236:239], v[192:195]\n" \
  "ds_read_b64_tr_b16 a[172:173], v26 offset:16384\n" \
  "ds_read_b64_tr_b16 a[174:175], v27 offset:16384\n" \
  "s_waitcnt lgkmcnt(12)\n" \
  "v_mfma_f32_16x16x32_bf16 v[196:199], a[148:151], v[236:239], v[196:199]\n" \
  "ds_read_b64_tr_b16 a[176:177], v20 offset:24576\n" \
  "ds_read_b64_tr_b16 a[178:179], v21 offset:24576\n" \
  "s_waitcnt lgkmcnt(12)\n" \
  "v_mfma_f32_16x16x32_bf16 v[200:203], a[152:155], v[236:239], v[200:203]\n" \
  "ds_read_b64_tr_b16 a[180:181], v22 offset:24576\n" \
  "ds_read_b64_tr_b16 a[182:183], v23 offset:24576\n" \
  "s_waitcnt lgkmcnt(12)\n" \
  "v_mfma_f32_16x16x32_bf16 v[204:207], a[156:159], v[236:239], v[204:207]\n" \
  "ds_read_b64_tr_b16 a[184:185], v24 offset:24576\n" \
  "ds_read_b64_tr_b16 a[186:187], v25 offset:24576\n" \
  "s_waitcnt lgkmcnt(12)\n" \
  "v_mfma_f32_16x16x32_bf16 v[192:195], a[160:163], v[236:239], v[192:195]\n" \
  "ds_read_b64_tr_b16 a[188:189], v26 offset:24576\n" \
  "ds_read_b64_tr_b16 a[190:191], v27 offset:24576\n" \
  "s_waitcnt lgkmcnt(12)\n" \
  "v_mfma_f32_16x16x32_bf16 v[196:199], a[164:167], v[236:239], v[196:199]\n" \
  "ds_read_b64_tr_b16 a[192:193], v20 offset:32768\n" \
  "ds_read_b64_tr_b16 a[194:195], v21 offset:32768\n" \
  "s_waitcnt lgkmcnt(12)\n" \
  "v_mfma_f32_16x16x32_bf16 v[200:203], a[168:171], v[236:239], v[200:203]\n" \
  "ds_read_b64_tr_b16 a[196:197], v22 offset:32768\n" \
  "ds_read_b64_tr_b16 a[198:199], v23 offset:32768\n" \
  "s_waitcnt lgkmcnt(12)\n" \
  "v_mfma_f32_16x16x32_bf16 v[204:207], a[172:175], v[236:239], v[204:207]\n" \
  "ds_read_b64_tr_b16 a[200:201], v24 offset:32768\n" \
  "ds_read_b64_tr_b16 a[202:203], v25 offset:32768\n" \
  "s_waitcnt lgkmcnt(12)\n" \
  "v_mfma_f32_16x16x32_bf16 v[192:195], a[176:179], v[236:239], v[192:195]\n" \
  "ds_read_b64_tr_b16 a[204:205], v26 offset:32768\n" \
  "ds_read_b64_tr_b16 a[206:207], v27 offset:32768\n" \
  "s_waitcnt lgkmcnt(12)\n" \
  "v_mfma_f32_16x16x32_bf16 v[196:199], a[180:183], v[236:239], v[196:199]\n" \
  "ds_read_b64_tr_b16 a[208:209], v20 offset:40960\n" \
  "ds_read_b64_tr_b16 a[210:211], v21 offset:40960\n" \
  "s_waitcnt lgkmcnt(12)\n" \
  "v_mfma_f32_16x16x32_bf16 v[200:203], a[184:187], v[236:239], v[200:203]\n" \
  "ds_read_b64_tr_b16 a[212:213], v22 offset:40960\n" \
  "ds_read_b64_tr_b16 a[214:215], v23 offset:40960\n" \
  "s_waitcnt lgkmcnt(12)\n" \
  "v_mfma_f32_16x16x32_bf16 v[204:207], a[188:191], v[236:239], v[204:207]\n" \
  "ds_read_b64_tr_b16 a[216:217], v24 offset:40960\n" \
  "ds_read_b64_tr_b16 a[218:219], v25 offset:40960\n" \
  "s_waitcnt lgkmcnt(12)\n" \
  "v_mfma_f32_16x16x32_bf16 v[192:195], a[192:195], v[236:239], v[192:195]\n" \
  "ds_read_b64_tr_b16 a[220:221], v26 offset:40960\n" \
  "ds_read_b64_tr_b16 a[222:223], v27 offset:40960\n" \
  "s_waitcnt lgkmcnt(12)\n" \
  "v_mfma_f32_16x16x32_bf16 v[196:199], a[196:199], v[236:239], v[196:199]\n" \
  "ds_read_b64_tr_b16 a[224:225], v20 offset:49152\n" \
  "ds_read_b64_tr_b16 a[226:227], v21 offset:49152\n" \
  "s_waitcnt lgkmcnt(12)\n" \
  "v_mfma_f32_16x16x32_bf16 v[200:203], a[200:203], v[236:239], v[200:203]\n" \
  "ds_read_b64_tr_b16 a[228:229], v22 offset:49152\n" \
  "ds_read_b64_tr_b16 a[230:231], v23 offset:49152\n" \
  "s_waitcnt lgkmcnt(12)\n" \
  "v_mfma_f32_16x16x32_bf16 v[204:207], a[204:207], v[236:239], v[204:207]\n" \
  "ds_read_b64_tr_b16 a[232:233], v24 offset:49152\n" \
  "ds_read_b64_tr_b16 a[234:235], v25 offset:49152\n" \
  "s_waitcnt lgkmcnt(12)\n" \
  "v_mfma_f32_16x16x32_bf16 v[192:195], a[208:211], v[236:239], v[192:195]\n" \
  "ds_read_b64_tr_b16 a[236:237], v26 offset:49152\n" \
  "ds_read_b64_tr_b16 a[238:239], v27 offset:49152\n" \
  "s_waitcnt lgkmcnt(12)\n" \
  "v_mfma_f32_16x16x32_bf16 v[196:199], a[212:215], v[236:239], v[196:199]\n" \
  "ds_read_b64_tr_b16 a[240:241], v20 offset:57344\n" \
  "ds_read_b64_tr_b16 a[242:243], v21 offset:57344\n" \
  "s_waitcnt lgkmcnt(12)\n" \
  "v_mfma_f32_16x16x32_bf16 v[200:203], a[216:219], v[236:239], v[200:203]\n" \
  "ds_read_b64_tr_b16 a[244:245], v22 offset:57344\n" \
  "ds_read_b64_tr_b16 a[246:247], v23 offset:57344\n" \
  "s_waitcnt lgkmcnt(12)\n" \
  "v_mfma_f32_16x16x32_bf16 v[204:207], a[220:223], v[236:239], v[204:207]\n" \
  "ds_read_b64_tr_b16 a[248:249], v24 offset:57344\n" \
  "ds_read_b64_tr_b16 a[250:251], v25 offset:57344\n" \
  "s_waitcnt lgkmcnt(12)\n" \
  "v_mfma_f32_16x16x32_bf16 v[192:195], a[224:227], v[236:239], v[192:195]\n" \
  "ds_read_b64_tr_b16 a[252:253], v26 offset:57344\n" \
  "ds_read_b64_tr_b16 a[254:255], v27 offset:57344\n" \
  "s_waitcnt lgkmcnt(12)\n" \
  "v_mfma_f32_16x16x32_bf16 v[196:199], a[228:231], v[236:239], v[196:199]\n" \
  "s_waitcnt lgkmcnt(10)\n" \
  "v_mfma_f32_16x16x32_bf16 v[200:203], a[232:235], v[236:239], v[200:203]\n" \
  "s_waitcnt lgkmcnt(8)\n" \
  "v_mfma_f32_16x16x32_bf16 v[204:207], a[236:239], v[236:239], v[204:207]\n" \
  "s_waitcnt lgkmcnt(6)\n" \
  "v_mfma_f32_16x16x32_bf16 v[192:195], a[240:243], v[236:239], v[192:195]\n" \
  "s_waitcnt lgkmcnt(4)\n" \
  "v_mfma_f32_16x16x32_bf16 v[196:199], a[244:247], v[236:239], v[196:199]\n" \
  "s_waitcnt lgkmcnt(2)\n" \
  "v_mfma_f32_16x16x32_bf16 v[200:203], a[248:251], v[236:239], v[200:203]\n" \
  "s_waitcnt lgkmcnt(0)\n" \
  "v_mfma_f32_16x16x32_bf16 v[204:207], a[252:255], v[236:239], v[204:207]\n" \
  "s_waitcnt vmcnt(0) lgkmcnt(0)\n" \
  "s_branch .Lqk_sel\n" \
  ".Lqk_q:\n" \
  "ds_read_b128 v[64:67], v8 offset:32768\n" \
  "ds_read_b128 v[68:71], v9 offset:32768\n" \
  "ds_read_b128 v[72:75], v10 offset:32768\n" \
  "ds_read_b128 v[76:79], v11 offset:32768\n" \
  "ds_read_b128 v[80:83], v8 offset:33024\n" \
  "ds_read_b128 v[84:87], v9 offset:33024\n" \
  "ds_read_b128 v[88:91], v10 offset:33024\n" \
  "ds_read_b128 v[92:95], v11 offset:33024\n" \
  "ds_read_b128 v[96:99], v8 offset:33280\n" \
  "ds_read_b128 v[100:103], v9 offset:33280\n" \
  "ds_read_b128 v[104:107], v10 offset:33280\n" \
  "ds_read_b128 v[108:111], v11 offset:33280\n" \
  "ds_read_b128 v[112:115], v8 offset:33536\n" \
  "ds_read_b128 v[116:119], v9 offset:33536\n" \
  "ds_read_b128 v[120:123], v10 offset:33536\n" \
  "ds_read_b128 v[124:127], v11 offset:33536\n" \
  "s_mov_b32 m0, s40\n" \
  "s_branch .Lqk_pq_c\n" \
  ".Lqk_pq:\n" \
  ".Lqk_pq_f:\n" \
  "buffer_load_dword v62, v30, s[36:39], 0 offen sc1\n" \
  "s_sub_u32 s70, s70, 1\n" \
  "s_cselect_b32 s77, -1, s77\n" \
  "s_cselect_b32 s70, 0, s70\n" \
  "s_waitcnt vmcnt(0)\n" \
  ".Lqk_pq_c:\n" \
  "v_cmp_gt_i32 vcc, s77, v62\n" \
  "s_and_b32 vcc_lo, vcc_lo, s29\n" \
  "s_cbranch_scc1 .Lqk_pq\n" \
  "s_barrier\n" \
  "buffer_load_dwordx4 v0, s[32:35], s61 offen sc1 lds\n" \
  "s_waitcnt lgkmcnt(14)\n" \
  "v_mfma_f32_16x16x32_bf16 v[32:35], a[0:3], v[64:67], 0\n" \
  "buffer_load_dwordx2 v[240:241], v235, s[36:39], s61 offen offset:0 sc1\n" \
  "v_mfma_f32_16x16x32_bf16 v[36:39], a[4:7], v[64:67], 0\n" \
  "s_mov_b32 m0, s41\n" \
  "v_mfma_f32_16x16x32_bf16 v[32:35], a[16:19], v[68:71], v[32:35]\n" \
  "buffer_load_dwordx4 v1, s[32:35], s61 offen sc1 lds\n" \
  "v_mfma_f32_16x16x32_bf16 v[36:39], a[20:23], v[68:71], v[36:39]\n" \
  "buffer_load_dwordx2 v[242:243], v235, s[36:39], s61 offen offset:128 sc1\n" \
  "s_waitcnt lgkmcnt(13)\n" \
  "v_mfma_f32_16x16x32_bf16 v[32:35], a[32:35], v[72:75], v[32:35]\n" \
  "s_mov_b32 m0, s42\n" \
  "v_add_f32 v208, v248, v250\n" \
  "v_add_f32 v209, v249, v251\n" \
  "v_mfma_f32_16x16x32_bf16 v[36:39], a[36:39], v[72:75], v[36:39]\n" \
  "buffer_load_dwordx4 v2, s[32:35], s61 offen sc1 lds\n" \
  "s_waitcnt lgkmcnt(12)\n" \
  "v_mfma_f32_16x16x32_bf16 v[32:35], a[48:51], v[76:79], v[32:35]\n" \
  "v_permlane32_swap_b32 v208, v209\n" \
  "v_mfma_f32_16x16x32_bf16 v[36:39], a[52:55], v[76:79], v[36:39]\n" \
  "s_mov_b32 m0, s43\n" \
  "v_add_f32 v210, v208, v209\n" \
  "v_add_f32 v211, v208, v209\n" \
  "s_waitcnt lgkmcnt(11)\n" \
  "v_mfma_f32_16x16x32_bf16 v[32:35], a[64:67], v[80:83], v[32:35]\n" \
  "buffer_load_dwordx4 v3, s[32:35], s61 offen sc1 lds\n" \
  "v_mfma_f32_16x16x32_bf16 v[36:39], a[68:71], v[80:83], v[36:39]\n" \
  "v_permlane16_swap_b32 v210, v211\n" \
  "s_waitcnt lgkmcnt(10)\n" \
  "v_mfma_f32_16x16x32_bf16 v[32:35], a[80:83], v[84:87], v[32:35]\n" \
  "s_mov_b32 m0, s44\n" \
  "v_add_f32 v208, v210, v211\n" \
  "v_add_f32 v209, v210, v211\n" \
  "v_mfma_f32_16x16x32_bf16 v[36:39], a[84:87], v[84:87], v[36:39]\n" \
  "buffer_load_dwordx4 v4, s[32:35], s61 offen sc1 lds\n" \
  "s_waitcnt lgkmcnt(9)\n" \
  "v_mfma_f32_16x16x32_bf16 v[32:35], a[96:99], v[88:91], v[32:35]\n" \
  "v_permlane32_swap_b32 v208, v209\n" \
  "v_mfma_f32_16x16x32_bf16 v[36:39], a[100:103], v[88:91], v[36:39]\n" \
  "s_mov_b32 m0, s45\n" \
  "s_waitcnt lgkmcnt(8)\n" \
  "v_mfma_f32_16x16x32_bf16 v[32:35], a[112:115], v[92:95], v[32:35]\n" \
  "buffer_load_dwordx4 v5, s[32:35], s61 offen sc1 lds\n" \
  "v_mfma_f32_16x16x32_bf16 v[36:39], a[116:119], v[92:95], v[36:39]\n" \
  "v_mul_f32 v208, 0xbb000000, v208\n" \
  "v_fmamk_f32 v212, v209, 0x3b000000, v234\n" \
  "s_waitcnt lgkmcnt(7)\n" \
  "v_mfma_f32_16x16x32_bf16 v[32:35], a[128:131], v[96:99], v[32:35]\n" \
  "s_mov_b32 m0, s46\n" \
  "v_fma_f32 v212, -v208, v208, v212\n" \
  "v_max_f32 v212, v234, v212\n" \
  "v_mfma_f32_16x16x32_bf16 v[36:39], a[132:135], v[96:99], v[36:39]\n" \
  "buffer_load_dwordx4 v6, s[32:35], s61 offen sc1 lds\n" \
  "s_waitcnt lgkmcnt(6)\n" \
  "v_mfma_f32_16x16x32_bf16 v[32:35], a[144:147], v[100:103], v[32:35]\n" \
  "v_rsq_f32 v209, v212\n" \
  "v_mfma_f32_16x16x32_bf16 v[36:39], a[148:151], v[100:103], v[36:39]\n" \
  "s_mov_b32 m0, s47\n" \
  "v_mul_f32 v208, v208, v209\n" \
  "s_waitcnt lgkmcnt(5)\n" \
  "v_mfma_f32_16x16x32_bf16 v[32:35], a[160:163], v[104:107], v[32:35]\n" \
  "buffer_load_dwordx4 v7, s[32:35], s61 offen sc1 lds\n" \
  "v_mfma_f32_16x16x32_bf16 v[36:39], a[164:167], v[104:107], v[36:39]\n" \
  "v_mul_f32 v216, v192, v208\n" \
  "v_mul_f32 v217, v193, v208\n" \
  "s_waitcnt lgkmcnt(4)\n" \
  "v_mfma_f32_16x16x32_bf16 v[32:35], a[176:179], v[108:111], v[32:35]\n" \
  "v_mul_f32 v218, v194, v208\n" \
  "v_mul_f32 v219, v195, v208\n" \
  "v_mfma_f32_16x16x32_bf16 v[36:39], a[180:183], v[108:111], v[36:39]\n" \
  "v_mul_f32 v220, v196, v208\n" \
  "v_mul_f32 v221, v197, v208\n" \
  "s_waitcnt lgkmcnt(3)\n" \
  "v_mfma_f32_16x16x32_bf16 v[32:35], a[192:195], v[112:115], v[32:35]\n" \
  "v_cmp_gt_i32 vcc, s63, v62\n" \
  "s_cmp_eq_u32 vcc_hi, 0\n" \
  "v_mul_f32 v222, v198, v208\n" \
  "v_mfma_f32_16x16x32_bf16 v[36:39], a[196:199], v[112:115], v[36:39]\n" \
  "s_cselect_b32 s72, s64, 0x40000000\n" \
  "v_mov_b32 v31, s72\n" \
  "v_mul_f32 v223, v199, v208\n" \
  "s_waitcnt lgkmcnt(2)\n" \
  "v_mfma_f32_16x16x32_bf16 v[32:35], a[208:211], v[116:119], v[32:35]\n" \
  "ds_write_b32 v15, v31\n" \
  "v_mul_f32 v224, v200, v208\n" \
  "v_mfma_f32_16x16x32_bf16 v[36:39], a[212:215], v[116:119], v[36:39]\n" \
  "v_mul_f32 v225, v201, v208\n" \
  "v_mul_f32 v226, v202, v208\n" \
  "s_waitcnt lgkmcnt(2)\n" \
  "v_mfma_f32_16x16x32_bf16 v[32:35], a[224:227], v[120:123], v[32:35]\n" \
  "v_mul_f32 v227, v203, v208\n" \
  "v_mul_f32 v228, v204, v208\n" \
  "v_mfma_f32_16x16x32_bf16 v[36:39], a[228:231], v[120:123], v[36:39]\n" \
  "v_mul_f32 v229, v205, v208\n" \
  "v_mul_f32 v230, v206, v208\n" \
  "s_waitcnt lgkmcnt(1)\n" \
  "v_mfma_f32_16x16x32_bf16 v[32:35], a[240:243], v[124:127], v[32:35]\n" \
  "v_mul_f32 v231, v207, v208\n" \
  "v_mfma_f32_16x16x32_bf16 v[36:39], a[244:247], v[124:127], v[36:39]\n" \
  "v_mfma_f32_16x16x32_bf16 v[40:43], a[8:11], v[64:67], 0\n" \
  "v_mfma_f32_16x16x32_bf16 v[44:47], a[12:15], v[64:67], 0\n" \
  "v_fmac_f32 v216, v209, v32\n" \
  "v_fmac_f32 v217, v209, v33\n" \
  "v_mfma_f32_16x16x32_bf16 v[40:43], a[24:27], v[68:71], v[40:43]\n" \
  "v_fmac_f32 v218, v209, v34\n" \
  "v_fmac_f32 v219, v209, v35\n" \
  "v_mfma_f32_16x16x32_bf16 v[44:47], a[28:31], v[68:71], v[44:47]\n" \
  "v_fmac_f32 v220, v209, v36\n" \
  "v_fmac_f32 v221, v209, v37\n" \
  "v_mfma_f32_16x16x32_bf16 v[40:43], a[40:43], v[72:75], v[40:43]\n" \
  "v_fmac_f32 v222, v209, v38\n" \
  "v_fmac_f32 v223, v209, v39\n" \
  "v_mfma_f32_16x16x32_bf16 v[44:47], a[44:47], v[72:75], v[44:47]\n" \
  "v_cvt_pk_bf16_f32 v48, v216, v220\n" \
  "v_cvt_pk_bf16_f32 v49, v217, v221\n" \
  "v_mfma_f32_16x16x32_bf16 v[40:43], a[56:59], v[76:79], v[40:43]\n" \
  "v_cvt_pk_bf16_f32 v50, v218, v222\n" \
  "v_cvt_pk_bf16_f32 v51, v219, v223\n" \
  "v_mfma_f32_16x16x32_bf16 v[44:47], a[60:63], v[76:79], v[44:47]\n" \
  "buffer_store_dwordx4 v[48:51], v12, s[36:39], s66 offen offset:0 sc1\n" \
  "v_mfma_f32_16x16x32_bf16 v[40:43], a[72:75], v[80:83], v[40:43]\n" \
  "v_mfma_f32_16x16x32_bf16 v[44:47], a[76:79], v[80:83], v[44:47]\n" \
  "v_mfma_f32_16x16x32_bf16 v[40:43], a[88:91], v[84:87], v[40:43]\n" \
  "v_mfma_f32_16x16x32_bf16 v[44:47], a[92:95], v[84:87], v[44:47]\n" \
  "v_mfma_f32_16x16x32_bf16 v[40:43], a[104:107], v[88:91], v[40:43]\n" \
  "v_mfma_f32_16x16x32_bf16 v[44:47], a[108:111], v[88:91], v[44:47]\n" \
  "v_mfma_f32_16x16x32_bf16 v[40:43], a[120:123], v[92:95], v[40:43]\n" \
  "v_mfma_f32_16x16x32_bf16 v[44:47], a[124:127], v[92:95], v[44:47]\n" \
  "v_mfma_f32_16x16x32_bf16 v[40:43], a[136:139], v[96:99], v[40:43]\n" \
  "v_mfma_f32_16x16x32_bf16 v[44:47], a[140:143], v[96:99], v[44:47]\n" \
  "v_mfma_f32_16x16x32_bf16 v[40:43], a[152:155], v[100:103], v[40:43]\n" \
  "v_mfma_f32_16x16x32_bf16 v[44:47], a[156:159], v[100:103], v[44:47]\n" \
  "v_mfma_f32_16x16x32_bf16 v[40:43], a[168:171], v[104:107], v[40:43]\n" \
  "v_mfma_f32_16x16x32_bf16 v[44:47], a[172:175], v[104:107], v[44:47]\n" \
  "v_mfma_f32_16x16x32_bf16 v[40:43], a[184:187], v[108:111], v[40:43]\n" \
  "v_mfma_f32_16x16x32_bf16 v[44:47], a[188:191], v[108:111], v[44:47]\n" \
  "v_mfma_f32_16x16x32_bf16 v[40:43], a[200:203], v[112:115], v[40:43]\n" \
  "v_mfma_f32_16x16x32_bf16 v[44:47], a[204:207], v[112:115], v[44:47]\n" \
  "v_mfma_f32_16x16x32_bf16 v[40:43], a[216:219], v[116:119], v[40:43]\n" \
  "v_mfma_f32_16x16x32_bf16 v[44:47], a[220:223], v[116:119], v[44:47]\n" \
  "v_mfma_f32_16x16x32_bf16 v[40:43], a[232:235], v[120:123], v[40:43]\n" \
  "v_mfma_f32_16x16x32_bf16 v[44:47], a[236:239], v[120:123], v[44:47]\n" \
  "v_mfma_f32_16x16x32_bf16 v[40:43], a[248:251], v[124:127], v[40:43]\n" \
  "v_mfma_f32_16x16x32_bf16 v[44:47], a[252:255], v[124:127], v[44:47]\n" \
  "s_nop 3\n" \
  "v_fmac_f32 v224, v209, v40\n" \
  "v_fmac_f32 v225, v209, v41\n" \
  "v_fmac_f32 v226, v209, v42\n" \
  "v_fmac_f32 v227, v209, v43\n" \
  "v_pk_fma_f32 v[228:229], v[44:45], v[208:209], v[228:229] op_sel:[0,1,0]\n" \
  "v_pk_fma_f32 v[230:231], v[46:47], v[208:209], v[230:231] op_sel:[0,1,0]\n" \
  "v_cvt_pk_bf16_f32 v52, v224, v228\n" \
  "v_cvt_pk_bf16_f32 v53, v225, v229\n" \
  "v_cvt_pk_bf16_f32 v54, v226, v230\n" \
  "v_cvt_pk_bf16_f32 v55, v227, v231\n" \
  "buffer_store_dwordx4 v[52:55], v12, s[36:39], s66 offen offset:1024 sc1\n" \
  "s_cmp_ge_u32 s60, 64\n" \
  "s_cbranch_scc1 .Lqk_last\n" \
  "s_waitcnt vmcnt(2) lgkmcnt(0)\n" \
  ".Lqk_p:\n" \
  "s_barrier\n" \
  "ds_read_b128 v[64:67], v8 offset:0\n" \
  "ds_read_b32 v31, v28\n" \
  "ds_read_b128 v[68:71], v9 offset:0\n" \
  "ds_read_b128 v[72:75], v10 offset:0\n" \
  "ds_read_b128 v[76:79], v11 offset:0\n" \
  "s_waitcnt lgkmcnt(4)\n" \
  "v_mfma_f32_16x16x32_bf16 v[32:35], a[0:3], v[64:67], 0\n" \
  "ds_read_b128 v[80:83], v8 offset:256\n" \
  "v_mfma_f32_16x16x32_bf16 v[36:39], a[4:7], v[64:67], 0\n" \
  "s_waitcnt lgkmcnt(4)\n" \
  "v_readfirstlane_b32 s69, v31\n" \
  "s_waitcnt lgkmcnt(3)\n" \
  "v_mfma_f32_16x16x32_bf16 v[32:35], a[16:19], v[68:71], v[32:35]\n" \
  "ds_read_b128 v[84:87], v9 offset:256\n" \
  "s_add_u32 m0, s40, 0x8000\n" \
  "v_mfma_f32_16x16x32_bf16 v[36:39], a[20:23], v[68:71], v[36:39]\n" \
  "buffer_load_dwordx4 v0, s[52:55], s69 offen sc1 lds\n" \
  "s_waitcnt lgkmcnt(3)\n" \
  "v_mfma_f32_16x16x32_bf16 v[32:35], a[32:35], v[72:75], v[32:35]\n" \
  "ds_read_b128 v[88:91], v10 offset:256\n" \
  "v_add_f32 v208, v240, v242\n" \
  "v_mfma_f32_16x16x32_bf16 v[36:39], a[36:39], v[72:75], v[36:39]\n" \
  "v_add_f32 v209, v241, v243\n" \
  "s_waitcnt lgkmcnt(3)\n" \
  "v_mfma_f32_16x16x32_bf16 v[32:35], a[48:51], v[76:79], v[32:35]\n" \
  "ds_read_b128 v[92:95], v11 offset:256\n" \
  "s_add_u32 m0, s41, 0x8000\n" \
  "v_mfma_f32_16x16x32_bf16 v[36:39], a[52:55], v[76:79], v[36:39]\n" \
  "buffer_load_dwordx4 v1, s[52:55], s69 offen sc1 lds\n" \
  "s_waitcnt lgkmcnt(3)\n" \
  "v_mfma_f32_16x16x32_bf16 v[32:35], a[64:67], v[80:83], v[32:35]\n" \
  "ds_read_b128 v[96:99], v8 offset:512\n" \
  "v_mfma_f32_16x16x32_bf16 v[36:39], a[68:71], v[80:83], v[36:39]\n" \
  "v_permlane32_swap_b32 v208, v209\n" \
  "s_waitcnt lgkmcnt(3)\n" \
  "v_mfma_f32_16x16x32_bf16 v[32:35], a[80:83], v[84:87], v[32:35]\n" \
  "ds_read_b128 v[100:103], v9 offset:512\n" \
  "s_add_u32 m0, s42, 0x8000\n" \
  "v_mfma_f32_16x16x32_bf16 v[36:39], a[84:87], v[84:87], v[36:39]\n" \
  "buffer_load_dwordx4 v2, s[52:55], s69 offen sc1 lds\n" \
  "s_waitcnt lgkmcnt(3)\n" \
  "v_mfma_f32_16x16x32_bf16 v[32:35], a[96:99], v[88:91], v[32:35]\n" \
  "ds_read_b128 v[104:107], v10 offset:512\n" \
  "v_add_f32 v210, v208, v209\n" \
  "v_mfma_f32_16x16x32_bf16 v[36:39], a[100:103], v[88:91], v[36:39]\n" \
  "v_add_f32 v211, v208, v209\n" \
  "s_waitcnt lgkmcnt(3)\n" \
  "v_mfma_f32_16x16x32_bf16 v[32:35], a[112:115], v[92:95], v[32:35]\n" \
  "ds_read_b128 v[108:111], v11 offset:512\n" \
  "s_add_u32 m0, s43, 0x8000\n" \
  "v_mfma_f32_16x16x32_bf16 v[36:39], a[116:119], v[92:95], v[36:39]\n" \
  "buffer_load_dwordx4 v3, s[52:55], s69 offen sc1 lds\n" \
  "s_waitcnt lgkmcnt(3)\n" \
  "v_mfma_f32_16x16x32_bf16 v[32:35], a[128:131], v[96:99], v[32:35]\n" \
  "ds_read_b128 v[112:115], v8 offset:768\n" \
  "v_mfma_f32_16x16x32_bf16 v[36:39], a[132:135], v[96:99], v[36:39]\n" \
  "v_permlane16_swap_b32 v210, v211\n" \
  "s_waitcnt lgkmcnt(3)\n" \
  "v_mfma_f32_16x16x32_bf16 v[32:35], a[144:147], v[100:103], v[32:35]\n" \
  "ds_read_b128 v[116:119], v9 offset:768\n" \
  "s_add_u32 m0, s44, 0x8000\n" \
  "v_mfma_f32_16x16x32_bf16 v[36:39], a[148:151], v[100:103], v[36:39]\n" \
  "buffer_load_dwordx4 v4, s[52:55], s69 offen sc1 lds\n" \
  "s_waitcnt lgkmcnt(3)\n" \
  "v_mfma_f32_16x16x32_bf16 v[32:35], a[160:163], v[104:107], v[32:35]\n" \
  "ds_read_b128 v[120:123], v10 offset:768\n" \
  "v_add_f32 v208, v210, v211\n" \
  "v_mfma_f32_16x16x32_bf16 v[36:39], a[164:167], v[104:107], v[36:39]\n" \
  "v_add_f32 v209, v210, v211\n" \
  "s_waitcnt lgkmcnt(3)\n" \
  "v_mfma_f32_16x16x32_bf16 v[32:35], a[176:179], v[108:111], v[32:35]\n" \
  "ds_read_b128 v[124:127], v11 offset:768\n" \
  "s_add_u32 m0, s45, 0x8000\n" \
  "v_mfma_f32_16x16x32_bf16 v[36:39], a[180:183], v[108:111], v[36:39]\n" \
  "buffer_load_dwordx4 v5, s[52:55], s69 offen sc1 lds\n" \
  "s_waitcnt lgkmcnt(3)\n" \
  "v_mfma_f32_16x16x32_bf16 v[32:35], a[192:195], v[112:115], v[32:35]\n" \
  "v_permlane32_swap_b32 v208, v209\n" \
  "v_mfma_f32_16x16x32_bf16 v[36:39], a[196:199], v[112:115], v[36:39]\n" \
  "s_waitcnt lgkmcnt(2)\n" \
  "v_mfma_f32_16x16x32_bf16 v[32:35], a[208:211], v[116:119], v[32:35]\n" \
  "s_add_u32 m0, s46, 0x8000\n" \
  "v_mul_f32 v208, 0xbb000000, v208\n" \
  "v_fmamk_f32 v212, v209, 0x3b000000, v234\n" \
  "v_mfma_f32_16x16x32_bf16 v[36:39], a[212:215], v[116:119], v[36:39]\n" \
  "buffer_load_dwordx4 v6, s[52:55], s69 offen sc1 lds\n" \
  "s_waitcnt lgkmcnt(1)\n" \
  "v_mfma_f32_16x16x32_bf16 v[32:35], a[224:227], v[120:123], v[32:35]\n" \
  "v_fma_f32 v212, -v208, v208, v212\n" \
  "v_max_f32 v212, v234, v212\n" \
  "v_mfma_f32_16x16x32_bf16 v[36:39], a[228:231], v[120:123], v[36:39]\n" \
  "v_rsq_f32 v209, v212\n" \
  "s_waitcnt lgkmcnt(0)\n" \
  "v_mfma_f32_16x16x32_bf16 v[32:35], a[240:243], v[124:127], v[32:35]\n" \
  "s_add_u32 m0, s47, 0x8000\n" \
  "v_mul_f32 v208, v208, v209\n" \
  "v_mfma_f32_16x16x32_bf16 v[36:39], a[244:247], v[124:127], v[36:39]\n" \
  "buffer_load_dwordx4 v7, s[52:55], s69 offen sc1 lds\n" \
  "v_mfma_f32_16x16x32_bf16 v[40:43], a[8:11], v[64:67], 0\n" \
  "v_mul_f32 v216, v192, v208\n" \
  "v_mul_f32 v217, v193, v208\n" \
  "v_mfma_f32_16x16x32_bf16 v[44:47], a[12:15], v[64:67], 0\n" \
  "buffer_load_dwordx2 v[248:249], v233, s[36:39], s69 offen offset:0 sc1\n" \
  "v_mfma_f32_16x16x32_bf16 v[40:43], a[24:27], v[68:71], v[40:43]\n" \
  "v_mul_f32 v218, v194, v208\n" \
  "v_mul_f32 v219, v195, v208\n" \
  "v_mfma_f32_16x16x32_bf16 v[44:47], a[28:31], v[68:71], v[44:47]\n" \
  "buffer_load_dwordx2 v[250:251], v233, s[36:39], s69 offen offset:128 sc1\n" \
  "v_mfma_f32_16x16x32_bf16 v[40:43], a[40:43], v[72:75], v[40:43]\n" \
  "v_mul_f32 v220, v196, v208\n" \
  "v_mul_f32 v221, v197, v208\n" \
  "v_mfma_f32_16x16x32_bf16 v[44:47], a[44:47], v[72:75], v[44:47]\n" \
  "v_mul_f32 v222, v198, v208\n" \
  "v_mul_f32 v223, v199, v208\n" \
  "v_mfma_f32_16x16x32_bf16 v[40:43], a[56:59], v[76:79], v[40:43]\n" \
  "v_mul_f32 v224, v200, v208\n" \
  "v_mul_f32 v225, v201, v208\n" \
  "v_mfma_f32_16x16x32_bf16 v[44:47], a[60:63], v[76:79], v[44:47]\n" \
  "v_mul_f32 v226, v202, v208\n" \
  "v_mul_f32 v227, v203, v208\n" \
  "v_mfma_f32_16x16x32_bf16 v[40:43], a[72:75], v[80:83], v[40:43]\n" \
  "v_mul_f32 v228, v204, v208\n" \
  "v_mul_f32 v229, v205, v208\n" \
  "v_mfma_f32_16x16x32_bf16 v[44:47], a[76:79], v[80:83], v[44:47]\n" \
  "v_mul_f32 v230, v206, v208\n" \
  "v_mul_f32 v231, v207, v208\n" \
  "v_mfma_f32_16x16x32_bf16 v[40:43], a[88:91], v[84:87], v[40:43]\n" \
  "v_fmac_f32 v216, v209, v32\n" \
  "v_fmac_f32 v217, v209, v33\n" \
  "v_mfma_f32_16x16x32_bf16 v[44:47], a[92:95], v[84:87], v[44:47]\n" \
  "v_fmac_f32 v218, v209, v34\n" \
  "v_fmac_f32 v219, v209, v35\n" \
  "v_mfma_f32_16x16x32_bf16 v[40:43], a[104:107], v[88:91], v[40:43]\n" \
  "v_fmac_f32 v220, v209, v36\n" \
  "v_fmac_f32 v221, v209, v37\n" \
  "v_mfma_f32_16x16x32_bf16 v[44:47], a[108:111], v[88:91], v[44:47]\n" \
  "v_fmac_f32 v222, v209, v38\n" \
  "v_fmac_f32 v223, v209, v39\n" \
  "v_mfma_f32_16x16x32_bf16 v[40:43], a[120:123], v[92:95], v[40:43]\n" \
  "v_cvt_pk_bf16_f32 v48, v216, v220\n" \
  "v_cvt_pk_bf16_f32 v49, v217, v221\n" \
  "v_mfma_f32_16x16x32_bf16 v[44:47], a[124:127], v[92:95], v[44:47]\n" \
  "v_cvt_pk_bf16_f32 v50, v218, v222\n" \
  "v_cvt_pk_bf16_f32 v51, v219, v223\n" \
  "v_mfma_f32_16x16x32_bf16 v[40:43], a[136:139], v[96:99], v[40:43]\n" \
  "buffer_store_dwordx4 v[48:51], v12, s[36:39], s62 offen offset:0 sc1\n" \
  "v_mfma_f32_16x16x32_bf16 v[44:47], a[140:143], v[96:99], v[44:47]\n" \
  "v_mfma_f32_16x16x32_bf16 v[40:43], a[152:155], v[100:103], v[40:43]\n" \
  "v_mfma_f32_16x16x32_bf16 v[44:47], a[156:159], v[100:103], v[44:47]\n" \
  "v_mfma_f32_16x16x32_bf16 v[40:43], a[168:171], v[104:107], v[40:43]\n" \
  "v_mfma_f32_16x16x32_bf16 v[44:47], a[172:175], v[104:107], v[44:47]\n" \
  "v_mfma_f32_16x16x32_bf16 v[40:43], a[184:187], v[108:111], v[40:43]\n" \
  "v_mfma_f32_16x16x32_bf16 v[44:47], a[188:191], v[108:111], v[44:47]\n" \
  "v_mfma_f32_16x16x32_bf16 v[40:43], a[200:203], v[112:115], v[40:43]\n" \
  "v_mfma_f32_16x16x32_bf16 v[44:47], a[204:207], v[112:115], v[44:47]\n" \
  "v_mfma_f32_16x16x32_bf16 v[40:43], a[216:219], v[116:119], v[40:43]\n" \
  "v_mfma_f32_16x16x32_bf16 v[44:47], a[220:223], v[116:119], v[44:47]\n" \
  "v_mfma_f32_16x16x32_bf16 v[40:43], a[232:235], v[120:123], v[40:43]\n" \
  "v_mfma_f32_16x16x32_bf16 v[44:47], a[236:239], v[120:123], v[44:47]\n" \
  "v_mfma_f32_16x16x32_bf16 v[40:43], a[248:251], v[124:127], v[40:43]\n" \
  "v_mfma_f32_16x16x32_bf16 v[44:47], a[252:255], v[124:127], v[44:47]\n" \
  "s_nop 3\n" \
  "v_fmac_f32 v224, v209, v40\n" \
  "v_fmac_f32 v225, v209, v41\n" \
  "v_fmac_f32 v226, v209, v42\n" \
  "v_fmac_f32 v227, v209, v43\n" \
  "v_pk_fma_f32 v[228:229], v[44:45], v[208:209], v[228:229] op_sel:[0,1,0]\n" \
  "v_pk_fma_f32 v[230:231], v[46:47], v[208:209], v[230:231] op_sel:[0,1,0]\n" \
  "v_cvt_pk_bf16_f32 v52, v224, v228\n" \
  "v_cvt_pk_bf16_f32 v53, v225, v229\n" \
  "v_cvt_pk_bf16_f32 v54, v226, v230\n" \
  "v_cvt_pk_bf16_f32 v55, v227, v231\n" \
  "buffer_store_dwordx4 v[52:55], v12, s[36:39], s62 offen offset:1024 sc1\n" \
  ".Lqk_sel:\n" \
  "s_mov_b32 s76, s59\n" \
  "s_cmp_lg_u32 s69, 0x40000000\n" \
  "s_addc_u32 s59, s59, 0\n" \
  "s_sub_u32 s71, s59, s58\n" \
  "s_cmp_lt_u32 s59, 64\n" \
  "s_cselect_b32 s71, s71, 64\n" \
  "s_cmp_ge_i32 s71, s50\n" \
  "s_cselect_b64 s[78:79], -1, 0\n" \
  "v_cndmask_b32_e64 v30, v14, v13, s[78:79]\n" \
  "buffer_load_dword v62, v30, s[36:39], 0 offen sc1\n" \
  "s_cselect_b32 s60, s58, s59\n" \
  "s_cselect_b64 s[32:33], s[36:37], s[52:53]\n" \
  "s_cselect_b32 s34, s38, s54\n" \
  "v_cndmask_b32_e64 v235, v233, v232, s[78:79]\n" \
  "s_cselect_b32 s75, s48, s49\n" \
  "s_cselect_b32 s71, 0, 1\n" \
  "v_cndmask_b32_e64 v32, v17, v16, s[78:79]\n" \
  "v_cndmask_b32_e64 v33, v19, v18, s[78:79]\n" \
  "s_addc_u32 s58, s58, 0\n" \
  "s_add_u32 s59, s59, s71\n" \
  "s_mov_b32 s66, s65\n" \
  "v_readlane_b32 s61, v32, s60\n" \
  "v_readlane_b32 s62, v33, s60\n" \
  "v_readlane_b32 s64, v17, s59\n" \
  "v_readlane_b32 s65, v19, s59\n" \
  "s_add_u32 s63, s59, s71\n" \
  "s_cmp_eq_u32 s59, 63\n" \
  "s_cselect_b32 s63, 63, s63\n" \
  "s_cmp_lt_u32 s60, 64\n" \
  "s_cselect_b32 s77, s60, -1\n" \
  "s_cselect_b32 s61, s61, 0x40000000\n" \
  "s_cselect_b32 s75, s75, 0x40000000\n" \
  "s_cselect_b32 s71, s71, 0\n" \
  "s_cmp_eq_u32 s71, 0\n" \
  "s_cselect_b32 s72, s49, 0x40000000\n" \
  "s_waitcnt vmcnt(1)\n" \
  "s_barrier\n" \
  "buffer_store_dword v60, v29, s[36:39], s67 offen sc1\n" \
  "buffer_store_dword v61, v29, s[36:39], s68 offen sc1\n" \
  "s_waitcnt vmcnt(2)\n" \
  "v_mov_b32 v60, s60\n" \
  "v_mov_b32 v61, s76\n" \
  "s_mov_b32 s67, s75\n" \
  "s_mov_b32 s68, s72\n" \
  "s_cmp_lg_u32 s69, 0x40000000\n" \
  "s_cbranch_scc1 .Lqk_q\n" \
  "s_mov_b32 s68, 0x40000000\n" \
  "s_cmp_ge_u32 s60, 64\n" \
  "s_cbranch_scc1 .Lqk_end\n" \
  "s_mov_b32 m0, s40\n" \
  "s_branch .Lqk_pn_c\n" \
  ".Lqk_pn:\n" \
  ".Lqk_pn_f:\n" \
  "buffer_load_dword v62, v30, s[36:39], 0 offen sc1\n" \
  "s_sub_u32 s70, s70, 1\n" \
  "s_cselect_b32 s77, -1, s77\n" \
  "s_cselect_b32 s70, 0, s70\n" \
  "s_waitcnt vmcnt(0)\n" \
  ".Lqk_pn_c:\n" \
  "v_cmp_gt_i32 vcc, s77, v62\n" \
  "s_and_b32 vcc_lo, vcc_lo, s29\n" \
  "s_cbranch_scc1 .Lqk_pn\n" \
  "s_barrier\n" \
  "buffer_load_dwordx4 v0, s[32:35], s61 offen sc1 lds\n" \
  "s_mov_b32 m0, s41\n" \
  "v_cmp_gt_i32 vcc, s63, v62\n" \
  "buffer_load_dwordx4 v1, s[32:35], s61 offen sc1 lds\n" \
  "s_mov_b32 m0, s42\n" \
  "s_cmp_eq_u32 vcc_hi, 0\n" \
  "buffer_load_dwordx4 v2, s[32:35], s61 offen sc1 lds\n" \
  "s_mov_b32 m0, s43\n" \
  "s_cselect_b32 s72, s64, 0x40000000\n" \
  "buffer_load_dwordx4 v3, s[32:35], s61 offen sc1 lds\n" \
  "s_mov_b32 m0, s44\n" \
  "v_mov_b32 v31, s72\n" \
  "buffer_load_dwordx4 v4, s[32:35], s61 offen sc1 lds\n" \
  "s_mov_b32 m0, s45\n" \
  "ds_write_b32 v15, v31\n" \
  "buffer_load_dwordx4 v5, s[32:35], s61 offen sc1 lds\n" \
  "s_mov_b32 m0, s46\n" \
  "s_nop 0\n" \
  "buffer_load_dwordx4 v6, s[32:35], s61 offen sc1 lds\n" \
  "s_mov_b32 m0, s47\n" \
  "s_nop 0\n" \
  "buffer_load_dwordx4 v7, s[32:35], s61 offen sc1 lds\n" \
  "buffer_load_dwordx2 v[240:241], v235, s[36:39], s61 offen offset:0 sc1\n" \
  "buffer_load_dwordx2 v[242:243], v235, s[36:39], s61 offen offset:128 sc1\n" \
  "s_waitcnt vmcnt(0) lgkmcnt(0)\n" \
  "s_branch .Lqk_p\n" \
  ".Lqk_last:\n" \
  "s_mov_b32 s69, 0x40000000\n" \
  "s_branch .Lqk_sel\n" \
  ".Lqk_end:\n" \
  "s_endpgm\n"
#define ASM_V \
  "s_nop 4\n" \
  "s_mov_b32 s58, 0\n" \
  "s_mov_b32 s59, 0\n" \
  "s_mov_b32 s70, 100000\n" \
  "s_mov_b32 s69, 0x40000000\n" \
  "s_mov_b32 s67, 0x40000000\n" \
  "s_mov_b32 s68, 0x40000000\n" \
  "v_mov_b32 v60, 0\n" \
  "v_mov_b32 v61, 0\n" \
  "v_cmp_eq_u32 vcc, v15, v28\n" \
  "s_mov_b32 s29, vcc_lo\n" \
  "s_mov_b32 s73, 0x3f803f80\n" \
  "v_mov_b32 v234, 0x3727c5ac\n" \
  "s_mov_b32 s35, 0x20000\n" \
  "v_mov_b32 v236, s73\n" \
  "v_mov_b32 v237, s73\n" \
  "v_mov_b32 v238, s73\n" \
  "v_mov_b32 v239, s73\n" \
  "ds_read_b64_tr_b16 a[0:1], v20 offset:0\n" \
  "ds_read_b64_tr_b16 a[2:3], v21 offset:0\n" \
  "ds_read_b64_tr_b16 a[4:5], v22 offset:0\n" \
  "ds_read_b64_tr_b16 a[6:7], v23 offset:0\n" \
  "ds_read_b64_tr_b16 a[8:9], v20 offset:8192\n" \
  "ds_read_b64_tr_b16 a[10:11], v21 offset:8192\n" \
  "ds_read_b64_tr_b16 a[12:13], v22 offset:8192\n" \
  "ds_read_b64_tr_b16 a[14:15], v23 offset:8192\n" \
  "ds_read_b64_tr_b16 a[16:17], v20 offset:16384\n" \
  "ds_read_b64_tr_b16 a[18:19], v21 offset:16384\n" \
  "ds_read_b64_tr_b16 a[20:21], v22 offset:16384\n" \
  "ds_read_b64_tr_b16 a[22:23], v23 offset:16384\n" \
  "ds_read_b64_tr_b16 a[24:25], v20 offset:24576\n" \
  "ds_read_b64_tr_b16 a[26:27], v21 offset:24576\n" \
  "s_waitcnt lgkmcnt(12)\n" \
  "v_mfma_f32_16x16x32_bf16 v[192:195], v[236:239], a[0:3], 0\n" \
  "ds_read_b64_tr_b16 a[28:29], v22 offset:24576\n" \
  "ds_read_b64_tr_b16 a[30:31], v23 offset:24576\n" \
  "s_waitcnt lgkmcnt(12)\n" \
  "v_mfma_f32_16x16x32_bf16 v[196:199], v[236:239], a[4:7], 0\n" \
  "ds_read_b64_tr_b16 a[32:33], v20 offset:32768\n" \
  "ds_read_b64_tr_b16 a[34:35], v21 offset:32768\n" \
  "s_waitcnt lgkmcnt(12)\n" \
  "v_mfma_f32_16x16x32_bf16 v[192:195], v[236:239], a[8:11], v[192:195]\n" \
  "ds_read_b64_tr_b16 a[36:37], v22 offset:32768\n" \
  "ds_read_b64_tr_b16 a[38:39], v23 offset:32768\n" \
  "s_waitcnt lgkmcnt(12)\n" \
  "v_mfma_f32_16x16x32_bf16 v[196:199], v[236:239], a[12:15], v[196:199]\n" \
  "ds_read_b64_tr_b16 a[40:41], v20 offset:40960\n" \
  "ds_read_b64_tr_b16 a[42:43], v21 offset:40960\n" \
  "s_waitcnt lgkmcnt(12)\n" \
  "v_mfma_f32_16x16x32_bf16 v[192:195], v[236:239], a[16:19], v[192:195]\n" \
  "ds_read_b64_tr_b16 a[44:45], v22 offset:40960\n" \
  "ds_read_b64_tr_b16 a[46:47], v23 offset:40960\n" \
  "s_waitcnt lgkmcnt(12)\n" \
  "v_mfma_f32_16x16x32_bf16 v[196:199], v[236:239], a[20:23], v[196:199]\n" \
  "ds_read_b64_tr_b16 a[48:49], v20 offset:49152\n" \
  "ds_read_b64_tr_b16 a[50:51], v21 offset:49152\n" \
  "s_waitcnt lgkmcnt(12)\n" \
  "v_mfma_f32_16x16x32_bf16 v[192:195], v[236:239], a[24:27], v[192:195]\n" \
  "ds_read_b64_tr_b16 a[52:53], v22 offset:49152\n" \
  "ds_read_b64_tr_b16 a[54:55], v23 offset:49152\n" \
  "s_waitcnt lgkmcnt(12)\n" \
  "v_mfma_f32_16x16x32_bf16 v[196:199], v[236:239], a[28:31], v[196:199]\n" \
  "ds_read_b64_tr_b16 a[56:57], v20 offset:57344\n" \
  "ds_read_b64_tr_b16 a[58:59], v21 offset:57344\n" \
  "s_waitcnt lgkmcnt(12)\n" \
  "v_mfma_f32_16x16x32_bf16 v[192:195], v[236:239], a[32:35], v[192:195]\n" \
  "ds_read_b64_tr_b16 a[60:61], v22 offset:57344\n" \
  "ds_read_b64_tr_b16 a[62:63], v23 offset:57344\n" \
  "s_waitcnt lgkmcnt(12)\n" \
  "v_mfma_f32_16x16x32_bf16 v[196:199], v[236:239], a[36:39], v[196:199]\n" \
  "v_xor_b32 v20, 0x10000, v20\n" \
  "v_xor_b32 v21, 0x10000, v21\n" \
  "v_xor_b32 v22, 0x10000, v22\n" \
  "v_xor_b32 v23, 0x10000, v23\n" \
  "ds_read_b64_tr_b16 a[64:65], v20 offset:0\n" \
  "ds_read_b64_tr_b16 a[66:67], v21 offset:0\n" \
  "s_waitcnt lgkmcnt(12)\n" \
  "v_mfma_f32_16x16x32_bf16 v[192:195], v[236:239], a[40:43], v[192:195]\n" \
  "ds_read_b64_tr_b16 a[68:69], v22 offset:0\n" \
  "ds_read_b64_tr_b16 a[70:71], v23 offset:0\n" \
  "s_waitcnt lgkmcnt(12)\n" \
  "v_mfma_f32_16x16x32_bf16 v[196:199], v[236:239], a[44:47], v[196:199]\n" \
  "ds_read_b64_tr_b16 a[72:73], v20 offset:8192\n" \
  "ds_read_b64_tr_b16 a[74:75], v21 offset:8192\n" \
  "s_waitcnt lgkmcnt(12)\n" \
  "v_mfma_f32_16x16x32_bf16 v[192:195], v[236:239], a[48:51], v[192:195]\n" \
  "ds_read_b64_tr_b16 a[76:77], v22 offset:8192\n" \
  "ds_read_b64_tr_b16 a[78:79], v23 offset:8192\n" \
  "s_waitcnt lgkmcnt(12)\n" \
  "v_mfma_f32_16x16x32_bf16 v[196:199], v[236:239], a[52:55], v[196:199]\n" \
  "ds_read_b64_tr_b16 a[80:81], v20 offset:16384\n" \
  "ds_read_b64_tr_b16 a[82:83], v21 offset:16384\n" \
  "s_waitcnt lgkmcnt(12)\n" \
  "v_mfma_f32_16x16x32_bf16 v[192:195], v[236:239], a[56:59], v[192:195]\n" \
  "ds_read_b64_tr_b16 a[84:85], v22 offset:16384\n" \
  "ds_read_b64_tr_b16 a[86:87], v23 offset:16384\n" \
  "s_waitcnt lgkmcnt(12)\n" \
  "v_mfma_f32_16x16x32_bf16 v[196:199], v[236:239], a[60:63], v[196:199]\n" \
  "ds_read_b64_tr_b16 a[88:89], v20 offset:24576\n" \
  "ds_read_b64_tr_b16 a[90:91], v21 offset:24576\n" \
  "s_waitcnt lgkmcnt(12)\n" \
  "v_mfma_f32_16x16x32_bf16 v[192:195], v[236:239], a[64:67], v[192:195]\n" \
  "ds_read_b64_tr_b16 a[92:93], v22 offset:24576\n" \
  "ds_read_b64_tr_b16 a[94:95], v23 offset:24576\n" \
  "s_waitcnt lgkmcnt(12)\n" \
  "v_mfma_f32_16x16x32_bf16 v[196:199], v[236:239], a[68:71], v[196:199]\n" \
  "ds_read_b64_tr_b16 a[96:97], v20 offset:32768\n" \
  "ds_read_b64_tr_b16 a[98:99], v21 offset:32768\n" \
  "s_waitcnt lgkmcnt(12)\n" \
  "v_mfma_f32_16x16x32_bf16 v[192:195], v[236:239], a[72:75], v[192:195]\n" \
  "ds_read_b64_tr_b16 a[100:101], v22 offset:32768\n" \
  "ds_read_b64_tr_b16 a[102:103], v23 offset:32768\n" \
  "s_waitcnt lgkmcnt(12)\n" \
  "v_mfma_f32_16x16x32_bf16 v[196:199], v[236:239], a[76:79], v[196:199]\n" \
  "ds_read_b64_tr_b16 a[104:105], v20 offset:40960\n" \
  "ds_read_b64_tr_b16 a[106:107], v21 offset:40960\n" \
  "s_waitcnt lgkmcnt(12)\n" \
  "v_mfma_f32_16x16x32_bf16 v[192:195], v[236:239], a[80:83], v[192:195]\n" \
  "ds_read_b64_tr_b16 a[108:109], v22 offset:40960\n" \
  "ds_read_b64_tr_b16 a[110:111], v23 offset:40960\n" \
  "s_waitcnt lgkmcnt(12)\n" \
  "v_mfma_f32_16x16x32_bf16 v[196:199], v[236:239], a[84:87], v[196:199]\n" \
  "ds_read_b64_tr_b16 a[112:113], v20 offset:49152\n" \
  "ds_read_b64_tr_b16 a[114:115], v21 offset:49152\n" \
  "s_waitcnt lgkmcnt(12)\n" \
  "v_mfma_f32_16x16x32_bf16 v[192:195], v[236:239], a[88:91], v[192:195]\n" \
  "ds_read_b64_tr_b16 a[116:117], v22 offset:49152\n" \
  "ds_read_b64_tr_b16 a[118:119], v23 offset:49152\n" \
  "s_waitcnt lgkmcnt(12)\n" \
  "v_mfma_f32_16x16x32_bf16 v[196:199], v[236:239], a[92:95], v[196:199]\n" \
  "ds_read_b64_tr_b16 a[120:121], v20 offset:57344\n" \
  "ds_read_b64_tr_b16 a[122:123], v21 offset:57344\n" \
  "s_waitcnt lgkmcnt(12)\n" \
  "v_mfma_f32_16x16x32_bf16 v[192:195], v[236:239], a[96:99], v[192:195]\n" \
  "ds_read_b64_tr_b16 a[124:125], v22 offset:57344\n" \
  "ds_read_b64_tr_b16 a[126:127], v23 offset:57344\n" \
  "s_waitcnt lgkmcnt(12)\n" \
  "v_mfma_f32_16x16x32_bf16 v[196:199], v[236:239], a[100:103], v[196:199]\n" \
  "s_waitcnt lgkmcnt(10)\n" \
  "v_mfma_f32_16x16x32_bf16 v[192:195], v[236:239], a[104:107], v[192:195]\n" \
  "s_waitcnt lgkmcnt(8)\n" \
  "v_mfma_f32_16x16x32_bf16 v[196:199], v[236:239], a[108:111], v[196:199]\n" \
  "s_waitcnt lgkmcnt(6)\n" \
  "v_mfma_f32_16x16x32_bf16 v[192:195], v[236:239], a[112:115], v[192:195]\n" \
  "s_waitcnt lgkmcnt(4)\n" \
  "v_mfma_f32_16x16x32_bf16 v[196:199], v[236:239], a[116:119], v[196:199]\n" \
  "s_waitcnt lgkmcnt(2)\n" \
  "v_mfma_f32_16x16x32_bf16 v[192:195], v[236:239], a[120:123], v[192:195]\n" \
  "s_waitcnt lgkmcnt(0)\n" \
  "v_mfma_f32_16x16x32_bf16 v[196:199], v[236:239], a[124:127], v[196:199]\n" \
  "s_waitcnt vmcnt(0) lgkmcnt(0)\n" \
  "s_branch .Lv_sel\n" \
  ".Lv_q:\n" \
  "ds_read_b128 v[64:67], v8 offset:32768\n" \
  "ds_read_b128 v[68:71], v9 offset:32768\n" \
  "ds_read_b128 v[72:75], v10 offset:32768\n" \
  "ds_read_b128 v[76:79], v11 offset:32768\n" \
  "ds_read_b128 v[80:83], v8 offset:33024\n" \
  "ds_read_b128 v[84:87], v9 offset:33024\n" \
  "ds_read_b128 v[88:91], v10 offset:33024\n" \
  "ds_read_b128 v[92:95], v11 offset:33024\n" \
  "ds_read_b128 v[96:99], v8 offset:33280\n" \
  "ds_read_b128 v[100:103], v9 offset:33280\n" \
  "ds_read_b128 v[104:107], v10 offset:33280\n" \
  "ds_read_b128 v[108:111], v11 offset:33280\n" \
  "ds_read_b128 v[112:115], v8 offset:33536\n" \
  "ds_read_b128 v[116:119], v9 offset:33536\n" \
  "ds_read_b128 v[120:123], v10 offset:33536\n" \
  "ds_read_b128 v[124:127], v11 offset:33536\n" \
  "ds_read_b128 v[128:131], v8 offset:49152\n" \
  "ds_read_b128 v[132:135], v9 offset:49152\n" \
  "ds_read_b128 v[136:139], v10 offset:49152\n" \
  "ds_read_b128 v[140:143], v11 offset:49152\n" \
  "ds_read_b128 v[144:147], v8 offset:49408\n" \
  "ds_read_b128 v[148:151], v9 offset:49408\n" \
  "ds_read_b128 v[152:155], v10 offset:49408\n" \
  "ds_read_b128 v[156:159], v11 offset:49408\n" \
  "ds_read_b128 v[160:163], v8 offset:49664\n" \
  "ds_read_b128 v[164:167], v9 offset:49664\n" \
  "ds_read_b128 v[168:171], v10 offset:49664\n" \
  "ds_read_b128 v[172:175], v11 offset:49664\n" \
  "ds_read_b128 v[176:179], v8 offset:49920\n" \
  "ds_read_b128 v[180:183], v9 offset:49920\n" \
  "ds_read_b128 v[184:187], v10 offset:49920\n" \
  "ds_read_b128 v[188:191], v11 offset:49920\n" \
  "s_mov_b32 m0, s40\n" \
  "s_branch .Lv_pq_c\n" \
  ".Lv_pq:\n" \
  ".Lv_pq_f:\n" \
  "buffer_load_dword v62, v30, s[36:39], 0 offen sc1\n" \
  "s_sub_u32 s70, s70, 1\n" \
  "s_cselect_b32 s77, -1, s77\n" \
  "s_cselect_b32 s70, 0, s70\n" \
  "s_waitcnt vmcnt(0)\n" \
  ".Lv_pq_c:\n" \
  "v_cmp_gt_i32 vcc, s77, v62\n" \
  "s_and_b32 vcc_lo, vcc_lo, s29\n" \
  "s_cbranch_scc1 .Lv_pq\n" \
  "s_barrier\n" \
  "buffer_load_dwordx4 v0, s[32:35], s61 offen sc1 lds\n" \
  "s_waitcnt lgkmcnt(14)\n" \
  "v_mfma_f32_16x16x32_bf16 v[32:35], v[64:67], a[0:3], 0\n" \
  "buffer_load_dwordx2 v[240:241], v235, s[36:39], s61 offen offset:0 sc1\n" \
  "v_mfma_f32_16x16x32_bf16 v[36:39], v[64:67], a[4:7], 0\n" \
  "s_mov_b32 m0, s41\n" \
  "v_mfma_f32_16x16x32_bf16 v[32:35], v[68:71], a[8:11], v[32:35]\n" \
  "buffer_load_dwordx4 v1, s[32:35], s61 offen sc1 lds\n" \
  "v_mfma_f32_16x16x32_bf16 v[36:39], v[68:71], a[12:15], v[36:39]\n" \
  "buffer_load_dwordx2 v[242:243], v235, s[36:39], s61 offen offset:128 sc1\n" \
  "v_mfma_f32_16x16x32_bf16 v[32:35], v[72:75], a[16:19], v[32:35]\n" \
  "s_mov_b32 m0, s42\n" \
  "v_add_f32 v208, v248, v250\n" \
  "v_add_f32 v209, v249, v251\n" \
  "v_mfma_f32_16x16x32_bf16 v[36:39], v[72:75], a[20:23], v[36:39]\n" \
  "buffer_load_dwordx4 v2, s[32:35], s61 offen sc1 lds\n" \
  "v_mfma_f32_16x16x32_bf16 v[32:35], v[76:79], a[24:27], v[32:35]\n" \
  "buffer_load_dwordx2 v[244:245], v235, s[36:39], s61 offen offset:256 sc1\n" \
  "v_mfma_f32_16x16x32_bf16 v[36:39], v[76:79], a[28:31], v[36:39]\n" \
  "s_mov_b32 m0, s43\n" \
  "v_add_f32 v210, v252, v254\n" \
  "v_add_f32 v211, v253, v255\n" \
  "v_mfma_f32_16x16x32_bf16 v[32:35], v[80:83], a[32:35], v[32:35]\n" \
  "buffer_load_dwordx4 v3, s[32:35], s61 offen sc1 lds\n" \
  "v_mfma_f32_16x16x32_bf16 v[36:39], v[80:83], a[36:39], v[36:39]\n" \
  "buffer_load_dwordx2 v[246:247], v235, s[36:39], s61 offen offset:384 sc1\n" \
  "v_mfma_f32_16x16x32_bf16 v[32:35], v[84:87], a[40:43], v[32:35]\n" \
  "s_mov_b32 m0, s44\n" \
  "v_add_f32 v208, v208, v210\n" \
  "v_add_f32 v209, v209, v211\n" \
  "v_mfma_f32_16x16x32_bf16 v[36:39], v[84:87], a[44:47], v[36:39]\n" \
  "buffer_load_dwordx4 v4, s[32:35], s61 offen sc1 lds\n" \
  "v_mfma_f32_16x16x32_bf16 v[32:35], v[88:91], a[48:51], v[32:35]\n" \
  "v_add_f32_dpp v208, v208, v208 row_ror:8 row_mask:0xf bank_mask:0xf\n" \
  "v_add_f32_dpp v209, v209, v209 row_ror:8 row_mask:0xf bank_mask:0xf\n" \
  "v_mfma_f32_16x16x32_bf16 v[36:39], v[88:91], a[52:55], v[36:39]\n" \
  "s_mov_b32 m0, s45\n" \
  "v_mul_f32 v208, 0xbb000000, v208\n" \
  "v_fmamk_f32 v212, v209, 0x3b000000, v234\n" \
  "v_mfma_f32_16x16x32_bf16 v[32:35], v[92:95], a[56:59], v[32:35]\n" \
  "buffer_load_dwordx4 v5, s[32:35], s61 offen sc1 lds\n" \
  "v_mfma_f32_16x16x32_bf16 v[36:39], v[92:95], a[60:63], v[36:39]\n" \
  "v_fma_f32 v212, -v208, v208, v212\n" \
  "v_max_f32 v212, v234, v212\n" \
  "v_mfma_f32_16x16x32_bf16 v[32:35], v[96:99], a[64:67], v[32:35]\n" \
  "s_mov_b32 m0, s46\n" \
  "v_rsq_f32 v209, v212\n" \
  "v_mfma_f32_16x16x32_bf16 v[36:39], v[96:99], a[68:71], v[36:39]\n" \
  "buffer_load_dwordx4 v6, s[32:35], s61 offen sc1 lds\n" \
  "v_mfma_f32_16x16x32_bf16 v[32:35], v[100:103], a[72:75], v[32:35]\n" \
  "v_mul_f32 v208, v208, v209\n" \
  "v_mfma_f32_16x16x32_bf16 v[36:39], v[100:103], a[76:79], v[36:39]\n" \
  "s_mov_b32 m0, s47\n" \
  "v_mfma_f32_16x16x32_bf16 v[32:35], v[104:107], a[80:83], v[32:35]\n" \
  "buffer_load_dwordx4 v7, s[32:35], s61 offen sc1 lds\n" \
  "v_mfma_f32_16x16x32_bf16 v[36:39], v[104:107], a[84:87], v[36:39]\n" \
  "v_mul_f32_dpp v216, v208, v192 row_newbcast:0 row_mask:0xf bank_mask:0xf\n" \
  "v_mul_f32_dpp v217, v208, v192 row_newbcast:1 row_mask:0xf bank_mask:0xf\n" \
  "v_mfma_f32_16x16x32_bf16 v[32:35], v[108:111], a[88:91], v[32:35]\n" \
  "v_mul_f32_dpp v218, v208, v192 row_newbcast:2 row_mask:0xf bank_mask:0xf\n" \
  "v_mul_f32_dpp v219, v208, v192 row_newbcast:3 row_mask:0xf bank_mask:0xf\n" \
  "v_mfma_f32_16x16x32_bf16 v[36:39], v[108:111], a[92:95], v[36:39]\n" \
  "v_mul_f32_dpp v220, v208, v196 row_newbcast:0 row_mask:0xf bank_mask:0xf\n" \
  "v_mul_f32_dpp v221, v208, v196 row_newbcast:1 row_mask:0xf bank_mask:0xf\n" \
  "v_mfma_f32_16x16x32_bf16 v[32:35], v[112:115], a[96:99], v[32:35]\n" \
  "v_cmp_gt_i32 vcc, s63, v62\n" \
  "s_cmp_eq_u32 vcc_hi, 0\n" \
  "v_mul_f32_dpp v222, v208, v196 row_newbcast:2 row_mask:0xf bank_mask:0xf\n" \
  "v_mfma_f32_16x16x32_bf16 v[36:39], v[112:115], a[100:103], v[36:39]\n" \
  "s_cselect_b32 s72, s64, 0x40000000\n" \
  "v_mov_b32 v31, s72\n" \
  "v_mul_f32_dpp v223, v208, v196 row_newbcast:3 row_mask:0xf bank_mask:0xf\n" \
  "v_mfma_f32_16x16x32_bf16 v[32:35], v[116:119], a[104:107], v[32:35]\n" \
  "ds_write_b32 v15, v31\n" \
  "v_mul_f32_dpp v224, v208, v192 row_newbcast:4 row_mask:0xf bank_mask:0xf\n" \
  "v_mfma_f32_16x16x32_bf16 v[36:39], v[116:119], a[108:111], v[36:39]\n" \
  "v_mul_f32_dpp v225, v208, v192 row_newbcast:5 row_mask:0xf bank_mask:0xf\n" \
  "v_mul_f32_dpp v226, v208, v192 row_newbcast:6 row_mask:0xf bank_mask:0xf\n" \
  "v_mfma_f32_16x16x32_bf16 v[32:35], v[120:123], a[112:115], v[32:35]\n" \
  "v_mul_f32_dpp v227, v208, v192 row_newbcast:7 row_mask:0xf bank_mask:0xf\n" \
  "v_mul_f32_dpp v228, v208, v196 row_newbcast:4 row_mask:0xf bank_mask:0xf\n" \
  "v_mfma_f32_16x16x32_bf16 v[36:39], v[120:123], a[116:119], v[36:39]\n" \
  "v_mul_f32_dpp v229, v208, v196 row_newbcast:5 row_mask:0xf bank_mask:0xf\n" \
  "v_mul_f32_dpp v230, v208, v196 row_newbcast:6 row_mask:0xf bank_mask:0xf\n" \
  "v_mfma_f32_16x16x32_bf16 v[32:35], v[124:127], a[120:123], v[32:35]\n" \
  "v_mul_f32_dpp v231, v208, v196 row_newbcast:7 row_mask:0xf bank_mask:0xf\n" \
  "v_mfma_f32_16x16x32_bf16 v[36:39], v[124:127], a[124:127], v[36:39]\n" \
  "v_mfma_f32_16x16x32_bf16 v[40:43], v[128:131], a[0:3], 0\n" \
  "v_mfma_f32_16x16x32_bf16 v[44:47], v[128:131], a[4:7], 0\n" \
  "v_fmac_f32_dpp v216, v209, v32 row_newbcast:0 row_mask:0xf bank_mask:0xf\n" \
  "v_fmac_f32_dpp v217, v209, v33 row_newbcast:1 row_mask:0xf bank_mask:0xf\n" \
  "v_mfma_f32_16x16x32_bf16 v[40:43], v[132:135], a[8:11], v[40:43]\n" \
  "v_fmac_f32_dpp v218, v209, v34 row_newbcast:2 row_mask:0xf bank_mask:0xf\n" \
  "v_fmac_f32_dpp v219, v209, v35 row_newbcast:3 row_mask:0xf bank_mask:0xf\n" \
  "v_mfma_f32_16x16x32_bf16 v[44:47], v[132:135], a[12:15], v[44:47]\n" \
  "v_fmac_f32_dpp v220, v209, v36 row_newbcast:0 row_mask:0xf bank_mask:0xf\n" \
  "v_fmac_f32_dpp v221, v209, v37 row_newbcast:1 row_mask:0xf bank_mask:0xf\n" \
  "s_waitcnt lgkmcnt(14)\n" \
  "v_mfma_f32_16x16x32_bf16 v[40:43], v[136:139], a[16:19], v[40:43]\n" \
  "v_fmac_f32_dpp v222, v209, v38 row_newbcast:2 row_mask:0xf bank_mask:0xf\n" \
  "v_fmac_f32_dpp v223, v209, v39 row_newbcast:3 row_mask:0xf bank_mask:0xf\n" \
  "v_mfma_f32_16x16x32_bf16 v[44:47], v[136:139], a[20:23], v[44:47]\n" \
  "v_cvt_pk_bf16_f32 v48, v216, v217\n" \
  "v_cvt_pk_bf16_f32 v49, v218, v219\n" \
  "s_waitcnt lgkmcnt(13)\n" \
  "v_mfma_f32_16x16x32_bf16 v[40:43], v[140:143], a[24:27], v[40:43]\n" \
  "v_cvt_pk_bf16_f32 v52, v220, v221\n" \
  "v_cvt_pk_bf16_f32 v53, v222, v223\n" \
  "v_mfma_f32_16x16x32_bf16 v[44:47], v[140:143], a[28:31], v[44:47]\n" \
  "s_add_u32 s71, s66, 4096\n" \
  "s_waitcnt lgkmcnt(12)\n" \
  "v_mfma_f32_16x16x32_bf16 v[40:43], v[144:147], a[32:35], v[40:43]\n" \
  "v_mfma_f32_16x16x32_bf16 v[44:47], v[144:147], a[36:39], v[44:47]\n" \
  "s_waitcnt lgkmcnt(11)\n" \
  "v_mfma_f32_16x16x32_bf16 v[40:43], v[148:151], a[40:43], v[40:43]\n" \
  "v_mfma_f32_16x16x32_bf16 v[44:47], v[148:151], a[44:47], v[44:47]\n" \
  "s_waitcnt lgkmcnt(10)\n" \
  "v_mfma_f32_16x16x32_bf16 v[40:43], v[152:155], a[48:51], v[40:43]\n" \
  "v_mfma_f32_16x16x32_bf16 v[44:47], v[152:155], a[52:55], v[44:47]\n" \
  "s_waitcnt lgkmcnt(9)\n" \
  "v_mfma_f32_16x16x32_bf16 v[40:43], v[156:159], a[56:59], v[40:43]\n" \
  "v_mfma_f32_16x16x32_bf16 v[44:47], v[156:159], a[60:63], v[44:47]\n" \
  "s_waitcnt lgkmcnt(8)\n" \
  "v_mfma_f32_16x16x32_bf16 v[40:43], v[160:163], a[64:67], v[40:43]\n" \
  "v_mfma_f32_16x16x32_bf16 v[44:47], v[160:163], a[68:71], v[44:47]\n" \
  "s_waitcnt lgkmcnt(7)\n" \
  "v_mfma_f32_16x16x32_bf16 v[40:43], v[164:167], a[72:75], v[40:43]\n" \
  "v_mfma_f32_16x16x32_bf16 v[44:47], v[164:167], a[76:79], v[44:47]\n" \
  "s_waitcnt lgkmcnt(6)\n" \
  "v_mfma_f32_16x16x32_bf16 v[40:43], v[168:171], a[80:83], v[40:43]\n" \
  "v_mfma_f32_16x16x32_bf16 v[44:47], v[168:171], a[84:87], v[44:47]\n" \
  "s_waitcnt lgkmcnt(5)\n" \
  "v_mfma_f32_16x16x32_bf16 v[40:43], v[172:175], a[88:91], v[40:43]\n" \
  "v_mfma_f32_16x16x32_bf16 v[44:47], v[172:175], a[92:95], v[44:47]\n" \
  "s_waitcnt lgkmcnt(4)\n" \
  "v_mfma_f32_16x16x32_bf16 v[40:43], v[176:179], a[96:99], v[40:43]\n" \
  "v_mfma_f32_16x16x32_bf16 v[44:47], v[176:179], a[100:103], v[44:47]\n" \
  "s_waitcnt lgkmcnt(3)\n" \
  "v_mfma_f32_16x16x32_bf16 v[40:43], v[180:183], a[104:107], v[40:43]\n" \
  "v_mfma_f32_16x16x32_bf16 v[44:47], v[180:183], a[108:111], v[44:47]\n" \
  "s_waitcnt lgkmcnt(2)\n" \
  "v_mfma_f32_16x16x32_bf16 v[40:43], v[184:187], a[112:115], v[40:43]\n" \
  "v_mfma_f32_16x16x32_bf16 v[44:47], v[184:187], a[116:119], v[44:47]\n" \
  "s_waitcnt lgkmcnt(1)\n" \
  "v_mfma_f32_16x16x32_bf16 v[40:43], v[188:191], a[120:123], v[40:43]\n" \
  "v_mfma_f32_16x16x32_bf16 v[44:47], v[188:191], a[124:127], v[44:47]\n" \
  "s_nop 3\n" \
  "v_fmac_f32_dpp v224, v209, v40 row_newbcast:4 row_mask:0xf bank_mask:0xf\n" \
  "v_fmac_f32_dpp v225, v209, v41 row_newbcast:5 row_mask:0xf bank_mask:0xf\n" \
  "v_fmac_f32_dpp v226, v209, v42 row_newbcast:6 row_mask:0xf bank_mask:0xf\n" \
  "v_fmac_f32_dpp v227, v209, v43 row_newbcast:7 row_mask:0xf bank_mask:0xf\n" \
  "v_cvt_pk_bf16_f32 v50, v224, v225\n" \
  "v_cvt_pk_bf16_f32 v51, v226, v227\n" \
  "buffer_store_dwordx4 v[48:51], v12, s[36:39], s66 offen offset:0 sc1\n" \
  "v_fmac_f32_dpp v228, v209, v44 row_newbcast:4 row_mask:0xf bank_mask:0xf\n" \
  "v_fmac_f32_dpp v229, v209, v45 row_newbcast:5 row_mask:0xf bank_mask:0xf\n" \
  "v_fmac_f32_dpp v230, v209, v46 row_newbcast:6 row_mask:0xf bank_mask:0xf\n" \
  "v_fmac_f32_dpp v231, v209, v47 row_newbcast:7 row_mask:0xf bank_mask:0xf\n" \
  "v_cvt_pk_bf16_f32 v54, v228, v229\n" \
  "v_cvt_pk_bf16_f32 v55, v230, v231\n" \
  "buffer_store_dwordx4 v[52:55], v12, s[36:39], s71 offen sc1\n" \
  "s_cmp_ge_u32 s60, 64\n" \
  "s_cbranch_scc1 .Lv_last\n" \
  "s_waitcnt vmcnt(2) lgkmcnt(0)\n" \
  ".Lv_p:\n" \
  "s_barrier\n" \
  "ds_read_b128 v[64:67], v8 offset:0\n" \
  "ds_read_b32 v31, v28\n" \
  "ds_read_b128 v[68:71], v9 offset:0\n" \
  "ds_read_b128 v[72:75], v10 offset:0\n" \
  "ds_read_b128 v[76:79], v11 offset:0\n" \
  "s_waitcnt lgkmcnt(4)\n" \
  "v_mfma_f32_16x16x32_bf16 v[32:35], v[64:67], a[0:3], 0\n" \
  "ds_read_b128 v[80:83], v8 offset:256\n" \
  "v_mfma_f32_16x16x32_bf16 v[36:39], v[64:67], a[4:7], 0\n" \
  "s_waitcnt lgkmcnt(4)\n" \
  "v_readfirstlane_b32 s69, v31\n" \
  "s_waitcnt lgkmcnt(3)\n" \
  "v_mfma_f32_16x16x32_bf16 v[32:35], v[68:71], a[8:11], v[32:35]\n" \
  "ds_read_b128 v[84:87], v9 offset:256\n" \
  "s_add_u32 m0, s40, 0x8000\n" \
  "v_mfma_f32_16x16x32_bf16 v[36:39], v[68:71], a[12:15], v[36:39]\n" \
  "buffer_load_dwordx4 v0, s[52:55], s69 offen sc1 lds\n" \
  "s_waitcnt lgkmcnt(3)\n" \
  "v_mfma_f32_16x16x32_bf16 v[32:35], v[72:75], a[16:19], v[32:35]\n" \
  "ds_read_b128 v[88:91], v10 offset:256\n" \
  "v_add_f32 v208, v240, v242\n" \
  "v_mfma_f32_16x16x32_bf16 v[36:39], v[72:75], a[20:23], v[36:39]\n" \
  "v_add_f32 v209, v241, v243\n" \
  "v_add_f32 v210, v244, v246\n" \
  "s_waitcnt lgkmcnt(3)\n" \
  "v_mfma_f32_16x16x32_bf16 v[32:35], v[76:79], a[24:27], v[32:35]\n" \
  "ds_read_b128 v[92:95], v11 offset:256\n" \
  "s_add_u32 m0, s41, 0x8000\n" \
  "v_mfma_f32_16x16x32_bf16 v[36:39], v[76:79], a[28:31], v[36:39]\n" \
  "buffer_load_dwordx4 v1, s[52:55], s69 offen sc1 lds\n" \
  "s_waitcnt lgkmcnt(3)\n" \
  "v_mfma_f32_16x16x32_bf16 v[32:35], v[80:83], a[32:35], v[32:35]\n" \
  "ds_read_b128 v[96:99], v8 offset:512\n" \
  "v_add_f32 v211, v245, v247\n" \
  "v_mfma_f32_16x16x32_bf16 v[36:39], v[80:83], a[36:39], v[36:39]\n" \
  "v_add_f32 v208, v208, v210\n" \
  "v_add_f32 v209, v209, v211\n" \
  "s_waitcnt lgkmcnt(3)\n" \
  "v_mfma_f32_16x16x32_bf16 v[32:35], v[84:87], a[40:43], v[32:35]\n" \
  "ds_read_b128 v[100:103], v9 offset:512\n" \
  "s_add_u32 m0, s42, 0x8000\n" \
  "v_mfma_f32_16x16x32_bf16 v[36:39], v[84:87], a[44:47], v[36:39]\n" \
  "buffer_load_dwordx4 v2, s[52:55], s69 offen sc1 lds\n" \
  "s_waitcnt lgkmcnt(3)\n" \
  "v_mfma_f32_16x16x32_bf16 v[32:35], v[88:91], a[48:51], v[32:35]\n" \
  "ds_read_b128 v[104:107], v10 offset:512\n" \
  "v_add_f32_dpp v208, v208, v208 row_ror:8 row_mask:0xf bank_mask:0xf\n" \
  "v_mfma_f32_16x16x32_bf16 v[36:39], v[88:91], a[52:55], v[36:39]\n" \
  "v_add_f32_dpp v209, v209, v209 row_ror:8 row_mask:0xf bank_mask:0xf\n" \
  "v_mul_f32 v208, 0xbb000000, v208\n" \
  "s_waitcnt lgkmcnt(3)\n" \
  "v_mfma_f32_16x16x32_bf16 v[32:35], v[92:95], a[56:59], v[32:35]\n" \
  "ds_read_b128 v[108:111], v11 offset:512\n" \
  "s_add_u32 m0, s43, 0x8000\n" \
  "v_mfma_f32_16x16x32_bf16 v[36:39], v[92:95], a[60:63], v[36:39]\n" \
  "buffer_load_dwordx4 v3, s[52:55], s69 offen sc1 lds\n" \
  "s_waitcnt lgkmcnt(3)\n" \
  "v_mfma_f32_16x16x32_bf16 v[32:35], v[96:99], a[64:67], v[32:35]\n" \
  "ds_read_b128 v[112:115], v8 offset:768\n" \
  "v_fmamk_f32 v212, v209, 0x3b000000, v234\n" \
  "v_mfma_f32_16x16x32_bf16 v[36:39], v[96:99], a[68:71], v[36:39]\n" \
  "v_fma_f32 v212, -v208, v208, v212\n" \
  "v_max_f32 v212, v234, v212\n" \
  "s_waitcnt lgkmcnt(3)\n" \
  "v_mfma_f32_16x16x32_bf16 v[32:35], v[100:103], a[72:75], v[32:35]\n" \
  "ds_read_b128 v[116:119], v9 offset:768\n" \
  "s_add_u32 m0, s44, 0x8000\n" \
  "v_mfma_f32_16x16x32_bf16 v[36:39], v[100:103], a[76:79], v[36:39]\n" \
  "buffer_load_dwordx4 v4, s[52:55], s69 offen sc1 lds\n" \
  "s_waitcnt lgkmcnt(3)\n" \
  "v_mfma_f32_16x16x32_bf16 v[32:35], v[104:107], a[80:83], v[32:35]\n" \
  "ds_read_b128 v[120:123], v10 offset:768\n" \
  "v_mfma_f32_16x16x32_bf16 v[36:39], v[104:107], a[84:87], v[36:39]\n" \
  "v_rsq_f32 v209, v212\n" \
  "s_waitcnt lgkmcnt(3)\n" \
  "v_mfma_f32_16x16x32_bf16 v[32:35], v[108:111], a[88:91], v[32:35]\n" \
  "ds_read_b128 v[124:127], v11 offset:768\n" \
  "s_add_u32 m0, s45, 0x8000\n" \
  "v_mfma_f32_16x16x32_bf16 v[36:39], v[108:111], a[92:95], v[36:39]\n" \
  "buffer_load_dwordx4 v5, s[52:55], s69 offen sc1 lds\n" \
  "s_waitcnt lgkmcnt(3)\n" \
  "v_mfma_f32_16x16x32_bf16 v[32:35], v[112:115], a[96:99], v[32:35]\n" \
  "ds_read_b128 v[128:131], v8 offset:16384\n" \
  "v_mul_f32 v208, v208, v209\n" \
  "v_mfma_f32_16x16x32_bf16 v[36:39], v[112:115], a[100:103], v[36:39]\n" \
  "s_waitcnt lgkmcnt(3)\n" \
  "v_mfma_f32_16x16x32_bf16 v[32:35], v[116:119], a[104:107], v[32:35]\n" \
  "ds_read_b128 v[132:135], v9 offset:16384\n" \
  "s_add_u32 m0, s46, 0x8000\n" \
  "v_mfma_f32_16x16x32_bf16 v[36:39], v[116:119], a[108:111], v[36:39]\n" \
  "buffer_load_dwordx4 v6, s[52:55], s69 offen sc1 lds\n" \
  "s_waitcnt lgkmcnt(3)\n" \
  "v_mfma_f32_16x16x32_bf16 v[32:35], v[120:123], a[112:115], v[32:35]\n" \
  "ds_read_b128 v[136:139], v10 offset:16384\n" \
  "v_mul_f32_dpp v216, v208, v192 row_newbcast:0 row_mask:0xf bank_mask:0xf\n" \
  "v_mfma_f32_16x16x32_bf16 v[36:39], v[120:123], a[116:119], v[36:39]\n" \
  "v_mul_f32_dpp v217, v208, v192 row_newbcast:1 row_mask:0xf bank_mask:0xf\n" \
  "v_mul_f32_dpp v218, v208, v192 row_newbcast:2 row_mask:0xf bank_mask:0xf\n" \
  "s_waitcnt lgkmcnt(3)\n" \
  "v_mfma_f32_16x16x32_bf16 v[32:35], v[124:127], a[120:123], v[32:35]\n" \
  "ds_read_b128 v[140:143], v11 offset:16384\n" \
  "s_add_u32 m0, s47, 0x8000\n" \
  "v_mfma_f32_16x16x32_bf16 v[36:39], v[124:127], a[124:127], v[36:39]\n" \
  "buffer_load_dwordx4 v7, s[52:55], s69 offen sc1 lds\n" \
  "s_waitcnt lgkmcnt(3)\n" \
  "v_mfma_f32_16x16x32_bf16 v[40:43], v[128:131], a[0:3], 0\n" \
  "ds_read_b128 v[144:147], v8 offset:16640\n" \
  "v_mul_f32_dpp v219, v208, v192 row_newbcast:3 row_mask:0xf bank_mask:0xf\n" \
  "v_mfma_f32_16x16x32_bf16 v[44:47], v[128:131], a[4:7], 0\n" \
  "buffer_load_dwordx2 v[248:249], v233, s[36:39], s69 offen offset:0 sc1\n" \
  "s_waitcnt lgkmcnt(3)\n" \
  "v_mfma_f32_16x16x32_bf16 v[40:43], v[132:135], a[8:11], v[40:43]\n" \
  "ds_read_b128 v[148:151], v9 offset:16640\n" \
  "v_mul_f32_dpp v220, v208, v196 row_newbcast:0 row_mask:0xf bank_mask:0xf\n" \
  "v_mfma_f32_16x16x32_bf16 v[44:47], v[132:135], a[12:15], v[44:47]\n" \
  "buffer_load_dwordx2 v[250:251], v233, s[36:39], s69 offen offset:128 sc1\n" \
  "s_waitcnt lgkmcnt(3)\n" \
  "v_mfma_f32_16x16x32_bf16 v[40:43], v[136:139], a[16:19], v[40:43]\n" \
  "ds_read_b128 v[152:155], v10 offset:16640\n" \
  "v_mul_f32_dpp v221, v208, v196 row_newbcast:1 row_mask:0xf bank_mask:0xf\n" \
  "v_mfma_f32_16x16x32_bf16 v[44:47], v[136:139], a[20:23], v[44:47]\n" \
  "buffer_load_dwordx2 v[252:253], v233, s[36:39], s69 offen offset:256 sc1\n" \
  "s_waitcnt lgkmcnt(3)\n" \
  "v_mfma_f32_16x16x32_bf16 v[40:43], v[140:143], a[24:27], v[40:43]\n" \
  "ds_read_b128 v[156:159], v11 offset:16640\n" \
  "v_mul_f32_dpp v222, v208, v196 row_newbcast:2 row_mask:0xf bank_mask:0xf\n" \
  "v_mfma_f32_16x16x32_bf16 v[44:47], v[140:143], a[28:31], v[44:47]\n" \
  "buffer_load_dwordx2 v[254:255], v233, s[36:39], s69 offen offset:384 sc1\n" \
  "s_waitcnt lgkmcnt(3)\n" \
  "v_mfma_f32_16x16x32_bf16 v[40:43], v[144:147], a[32:35], v[40:43]\n" \
  "ds_read_b128 v[160:163], v8 offset:16896\n" \
  "v_mul_f32_dpp v223, v208, v196 row_newbcast:3 row_mask:0xf bank_mask:0xf\n" \
  "v_mfma_f32_16x16x32_bf16 v[44:47], v[144:147], a[36:39], v[44:47]\n" \
  "v_mul_f32_dpp v224, v208, v192 row_newbcast:4 row_mask:0xf bank_mask:0xf\n" \
  "v_mul_f32_dpp v225, v208, v192 row_newbcast:5 row_mask:0xf bank_mask:0xf\n" \
  "s_waitcnt lgkmcnt(3)\n" \
  "v_mfma_f32_16x16x32_bf16 v[40:43], v[148:151], a[40:43], v[40:43]\n" \
  "ds_read_b128 v[164:167], v9 offset:16896\n" \
  "v_mul_f32_dpp v226, v208, v192 row_newbcast:6 row_mask:0xf bank_mask:0xf\n" \
  "v_mfma_f32_16x16x32_bf16 v[44:47], v[148:151], a[44:47], v[44:47]\n" \
  "v_mul_f32_dpp v227, v208, v192 row_newbcast:7 row_mask:0xf bank_mask:0xf\n" \
  "v_mul_f32_dpp v228, v208, v196 row_newbcast:4 row_mask:0xf bank_mask:0xf\n" \
  "s_waitcnt lgkmcnt(3)\n" \
  "v_mfma_f32_16x16x32_bf16 v[40:43], v[152:155], a[48:51], v[40:43]\n" \
  "ds_read_b128 v[168:171], v10 offset:16896\n" \
  "v_mul_f32_dpp v229, v208, v196 row_newbcast:5 row_mask:0xf bank_mask:0xf\n" \
  "v_mfma_f32_16x16x32_bf16 v[44:47], v[152:155], a[52:55], v[44:47]\n" \
  "v_mul_f32_dpp v230, v208, v196 row_newbcast:6 row_mask:0xf bank_mask:0xf\n" \
  "v_mul_f32_dpp v231, v208, v196 row_newbcast:7 row_mask:0xf bank_mask:0xf\n" \
  "s_waitcnt lgkmcnt(3)\n" \
  "v_mfma_f32_16x16x32_bf16 v[40:43], v[156:159], a[56:59], v[40:43]\n" \
  "ds_read_b128 v[172:175], v11 offset:16896\n" \
  "v_fmac_f32_dpp v216, v209, v32 row_newbcast:0 row_mask:0xf bank_mask:0xf\n" \
  "v_mfma_f32_16x16x32_bf16 v[44:47], v[156:159], a[60:63], v[44:47]\n" \
  "v_fmac_f32_dpp v217, v209, v33 row_newbcast:1 row_mask:0xf bank_mask:0xf\n" \
  "v_fmac_f32_dpp v218, v209, v34 row_newbcast:2 row_mask:0xf bank_mask:0xf\n" \
  "s_waitcnt lgkmcnt(3)\n" \
  "v_mfma_f32_16x16x32_bf16 v[40:43], v[160:163], a[64:67], v[40:43]\n" \
  "ds_read_b128 v[176:179], v8 offset:17152\n" \
  "v_fmac_f32_dpp v219, v209, v35 row_newbcast:3 row_mask:0xf bank_mask:0xf\n" \
  "v_mfma_f32_16x16x32_bf16 v[44:47], v[160:163], a[68:71], v[44:47]\n" \
  "v_fmac_f32_dpp v220, v209, v36 row_newbcast:0 row_mask:0xf bank_mask:0xf\n" \
  "v_fmac_f32_dpp v221, v209, v37 row_newbcast:1 row_mask:0xf bank_mask:0xf\n" \
  "s_waitcnt lgkmcnt(3)\n" \
  "v_mfma_f32_16x16x32_bf16 v[40:43], v[164:167], a[72:75], v[40:43]\n" \
  "ds_read_b128 v[180:183], v9 offset:17152\n" \
  "v_fmac_f32_dpp v222, v209, v38 row_newbcast:2 row_mask:0xf bank_mask:0xf\n" \
  "v_mfma_f32_16x16x32_bf16 v[44:47], v[164:167], a[76:79], v[44:47]\n" \
  "v_fmac_f32_dpp v223, v209, v39 row_newbcast:3 row_mask:0xf bank_mask:0xf\n" \
  "v_cvt_pk_bf16_f32 v48, v216, v217\n" \
  "s_waitcnt lgkmcnt(3)\n" \
  "v_mfma_f32_16x16x32_bf16 v[40:43], v[168:171], a[80:83], v[40:43]\n" \
  "ds_read_b128 v[184:187], v10 offset:17152\n" \
  "v_cvt_pk_bf16_f32 v49, v218, v219\n" \
  "v_mfma_f32_16x16x32_bf16 v[44:47], v[168:171], a[84:87], v[44:47]\n" \
  "v_cvt_pk_bf16_f32 v52, v220, v221\n" \
  "v_cvt_pk_bf16_f32 v53, v222, v223\n" \
  "s_waitcnt lgkmcnt(3)\n" \
  "v_mfma_f32_16x16x32_bf16 v[40:43], v[172:175], a[88:91], v[40:43]\n" \
  "ds_read_b128 v[188:191], v11 offset:17152\n" \
  "v_mfma_f32_16x16x32_bf16 v[44:47], v[172:175], a[92:95], v[44:47]\n" \
  "s_add_u32 s71, s62, 4096\n" \
  "s_waitcnt lgkmcnt(3)\n" \
  "v_mfma_f32_16x16x32_bf16 v[40:43], v[176:179], a[96:99], v[40:43]\n" \
  "v_mfma_f32_16x16x32_bf16 v[44:47], v[176:179], a[100:103], v[44:47]\n" \
  "s_waitcnt lgkmcnt(2)\n" \
  "v_mfma_f32_16x16x32_bf16 v[40:43], v[180:183], a[104:107], v[40:43]\n" \
  "v_mfma_f32_16x16x32_bf16 v[44:47], v[180:183], a[108:111], v[44:47]\n" \
  "s_waitcnt lgkmcnt(1)\n" \
  "v_mfma_f32_16x16x32_bf16 v[40:43], v[184:187], a[112:115], v[40:43]\n" \
  "v_mfma_f32_16x16x32_bf16 v[44:47], v[184:187], a[116:119], v[44:47]\n" \
  "s_waitcnt lgkmcnt(0)\n" \
  "v_mfma_f32_16x16x32_bf16 v[40:43], v[188:191], a[120:123], v[40:43]\n" \
  "v_mfma_f32_16x16x32_bf16 v[44:47], v[188:191], a[124:127], v[44:47]\n" \
  "s_nop 3\n" \
  "v_fmac_f32_dpp v224, v209, v40 row_newbcast:4 row_mask:0xf bank_mask:0xf\n" \
  "v_fmac_f32_dpp v225, v209, v41 row_newbcast:5 row_mask:0xf bank_mask:0xf\n" \
  "v_fmac_f32_dpp v226, v209, v42 row_newbcast:6 row_mask:0xf bank_mask:0xf\n" \
  "v_fmac_f32_dpp v227, v209, v43 row_newbcast:7 row_mask:0xf bank_mask:0xf\n" \
  "v_cvt_pk_bf16_f32 v50, v224, v225\n" \
  "v_cvt_pk_bf16_f32 v51, v226, v227\n" \
  "buffer_store_dwordx4 v[48:51], v12, s[36:39], s62 offen offset:0 sc1\n" \
  "v_fmac_f32_dpp v228, v209, v44 row_newbcast:4 row_mask:0xf bank_mask:0xf\n" \
  "v_fmac_f32_dpp v229, v209, v45 row_newbcast:5 row_mask:0xf bank_mask:0xf\n" \
  "v_fmac_f32_dpp v230, v209, v46 row_newbcast:6 row_mask:0xf bank_mask:0xf\n" \
  "v_fmac_f32_dpp v231, v209, v47 row_newbcast:7 row_mask:0xf bank_mask:0xf\n" \
  "v_cvt_pk_bf16_f32 v54, v228, v229\n" \
  "v_cvt_pk_bf16_f32 v55, v230, v231\n" \
  "buffer_store_dwordx4 v[52:55], v12, s[36:39], s71 offen sc1\n" \
  ".Lv_sel:\n" \
  "s_mov_b32 s76, s59\n" \
  "s_cmp_lg_u32 s69, 0x40000000\n" \
  "s_addc_u32 s59, s59, 0\n" \
  "s_sub_u32 s71, s59, s58\n" \
  "s_cmp_lt_u32 s59, 64\n" \
  "s_cselect_b32 s71, s71, 64\n" \
  "s_cmp_ge_i32 s71, s50\n" \
  "s_cselect_b64 s[78:79], -1, 0\n" \
  "v_cndmask_b32_e64 v30, v14, v13, s[78:79]\n" \
  "buffer_load_dword v62, v30, s[36:39], 0 offen sc1\n" \
  "s_cselect_b32 s60, s58, s59\n" \
  "s_cselect_b64 s[32:33], s[36:37], s[52:53]\n" \
  "s_cselect_b32 s34, s38, s54\n" \
  "v_cndmask_b32_e64 v235, v233, v232, s[78:79]\n" \
  "s_cselect_b32 s75, s48, s49\n" \
  "s_cselect_b32 s71, 0, 1\n" \
  "v_cndmask_b32_e64 v32, v17, v16, s[78:79]\n" \
  "v_cndmask_b32_e64 v33, v19, v18, s[78:79]\n" \
  "s_addc_u32 s58, s58, 0\n" \
  "s_add_u32 s59, s59, s71\n" \
  "s_mov_b32 s66, s65\n" \
  "v_readlane_b32 s61, v32, s60\n" \
  "v_readlane_b32 s62, v33, s60\n" \
  "v_readlane_b32 s64, v17, s59\n" \
  "v_readlane_b32 s65, v19, s59\n" \
  "s_add_u32 s63, s59, s71\n" \
  "s_cmp_eq_u32 s59, 63\n" \
  "s_cselect_b32 s63, 63, s63\n" \
  "s_cmp_lt_u32 s60, 64\n" \
  "s_cselect_b32 s77, s60, -1\n" \
  "s_cselect_b32 s61, s61, 0x40000000\n" \
  "s_cselect_b32 s75, s75, 0x40000000\n" \
  "s_cselect_b32 s71, s71, 0\n" \
  "s_cmp_eq_u32 s71, 0\n" \
  "s_cselect_b32 s72, s49, 0x40000000\n" \
  "s_waitcnt vmcnt(1)\n" \
  "s_barrier\n" \
  "buffer_store_dword v60, v29, s[36:39], s67 offen sc1\n" \
  "buffer_store_dword v61, v29, s[36:39], s68 offen sc1\n" \
  "s_waitcnt vmcnt(2)\n" \
  "v_mov_b32 v60, s60\n" \
  "v_mov_b32 v61, s76\n" \
  "s_mov_b32 s67, s75\n" \
  "s_mov_b32 s68, s72\n" \
  "s_cmp_lg_u32 s69, 0x40000000\n" \
  "s_cbranch_scc1 .Lv_q\n" \
  "s_mov_b32 s68, 0x40000000\n" \
  "s_cmp_ge_u32 s60, 64\n" \
  "s_cbranch_scc1 .Lv_end\n" \
  "s_mov_b32 m0, s40\n" \
  "s_branch .Lv_pn_c\n" \
  ".Lv_pn:\n" \
  ".Lv_pn_f:\n" \
  "buffer_load_dword v62, v30, s[36:39], 0 offen sc1\n" \
  "s_sub_u32 s70, s70, 1\n" \
  "s_cselect_b32 s77, -1, s77\n" \
  "s_cselect_b32 s70, 0, s70\n" \
  "s_waitcnt vmcnt(0)\n" \
  ".Lv_pn_c:\n" \
  "v_cmp_gt_i32 vcc, s77, v62\n" \
  "s_and_b32 vcc_lo, vcc_lo, s29\n" \
  "s_cbranch_scc1 .Lv_pn\n" \
  "s_barrier\n" \
  "buffer_load_dwordx4 v0, s[32:35], s61 offen sc1 lds\n" \
  "s_mov_b32 m0, s41\n" \
  "v_cmp_gt_i32 vcc, s63, v62\n" \
  "buffer_load_dwordx4 v1, s[32:35], s61 offen sc1 lds\n" \
  "s_mov_b32 m0, s42\n" \
  "s_cmp_eq_u32 vcc_hi, 0\n" \
  "buffer_load_dwordx4 v2, s[32:35], s61 offen sc1 lds\n" \
  "s_mov_b32 m0, s43\n" \
  "s_cselect_b32 s72, s64, 0x40000000\n" \
  "buffer_load_dwordx4 v3, s[32:35], s61 offen sc1 lds\n" \
  "s_mov_b32 m0, s44\n" \
  "v_mov_b32 v31, s72\n" \
  "buffer_load_dwordx4 v4, s[32:35], s61 offen sc1 lds\n" \
  "s_mov_b32 m0, s45\n" \
  "ds_write_b32 v15, v31\n" \
  "buffer_load_dwordx4 v5, s[32:35], s61 offen sc1 lds\n" \
  "s_mov_b32 m0, s46\n" \
  "s_nop 0\n" \
  "buffer_load_dwordx4 v6, s[32:35], s61 offen sc1 lds\n" \
  "s_mov_b32 m0, s47\n" \
  "s_nop 0\n" \
  "buffer_load_dwordx4 v7, s[32:35], s61 offen sc1 lds\n" \
  "buffer_load_dwordx2 v[240:241], v235, s[36:39], s61 offen offset:0 sc1\n" \
  "buffer_load_dwordx2 v[242:243], v235, s[36:39], s61 offen offset:128 sc1\n" \
  "buffer_load_dwordx2 v[244:245], v235, s[36:39], s61 offen offset:256 sc1\n" \
  "buffer_load_dwordx2 v[246:247], v235, s[36:39], s61 offen offset:384 sc1\n" \
  "s_waitcnt vmcnt(0) lgkmcnt(0)\n" \
  "s_branch .Lv_p\n" \
  ".Lv_last:\n" \
  "s_mov_b32 s69, 0x40000000\n" \
  "s_branch .Lv_sel\n" \
  ".Lv_end:\n" \
  "s_endpgm\n"
#define ASM_WO \
  "s_nop 4\n" \
  "s_mov_b32 s58, 0\n" \
  "s_mov_b32 s59, 0\n" \
  "s_mov_b32 s70, 100000\n" \
  "s_mov_b32 s69, 0x40000000\n" \
  "s_mov_b32 s67, 0x40000000\n" \
  "s_mov_b32 s68, 0x40000000\n" \
  "v_mov_b32 v60, 0\n" \
  "v_mov_b32 v61, 0\n" \
  "v_cmp_eq_u32 vcc, v15, v28\n" \
  "s_mov_b32 s29, vcc_lo\n" \
  "s_mov_b32 s73, 0x05040100\n" \
  "s_mov_b32 s74, 0x07060302\n" \
  "s_mov_b32 s71, 0\n" \
  "s_mov_b32 s72, 16384\n" \
  "buffer_load_dwordx2 v[64:65], v20, s[52:55], s71 offen offset:0\n" \
  "buffer_load_dwordx2 v[66:67], v20, s[52:55], s72 offen offset:0\n" \
  "buffer_load_dwordx2 v[68:69], v20, s[52:55], s71 offen offset:1024\n" \
  "buffer_load_dwordx2 v[70:71], v20, s[52:55], s72 offen offset:1024\n" \
  "buffer_load_dwordx2 v[72:73], v20, s[52:55], s71 offen offset:2048\n" \
  "buffer_load_dwordx2 v[74:75], v20, s[52:55], s72 offen offset:2048\n" \
  "buffer_load_dwordx2 v[76:77], v20, s[52:55], s71 offen offset:3072\n" \
  "buffer_load_dwordx2 v[78:79], v20, s[52:55], s72 offen offset:3072\n" \
  "s_mov_b32 s71, 32768\n" \
  "s_mov_b32 s72, 49152\n" \
  "buffer_load_dwordx2 v[80:81], v20, s[52:55], s71 offen offset:0\n" \
  "buffer_load_dwordx2 v[82:83], v20, s[52:55], s72 offen offset:0\n" \
  "buffer_load_dwordx2 v[84:85], v20, s[52:55], s71 offen offset:1024\n" \
  "buffer_load_dwordx2 v[86:87], v20, s[52:55], s72 offen offset:1024\n" \
  "buffer_load_dwordx2 v[88:89], v20, s[52:55], s71 offen offset:2048\n" \
  "buffer_load_dwordx2 v[90:91], v20, s[52:55], s72 offen offset:2048\n" \
  "buffer_load_dwordx2 v[92:93], v20, s[52:55], s71 offen offset:3072\n" \
  "buffer_load_dwordx2 v[94:95], v20, s[52:55], s72 offen offset:3072\n" \
  "s_mov_b32 s71, 65536\n" \
  "s_mov_b32 s72, 81920\n" \
  "buffer_load_dwordx2 v[96:97], v20, s[52:55], s71 offen offset:0\n" \
  "buffer_load_dwordx2 v[98:99], v20, s[52:55], s72 offen offset:0\n" \
  "buffer_load_dwordx2 v[100:101], v20, s[52:55], s71 offen offset:1024\n" \
  "buffer_load_dwordx2 v[102:103], v20, s[52:55], s72 offen offset:1024\n" \
  "buffer_load_dwordx2 v[104:105], v20, s[52:55], s71 offen offset:2048\n" \
  "buffer_load_dwordx2 v[106:107], v20, s[52:55], s72 offen offset:2048\n" \
  "buffer_load_dwordx2 v[108:109], v20, s[52:55], s71 offen offset:3072\n" \
  "buffer_load_dwordx2 v[110:111], v20, s[52:55], s72 offen offset:3072\n" \
  "s_mov_b32 s71, 98304\n" \
  "s_mov_b32 s72, 114688\n" \
  "buffer_load_dwordx2 v[112:113], v20, s[52:55], s71 offen offset:0\n" \
  "buffer_load_dwordx2 v[114:115], v20, s[52:55], s72 offen offset:0\n" \
  "buffer_load_dwordx2 v[116:117], v20, s[52:55], s71 offen offset:1024\n" \
  "buffer_load_dwordx2 v[118:119], v20, s[52:55], s72 offen offset:1024\n" \
  "buffer_load_dwordx2 v[120:121], v20, s[52:55], s71 offen offset:2048\n" \
  "buffer_load_dwordx2 v[122:123], v20, s[52:55], s72 offen offset:2048\n" \
  "buffer_load_dwordx2 v[124:125], v20, s[52:55], s71 offen offset:3072\n" \
  "buffer_load_dwordx2 v[126:127], v20, s[52:55], s72 offen offset:3072\n" \
  "s_mov_b32 s71, 131072\n" \
  "s_mov_b32 s72, 147456\n" \
  "buffer_load_dwordx2 v[128:129], v20, s[52:55], s71 offen offset:0\n" \
  "buffer_load_dwordx2 v[130:131], v20, s[52:55], s72 offen offset:0\n" \
  "buffer_load_dwordx2 v[132:133], v20, s[52:55], s71 offen offset:1024\n" \
  "buffer_load_dwordx2 v[134:135], v20, s[52:55], s72 offen offset:1024\n" \
  "buffer_load_dwordx2 v[136:137], v20, s[52:55], s71 offen offset:2048\n" \
  "buffer_load_dwordx2 v[138:139], v20, s[52:55], s72 offen offset:2048\n" \
  "buffer_load_dwordx2 v[140:141], v20, s[52:55], s71 offen offset:3072\n" \
  "buffer_load_dwordx2 v[142:143], v20, s[52:55], s72 offen offset:3072\n" \
  "s_mov_b32 s71, 163840\n" \
  "s_mov_b32 s72, 180224\n" \
  "buffer_load_dwordx2 v[144:145], v20, s[52:55], s71 offen offset:0\n" \
  "buffer_load_dwordx2 v[146:147], v20, s[52:55], s72 offen offset:0\n" \
  "buffer_load_dwordx2 v[148:149], v20, s[52:55], s71 offen offset:1024\n" \
  "buffer_load_dwordx2 v[150:151], v20, s[52:55], s72 offen offset:1024\n" \
  "buffer_load_dwordx2 v[152:153], v20, s[52:55], s71 offen offset:2048\n" \
  "buffer_load_dwordx2 v[154:155], v20, s[52:55], s72 offen offset:2048\n" \
  "buffer_load_dwordx2 v[156:157], v20, s[52:55], s71 offen offset:3072\n" \
  "buffer_load_dwordx2 v[158:159], v20, s[52:55], s72 offen offset:3072\n" \
  "s_mov_b32 s71, 196608\n" \
  "s_mov_b32 s72, 212992\n" \
  "buffer_load_dwordx2 v[160:161], v20, s[52:55], s71 offen offset:0\n" \
  "buffer_load_dwordx2 v[162:163], v20, s[52:55], s72 offen offset:0\n" \
  "buffer_load_dwordx2 v[164:165], v20, s[52:55], s71 offen offset:1024\n" \
  "buffer_load_dwordx2 v[166:167], v20, s[52:55], s72 offen offset:1024\n" \
  "buffer_load_dwordx2 v[168:169], v20, s[52:55], s71 offen offset:2048\n" \
  "buffer_load_dwordx2 v[170:171], v20, s[52:55], s72 offen offset:2048\n" \
  "buffer_load_dwordx2 v[172:173], v20, s[52:55], s71 offen offset:3072\n" \
  "buffer_load_dwordx2 v[174:175], v20, s[52:55], s72 offen offset:3072\n" \
  "s_waitcnt vmcnt(54)\n" \
  "v_perm_b32 v32, v66, v64, s73\n" \
  "s_waitcnt vmcnt(52)\n" \
  "v_perm_b32 v33, v70, v68, s73\n" \
  "s_waitcnt vmcnt(50)\n" \
  "v_perm_b32 v34, v74, v72, s73\n" \
  "s_waitcnt vmcnt(48)\n" \
  "v_perm_b32 v35, v78, v76, s73\n" \
  "v_perm_b32 v36, v66, v64, s74\n" \
  "v_perm_b32 v37, v70, v68, s74\n" \
  "v_perm_b32 v38, v74, v72, s74\n" \
  "v_perm_b32 v39, v78, v76, s74\n" \
  "v_perm_b32 v40, v67, v65, s73\n" \
  "v_perm_b32 v41, v71, v69, s73\n" \
  "v_perm_b32 v42, v75, v73, s73\n" \
  "v_perm_b32 v43, v79, v77, s73\n" \
  "v_perm_b32 v44, v67, v65, s74\n" \
  "v_perm_b32 v45, v71, v69, s74\n" \
  "v_perm_b32 v46, v75, v73, s74\n" \
  "v_perm_b32 v47, v79, v77, s74\n" \
  "v_accvgpr_write_b32 a0, v32\n" \
  "v_accvgpr_write_b32 a1, v33\n" \
  "v_accvgpr_write_b32 a2, v34\n" \
  "v_accvgpr_write_b32 a3, v35\n" \
  "v_accvgpr_write_b32 a4, v36\n" \
  "v_accvgpr_write_b32 a5, v37\n" \
  "v_accvgpr_write_b32 a6, v38\n" \
  "v_accvgpr_write_b32 a7, v39\n" \
  "v_accvgpr_write_b32 a8, v40\n" \
  "v_accvgpr_write_b32 a9, v41\n" \
  "v_accvgpr_write_b32 a10, v42\n" \
  "v_accvgpr_write_b32 a11, v43\n" \
  "v_accvgpr_write_b32 a12, v44\n" \
  "v_accvgpr_write_b32 a13, v45\n" \
  "v_accvgpr_write_b32 a14, v46\n" \
  "v_accvgpr_write_b32 a15, v47\n" \
  "s_mov_b32 s71, 229376\n" \
  "s_mov_b32 s72, 245760\n" \
  "buffer_load_dwordx2 v[64:65], v20, s[52:55], s71 offen offset:0\n" \
  "buffer_load_dwordx2 v[66:67], v20, s[52:55], s72 offen offset:0\n" \
  "buffer_load_dwordx2 v[68:69], v20, s[52:55], s71 offen offset:1024\n" \
  "buffer_load_dwordx2 v[70:71], v20, s[52:55], s72 offen offset:1024\n" \
  "buffer_load_dwordx2 v[72:73], v20, s[52:55], s71 offen offset:2048\n" \
  "buffer_load_dwordx2 v[74:75], v20, s[52:55], s72 offen offset:2048\n" \
  "buffer_load_dwordx2 v[76:77], v20, s[52:55], s71 offen offset:3072\n" \
  "buffer_load_dwordx2 v[78:79], v20, s[52:55], s72 offen offset:3072\n" \
  "s_waitcnt vmcnt(54)\n" \
  "v_perm_b32 v32, v82, v80, s73\n" \
  "s_waitcnt vmcnt(52)\n" \
  "v_perm_b32 v33, v86, v84, s73\n" \
  "s_waitcnt vmcnt(50)\n" \
  "v_perm_b32 v34, v90, v88, s73\n" \
  "s_waitcnt vmcnt(48)\n" \
  "v_perm_b32 v35, v94, v92, s73\n" \
  "v_perm_b32 v36, v82, v80, s74\n" \
  "v_perm_b32 v37, v86, v84, s74\n" \
  "v_perm_b32 v38, v90, v88, s74\n" \
  "v_perm_b32 v39, v94, v92, s74\n" \
  "v_perm_b32 v40, v83, v81, s73\n" \
  "v_perm_b32 v41, v87, v85, s73\n" \
  "v_perm_b32 v42, v91, v89, s73\n" \
  "v_perm_b32 v43, v95, v93, s73\n" \
  "v_perm_b32 v44, v83, v81, s74\n" \
  "v_perm_b32 v45, v87, v85, s74\n" \
  "v_perm_b32 v46, v91, v89, s74\n" \
  "v_perm_b32 v47, v95, v93, s74\n" \
  "v_accvgpr_write_b32 a16, v32\n" \
  "v_accvgpr_write_b32 a17, v33\n" \
  "v_accvgpr_write_b32 a18, v34\n" \
  "v_accvgpr_write_b32 a19, v35\n" \
  "v_accvgpr_write_b32 a20, v36\n" \
  "v_accvgpr_write_b32 a21, v37\n" \
  "v_accvgpr_write_b32 a22, v38\n" \
  "v_accvgpr_write_b32 a23, v39\n" \
  "v_accvgpr_write_b32 a24, v40\n" \
  "v_accvgpr_write_b32 a25, v41\n" \
  "v_accvgpr_write_b32 a26, v42\n" \
  "v_accvgpr_write_b32 a27, v43\n" \
  "v_accvgpr_write_b32 a28, v44\n" \
  "v_accvgpr_write_b32 a29, v45\n" \
  "v_accvgpr_write_b32 a30, v46\n" \
  "v_accvgpr_write_b32 a31, v47\n" \
  "s_mov_b32 s71, 262144\n" \
  "s_mov_b32 s72, 278528\n" \
  "buffer_load_dwordx2 v[80:81], v20, s[52:55], s71 offen offset:0\n" \
  "buffer_load_dwordx2 v[82:83], v20, s[52:55], s72 offen offset:0\n" \
  "buffer_load_dwordx2 v[84:85], v20, s[52:55], s71 offen offset:1024\n" \
  "buffer_load_dwordx2 v[86:87], v20, s[52:55], s72 offen offset:1024\n" \
  "buffer_load_dwordx2 v[88:89], v20, s[52:55], s71 offen offset:2048\n" \
  "buffer_load_dwordx2 v[90:91], v20, s[52:55], s72 offen offset:2048\n" \
  "buffer_load_dwordx2 v[92:93], v20, s[52:55], s71 offen offset:3072\n" \
  "buffer_load_dwordx2 v[94:95], v20, s[52:55], s72 offen offset:3072\n" \
  "s_waitcnt vmcnt(54)\n" \
  "v_perm_b32 v32, v98, v96, s73\n" \
  "s_waitcnt vmcnt(52)\n" \
  "v_perm_b32 v33, v102, v100, s73\n" \
  "s_waitcnt vmcnt(50)\n" \
  "v_perm_b32 v34, v106, v104, s73\n" \
  "s_waitcnt vmcnt(48)\n" \
  "v_perm_b32 v35, v110, v108, s73\n" \
  "v_perm_b32 v36, v98, v96, s74\n" \
  "v_perm_b32 v37, v102, v100, s74\n" \
  "v_perm_b32 v38, v106, v104, s74\n" \
  "v_perm_b32 v39, v110, v108, s74\n" \
  "v_perm_b32 v40, v99, v97, s73\n" \
  "v_perm_b32 v41, v103, v101, s73\n" \
  "v_perm_b32 v42, v107, v105, s73\n" \
  "v_perm_b32 v43, v111, v109, s73\n" \
  "v_perm_b32 v44, v99, v97, s74\n" \
  "v_perm_b32 v45, v103, v101, s74\n" \
  "v_perm_b32 v46, v107, v105, s74\n" \
  "v_perm_b32 v47, v111, v109, s74\n" \
  "v_accvgpr_write_b32 a32, v32\n" \
  "v_accvgpr_write_b32 a33, v33\n" \
  "v_accvgpr_write_b32 a34, v34\n" \
  "v_accvgpr_write_b32 a35, v35\n" \
  "v_accvgpr_write_b32 a36, v36\n" \
  "v_accvgpr_write_b32 a37, v37\n" \
  "v_accvgpr_write_b32 a38, v38\n" \
  "v_accvgpr_write_b32 a39, v39\n" \
  "v_accvgpr_write_b32 a40, v40\n" \
  "v_accvgpr_write_b32 a41, v41\n" \
  "v_accvgpr_write_b32 a42, v42\n" \
  "v_accvgpr_write_b32 a43, v43\n" \
  "v_accvgpr_write_b32 a44, v44\n" \
  "v_accvgpr_write_b32 a45, v45\n" \
  "v_accvgpr_write_b32 a46, v46\n" \
  "v_accvgpr_write_b32 a47, v47\n" \
  "s_mov_b32 s71, 294912\n" \
  "s_mov_b32 s72, 311296\n" \
  "buffer_load_dwordx2 v[96:97], v20, s[52:55], s71 offen offset:0\n" \
  "buffer_load_dwordx2 v[98:99], v20, s[52:55], s72 offen offset:0\n" \
  "buffer_load_dwordx2 v[100:101], v20, s[52:55], s71 offen offset:1024\n" \
  "buffer_load_dwordx2 v[102:103], v20, s[52:55], s72 offen offset:1024\n" \
  "buffer_load_dwordx2 v[104:105], v20, s[52:55], s71 offen offset:2048\n" \
  "buffer_load_dwordx2 v[106:107], v20, s[52:55], s72 offen offset:2048\n" \
  "buffer_load_dwordx2 v[108:109], v20, s[52:55], s71 offen offset:3072\n" \
  "buffer_load_dwordx2 v[110:111], v20, s[52:55], s72 offen offset:3072\n" \
  "s_waitcnt vmcnt(54)\n" \
  "v_perm_b32 v32, v114, v112, s73\n" \
  "s_waitcnt vmcnt(52)\n" \
  "v_perm_b32 v33, v118, v116, s73\n" \
  "s_waitcnt vmcnt(50)\n" \
  "v_perm_b32 v34, v122, v120, s73\n" \
  "s_waitcnt vmcnt(48)\n" \
  "v_perm_b32 v35, v126, v124, s73\n" \
  "v_perm_b32 v36, v114, v112, s74\n" \
  "v_perm_b32 v37, v118, v116, s74\n" \
  "v_perm_b32 v38, v122, v120, s74\n" \
  "v_perm_b32 v39, v126, v124, s74\n" \
  "v_perm_b32 v40, v115, v113, s73\n" \
  "v_perm_b32 v41, v119, v117, s73\n" \
  "v_perm_b32 v42, v123, v121, s73\n" \
  "v_perm_b32 v43, v127, v125, s73\n" \
  "v_perm_b32 v44, v115, v113, s74\n" \
  "v_perm_b32 v45, v119, v117, s74\n" \
  "v_perm_b32 v46, v123, v121, s74\n" \
  "v_perm_b32 v47, v127, v125, s74\n" \
  "v_accvgpr_write_b32 a48, v32\n" \
  "v_accvgpr_write_b32 a49, v33\n" \
  "v_accvgpr_write_b32 a50, v34\n" \
  "v_accvgpr_write_b32 a51, v35\n" \
  "v_accvgpr_write_b32 a52, v36\n" \
  "v_accvgpr_write_b32 a53, v37\n" \
  "v_accvgpr_write_b32 a54, v38\n" \
  "v_accvgpr_write_b32 a55, v39\n" \
  "v_accvgpr_write_b32 a56, v40\n" \
  "v_accvgpr_write_b32 a57, v41\n" \
  "v_accvgpr_write_b32 a58, v42\n" \
  "v_accvgpr_write_b32 a59, v43\n" \
  "v_accvgpr_write_b32 a60, v44\n" \
  "v_accvgpr_write_b32 a61, v45\n" \
  "v_accvgpr_write_b32 a62, v46\n" \
  "v_accvgpr_write_b32 a63, v47\n" \
  "s_mov_b32 s71, 327680\n" \
  "s_mov_b32 s72, 344064\n" \
  "buffer_load_dwordx2 v[112:113], v20, s[52:55], s71 offen offset:0\n" \
  "buffer_load_dwordx2 v[114:115], v20, s[52:55], s72 offen offset:0\n" \
  "buffer_load_dwordx2 v[116:117], v20, s[52:55], s71 offen offset:1024\n" \
  "buffer_load_dwordx2 v[118:119], v20, s[52:55], s72 offen offset:1024\n" \
  "buffer_load_dwordx2 v[120:121], v20, s[52:55], s71 offen offset:2048\n" \
  "buffer_load_dwordx2 v[122:123], v20, s[52:55], s72 offen offset:2048\n" \
  "buffer_load_dwordx2 v[124:125], v20, s[52:55], s71 offen offset:3072\n" \
  "buffer_load_dwordx2 v[126:127], v20, s[52:55], s72 offen offset:3072\n" \
  "s_waitcnt vmcnt(54)\n" \
  "v_perm_b32 v32, v130, v128, s73\n" \
  "s_waitcnt vmcnt(52)\n" \
  "v_perm_b32 v33, v134, v132, s73\n" \
  "s_waitcnt vmcnt(50)\n" \
  "v_perm_b32 v34, v138, v136, s73\n" \
  "s_waitcnt vmcnt(48)\n" \
  "v_perm_b32 v35, v142, v140, s73\n" \
  "v_perm_b32 v36, v130, v128, s74\n" \
  "v_perm_b32 v37, v134, v132, s74\n" \
  "v_perm_b32 v38, v138, v136, s74\n" \
  "v_perm_b32 v39, v142, v140, s74\n" \
  "v_perm_b32 v40, v131, v129, s73\n" \
  "v_perm_b32 v41, v135, v133, s73\n" \
  "v_perm_b32 v42, v139, v137, s73\n" \
  "v_perm_b32 v43, v143, v141, s73\n" \
  "v_perm_b32 v44, v131, v129, s74\n" \
  "v_perm_b32 v45, v135, v133, s74\n" \
  "v_perm_b32 v46, v139, v137, s74\n" \
  "v_perm_b32 v47, v143, v141, s74\n" \
  "v_accvgpr_write_b32 a64, v32\n" \
  "v_accvgpr_write_b32 a65, v33\n" \
  "v_accvgpr_write_b32 a66, v34\n" \
  "v_accvgpr_write_b32 a67, v35\n" \
  "v_accvgpr_write_b32 a68, v36\n" \
  "v_accvgpr_write_b32 a69, v37\n" \
  "v_accvgpr_write_b32 a70, v38\n" \
  "v_accvgpr_write_b32 a71, v39\n" \
  "v_accvgpr_write_b32 a72, v40\n" \
  "v_accvgpr_write_b32 a73, v41\n" \
  "v_accvgpr_write_b32 a74, v42\n" \
  "v_accvgpr_write_b32 a75, v43\n" \
  "v_accvgpr_write_b32 a76, v44\n" \
  "v_accvgpr_write_b32 a77, v45\n" \
  "v_accvgpr_write_b32 a78, v46\n" \
  "v_accvgpr_write_b32 a79, v47\n" \
  "s_mov_b32 s71, 360448\n" \
  "s_mov_b32 s72, 376832\n" \
  "buffer_load_dwordx2 v[128:129], v20, s[52:55], s71 offen offset:0\n" \
  "buffer_load_dwordx2 v[130:131], v20, s[52:55], s72 offen offset:0\n" \
  "buffer_load_dwordx2 v[132:133], v20, s[52:55], s71 offen offset:1024\n" \
  "buffer_load_dwordx2 v[134:135], v20, s[52:55], s72 offen offset:1024\n" \
  "buffer_load_dwordx2 v[136:137], v20, s[52:55], s71 offen offset:2048\n" \
  "buffer_load_dwordx2 v[138:139], v20, s[52:55], s72 offen offset:2048\n" \
  "buffer_load_dwordx2 v[140:141], v20, s[52:55], s71 offen offset:3072\n" \
  "buffer_load_dwordx2 v[142:143], v20, s[52:55], s72 offen offset:3072\n" \
  "s_waitcnt vmcnt(54)\n" \
  "v_perm_b32 v32, v146, v144, s73\n" \
  "s_waitcnt vmcnt(52)\n" \
  "v_perm_b32 v33, v150, v148, s73\n" \
  "s_waitcnt vmcnt(50)\n" \
  "v_perm_b32 v34, v154, v152, s73\n" \
  "s_waitcnt vmcnt(48)\n" \
  "v_perm_b32 v35, v158, v156, s73\n" \
  "v_perm_b32 v36, v146, v144, s74\n" \
  "v_perm_b32 v37, v150, v148, s74\n" \
  "v_perm_b32 v38, v154, v152, s74\n" \
  "v_perm_b32 v39, v158, v156, s74\n" \
  "v_perm_b32 v40, v147, v145, s73\n" \
  "v_perm_b32 v41, v151, v149, s73\n" \
  "v_perm_b32 v42, v155, v153, s73\n" \
  "v_perm_b32 v43, v159, v157, s73\n" \
  "v_perm_b32 v44, v147, v145, s74\n" \
  "v_perm_b32 v45, v151, v149, s74\n" \
  "v_perm_b32 v46, v155, v153, s74\n" \
  "v_perm_b32 v47, v159, v157, s74\n" \
  "v_accvgpr_write_b32 a80, v32\n" \
  "v_accvgpr_write_b32 a81, v33\n" \
  "v_accvgpr_write_b32 a82, v34\n" \
  "v_accvgpr_write_b32 a83, v35\n" \
  "v_accvgpr_write_b32 a84, v36\n" \
  "v_accvgpr_write_b32 a85, v37\n" \
  "v_accvgpr_write_b32 a86, v38\n" \
  "v_accvgpr_write_b32 a87, v39\n" \
  "v_accvgpr_write_b32 a88, v40\n" \
  "v_accvgpr_write_b32 a89, v41\n" \
  "v_accvgpr_write_b32 a90, v42\n" \
  "v_accvgpr_write_b32 a91, v43\n" \
  "v_accvgpr_write_b32 a92, v44\n" \
  "v_accvgpr_write_b32 a93, v45\n" \
  "v_accvgpr_write_b32 a94, v46\n" \
  "v_accvgpr_write_b32 a95, v47\n" \
  "s_mov_b32 s71, 393216\n" \
  "s_mov_b32 s72, 409600\n" \
  "buffer_load_dwordx2 v[144:145], v20, s[52:55], s71 offen offset:0\n" \
  "buffer_load_dwordx2 v[146:147], v20, s[52:55], s72 offen offset:0\n" \
  "buffer_load_dwordx2 v[148:149], v20, s[52:55], s71 offen offset:1024\n" \
  "buffer_load_dwordx2 v[150:151], v20, s[52:55], s72 offen offset:1024\n" \
  "buffer_load_dwordx2 v[152:153], v20, s[52:55], s71 offen offset:2048\n" \
  "buffer_load_dwordx2 v[154:155], v20, s[52:55], s72 offen offset:2048\n" \
  "buffer_load_dwordx2 v[156:157], v20, s[52:55], s71 offen offset:3072\n" \
  "buffer_load_dwordx2 v[158:159], v20, s[52:55], s72 offen offset:3072\n" \
  "s_waitcnt vmcnt(54)\n" \
  "v_perm_b32 v32, v162, v160, s73\n" \
  "s_waitcnt vmcnt(52)\n" \
  "v_perm_b32 v33, v166, v164, s73\n" \
  "s_waitcnt vmcnt(50)\n" \
  "v_perm_b32 v34, v170, v168, s73\n" \
  "s_waitcnt vmcnt(48)\n" \
  "v_perm_b32 v35, v174, v172, s73\n" \
  "v_perm_b32 v36, v162, v160, s74\n" \
  "v_perm_b32 v37, v166, v164, s74\n" \
  "v_perm_b32 v38, v170, v168, s74\n" \
  "v_perm_b32 v39, v174, v172, s74\n" \
  "v_perm_b32 v40, v163, v161, s73\n" \
  "v_perm_b32 v41, v167, v165, s73\n" \
  "v_perm_b32 v42, v171, v169, s73\n" \
  "v_perm_b32 v43, v175, v173, s73\n" \
  "v_perm_b32 v44, v163, v161, s74\n" \
  "v_perm_b32 v45, v167, v165, s74\n" \
  "v_perm_b32 v46, v171, v169, s74\n" \
  "v_perm_b32 v47, v175, v173, s74\n" \
  "v_accvgpr_write_b32 a96, v32\n" \
  "v_accvgpr_write_b32 a97, v33\n" \
  "v_accvgpr_write_b32 a98, v34\n" \
  "v_accvgpr_write_b32 a99, v35\n" \
  "v_accvgpr_write_b32 a100, v36\n" \
  "v_accvgpr_write_b32 a101, v37\n" \
  "v_accvgpr_write_b32 a102, v38\n" \
  "v_accvgpr_write_b32 a103, v39\n" \
  "v_accvgpr_write_b32 a104, v40\n" \
  "v_accvgpr_write_b32 a105, v41\n" \
  "v_accvgpr_write_b32 a106, v42\n" \
  "v_accvgpr_write_b32 a107, v43\n" \
  "v_accvgpr_write_b32 a108, v44\n" \
  "v_accvgpr_write_b32 a109, v45\n" \
  "v_accvgpr_write_b32 a110, v46\n" \
  "v_accvgpr_write_b32 a111, v47\n" \
  "s_mov_b32 s71, 425984\n" \
  "s_mov_b32 s72, 442368\n" \
  "buffer_load_dwordx2 v[160:161], v20, s[52:55], s71 offen offset:0\n" \
  "buffer_load_dwordx2 v[162:163], v20, s[52:55], s72 offen offset:0\n" \
  "buffer_load_dwordx2 v[164:165], v20, s[52:55], s71 offen offset:1024\n" \
  "buffer_load_dwordx2 v[166:167], v20, s[52:55], s72 offen offset:1024\n" \
  "buffer_load_dwordx2 v[168:169], v20, s[52:55], s71 offen offset:2048\n" \
  "buffer_load_dwordx2 v[170:171], v20, s[52:55], s72 offen offset:2048\n" \
  "buffer_load_dwordx2 v[172:173], v20, s[52:55], s71 offen offset:3072\n" \
  "buffer_load_dwordx2 v[174:175], v20, s[52:55], s72 offen offset:3072\n" \
  "s_waitcnt vmcnt(54)\n" \
  "v_perm_b32 v32, v66, v64, s73\n" \
  "s_waitcnt vmcnt(52)\n" \
  "v_perm_b32 v33, v70, v68, s73\n" \
  "s_waitcnt vmcnt(50)\n" \
  "v_perm_b32 v34, v74, v72, s73\n" \
  "s_waitcnt vmcnt(48)\n" \
  "v_perm_b32 v35, v78, v76, s73\n" \
  "v_perm_b32 v36, v66, v64, s74\n" \
  "v_perm_b32 v37, v70, v68, s74\n" \
  "v_perm_b32 v38, v74, v72, s74\n" \
  "v_perm_b32 v39, v78, v76, s74\n" \
  "v_perm_b32 v40, v67, v65, s73\n" \
  "v_perm_b32 v41, v71, v69, s73\n" \
  "v_perm_b32 v42, v75, v73, s73\n" \
  "v_perm_b32 v43, v79, v77, s73\n" \
  "v_perm_b32 v44, v67, v65, s74\n" \
  "v_perm_b32 v45, v71, v69, s74\n" \
  "v_perm_b32 v46, v75, v73, s74\n" \
  "v_perm_b32 v47, v79, v77, s74\n" \
  "v_accvgpr_write_b32 a112, v32\n" \
  "v_accvgpr_write_b32 a113, v33\n" \
  "v_accvgpr_write_b32 a114, v34\n" \
  "v_accvgpr_write_b32 a115, v35\n" \
  "v_accvgpr_write_b32 a116, v36\n" \
  "v_accvgpr_write_b32 a117, v37\n" \
  "v_accvgpr_write_b32 a118, v38\n" \
  "v_accvgpr_write_b32 a119, v39\n" \
  "v_accvgpr_write_b32 a120, v40\n" \
  "v_accvgpr_write_b32 a121, v41\n" \
  "v_accvgpr_write_b32 a122, v42\n" \
  "v_accvgpr_write_b32 a123, v43\n" \
  "v_accvgpr_write_b32 a124, v44\n" \
  "v_accvgpr_write_b32 a125, v45\n" \
  "v_accvgpr_write_b32 a126, v46\n" \
  "v_accvgpr_write_b32 a127, v47\n" \
  "s_mov_b32 s71, 458752\n" \
  "s_mov_b32 s72, 475136\n" \
  "buffer_load_dwordx2 v[64:65], v20, s[52:55], s71 offen offset:0\n" \
  "buffer_load_dwordx2 v[66:67], v20, s[52:55], s72 offen offset:0\n" \
  "buffer_load_dwordx2 v[68:69], v20, s[52:55], s71 offen offset:1024\n" \
  "buffer_load_dwordx2 v[70:71], v20, s[52:55], s72 offen offset:1024\n" \
  "buffer_load_dwordx2 v[72:73], v20, s[52:55], s71 offen offset:2048\n" \
  "buffer_load_dwordx2 v[74:75], v20, s[52:55], s72 offen offset:2048\n" \
  "buffer_load_dwordx2 v[76:77], v20, s[52:55], s71 offen offset:3072\n" \
  "buffer_load_dwordx2 v[78:79], v20, s[52:55], s72 offen offset:3072\n" \
  "s_waitcnt vmcnt(54)\n" \
  "v_perm_b32 v32, v82, v80, s73\n" \
  "s_waitcnt vmcnt(52)\n" \
  "v_perm_b32 v33, v86, v84, s73\n" \
  "s_waitcnt vmcnt(50)\n" \
  "v_perm_b32 v34, v90, v88, s73\n" \
  "s_waitcnt vmcnt(48)\n" \
  "v_perm_b32 v35, v94, v92, s73\n" \
  "v_perm_b32 v36, v82, v80, s74\n" \
  "v_perm_b32 v37, v86, v84, s74\n" \
  "v_perm_b32 v38, v90, v88, s74\n" \
  "v_perm_b32 v39, v94, v92, s74\n" \
  "v_perm_b32 v40, v83, v81, s73\n" \
  "v_perm_b32 v41, v87, v85, s73\n" \
  "v_perm_b32 v42, v91, v89, s73\n" \
  "v_perm_b32 v43, v95, v93, s73\n" \
  "v_perm_b32 v44, v83, v81, s74\n" \
  "v_perm_b32 v45, v87, v85, s74\n" \
  "v_perm_b32 v46, v91, v89, s74\n" \
  "v_perm_b32 v47, v95, v93, s74\n" \
  "v_accvgpr_write_b32 a128, v32\n" \
  "v_accvgpr_write_b32 a129, v33\n" \
  "v_accvgpr_write_b32 a130, v34\n" \
  "v_accvgpr_write_b32 a131, v35\n" \
  "v_accvgpr_write_b32 a132, v36\n" \
  "v_accvgpr_write_b32 a133, v37\n" \
  "v_accvgpr_write_b32 a134, v38\n" \
  "v_accvgpr_write_b32 a135, v39\n" \
  "v_accvgpr_write_b32 a136, v40\n" \
  "v_accvgpr_write_b32 a137, v41\n" \
  "v_accvgpr_write_b32 a138, v42\n" \
  "v_accvgpr_write_b32 a139, v43\n" \
  "v_accvgpr_write_b32 a140, v44\n" \
  "v_accvgpr_write_b32 a141, v45\n" \
  "v_accvgpr_write_b32 a142, v46\n" \
  "v_accvgpr_write_b32 a143, v47\n" \
  "s_mov_b32 s71, 491520\n" \
  "s_mov_b32 s72, 507904\n" \
  "buffer_load_dwordx2 v[80:81], v20, s[52:55], s71 offen offset:0\n" \
  "buffer_load_dwordx2 v[82:83], v20, s[52:55], s72 offen offset:0\n" \
  "buffer_load_dwordx2 v[84:85], v20, s[52:55], s71 offen offset:1024\n" \
  "buffer_load_dwordx2 v[86:87], v20, s[52:55], s72 offen offset:1024\n" \
  "buffer_load_dwordx2 v[88:89], v20, s[52:55], s71 offen offset:2048\n" \
  "buffer_load_dwordx2 v[90:91], v20, s[52:55], s72 offen offset:2048\n" \
  "buffer_load_dwordx2 v[92:93], v20, s[52:55], s71 offen offset:3072\n" \
  "buffer_load_dwordx2 v[94:95], v20, s[52:55], s72 offen offset:3072\n" \
  "s_waitcnt vmcnt(54)\n" \
  "v_perm_b32 v32, v98, v96, s73\n" \
  "s_waitcnt vmcnt(52)\n" \
  "v_perm_b32 v33, v102, v100, s73\n" \
  "s_waitcnt vmcnt(50)\n" \
  "v_perm_b32 v34, v106, v104, s73\n" \
  "s_waitcnt vmcnt(48)\n" \
  "v_perm_b32 v35, v110, v108, s73\n" \
  "v_perm_b32 v36, v98, v96, s74\n" \
  "v_perm_b32 v37, v102, v100, s74\n" \
  "v_perm_b32 v38, v106, v104, s74\n" \
  "v_perm_b32 v39, v110, v108, s74\n" \
  "v_perm_b32 v40, v99, v97, s73\n" \
  "v_perm_b32 v41, v103, v101, s73\n" \
  "v_perm_b32 v42, v107, v105, s73\n" \
  "v_perm_b32 v43, v111, v109, s73\n" \
  "v_perm_b32 v44, v99, v97, s74\n" \
  "v_perm_b32 v45, v103, v101, s74\n" \
  "v_perm_b32 v46, v107, v105, s74\n" \
  "v_perm_b32 v47, v111, v109, s74\n" \
  "v_accvgpr_write_b32 a144, v32\n" \
  "v_accvgpr_write_b32 a145, v33\n" \
  "v_accvgpr_write_b32 a146, v34\n" \
  "v_accvgpr_write_b32 a147, v35\n" \
  "v_accvgpr_write_b32 a148, v36\n" \
  "v_accvgpr_write_b32 a149, v37\n" \
  "v_accvgpr_write_b32 a150, v38\n" \
  "v_accvgpr_write_b32 a151, v39\n" \
  "v_accvgpr_write_b32 a152, v40\n" \
  "v_accvgpr_write_b32 a153, v41\n" \
  "v_accvgpr_write_b32 a154, v42\n" \
  "v_accvgpr_write_b32 a155, v43\n" \
  "v_accvgpr_write_b32 a156, v44\n" \
  "v_accvgpr_write_b32 a157, v45\n" \
  "v_accvgpr_write_b32 a158, v46\n" \
  "v_accvgpr_write_b32 a159, v47\n" \
  "s_waitcnt vmcnt(46)\n" \
  "v_perm_b32 v32, v114, v112, s73\n" \
  "s_waitcnt vmcnt(44)\n" \
  "v_perm_b32 v33, v118, v116, s73\n" \
  "s_waitcnt vmcnt(42)\n" \
  "v_perm_b32 v34, v122, v120, s73\n" \
  "s_waitcnt vmcnt(40)\n" \
  "v_perm_b32 v35, v126, v124, s73\n" \
  "v_perm_b32 v36, v114, v112, s74\n" \
  "v_perm_b32 v37, v118, v116, s74\n" \
  "v_perm_b32 v38, v122, v120, s74\n" \
  "v_perm_b32 v39, v126, v124, s74\n" \
  "v_perm_b32 v40, v115, v113, s73\n" \
  "v_perm_b32 v41, v119, v117, s73\n" \
  "v_perm_b32 v42, v123, v121, s73\n" \
  "v_perm_b32 v43, v127, v125, s73\n" \
  "v_perm_b32 v44, v115, v113, s74\n" \
  "v_perm_b32 v45, v119, v117, s74\n" \
  "v_perm_b32 v46, v123, v121, s74\n" \
  "v_perm_b32 v47, v127, v125, s74\n" \
  "v_accvgpr_write_b32 a160, v32\n" \
  "v_accvgpr_write_b32 a161, v33\n" \
  "v_accvgpr_write_b32 a162, v34\n" \
  "v_accvgpr_write_b32 a163, v35\n" \
  "v_accvgpr_write_b32 a164, v36\n" \
  "v_accvgpr_write_b32 a165, v37\n" \
  "v_accvgpr_write_b32 a166, v38\n" \
  "v_accvgpr_write_b32 a167, v39\n" \
  "v_accvgpr_write_b32 a168, v40\n" \
  "v_accvgpr_write_b32 a169, v41\n" \
  "v_accvgpr_write_b32 a170, v42\n" \
  "v_accvgpr_write_b32 a171, v43\n" \
  "v_accvgpr_write_b32 a172, v44\n" \
  "v_accvgpr_write_b32 a173, v45\n" \
  "v_accvgpr_write_b32 a174, v46\n" \
  "v_accvgpr_write_b32 a175, v47\n" \
  "s_waitcnt vmcnt(38)\n" \
  "v_perm_b32 v32, v130, v128, s73\n" \
  "s_waitcnt vmcnt(36)\n" \
  "v_perm_b32 v33, v134, v132, s73\n" \
  "s_waitcnt vmcnt(34)\n" \
  "v_perm_b32 v34, v138, v136, s73\n" \
  "s_waitcnt vmcnt(32)\n" \
  "v_perm_b32 v35, v142, v140, s73\n" \
  "v_perm_b32 v36, v130, v128, s74\n" \
  "v_perm_b32 v37, v134, v132, s74\n" \
  "v_perm_b32 v38, v138, v136, s74\n" \
  "v_perm_b32 v39, v142, v140, s74\n" \
  "v_perm_b32 v40, v131, v129, s73\n" \
  "v_perm_b32 v41, v135, v133, s73\n" \
  "v_perm_b32 v42, v139, v137, s73\n" \
  "v_perm_b32 v43, v143, v141, s73\n" \
  "v_perm_b32 v44, v131, v129, s74\n" \
  "v_perm_b32 v45, v135, v133, s74\n" \
  "v_perm_b32 v46, v139, v137, s74\n" \
  "v_perm_b32 v47, v143, v141, s74\n" \
  "v_accvgpr_write_b32 a176, v32\n" \
  "v_accvgpr_write_b32 a177, v33\n" \
  "v_accvgpr_write_b32 a178, v34\n" \
  "v_accvgpr_write_b32 a179, v35\n" \
  "v_accvgpr_write_b32 a180, v36\n" \
  "v_accvgpr_write_b32 a181, v37\n" \
  "v_accvgpr_write_b32 a182, v38\n" \
  "v_accvgpr_write_b32 a183, v39\n" \
  "v_accvgpr_write_b32 a184, v40\n" \
  "v_accvgpr_write_b32 a185, v41\n" \
  "v_accvgpr_write_b32 a186, v42\n" \
  "v_accvgpr_write_b32 a187, v43\n" \
  "v_accvgpr_write_b32 a188, v44\n" \
  "v_accvgpr_write_b32 a189, v45\n" \
  "v_accvgpr_write_b32 a190, v46\n" \
  "v_accvgpr_write_b32 a191, v47\n" \
  "s_waitcnt vmcnt(30)\n" \
  "v_perm_b32 v32, v146, v144, s73\n" \
  "s_waitcnt vmcnt(28)\n" \
  "v_perm_b32 v33, v150, v148, s73\n" \
  "s_waitcnt vmcnt(26)\n" \
  "v_perm_b32 v34, v154, v152, s73\n" \
  "s_waitcnt vmcnt(24)\n" \
  "v_perm_b32 v35, v158, v156, s73\n" \
  "v_perm_b32 v36, v146, v144, s74\n" \
  "v_perm_b32 v37, v150, v148, s74\n" \
  "v_perm_b32 v38, v154, v152, s74\n" \
  "v_perm_b32 v39, v158, v156, s74\n" \
  "v_perm_b32 v40, v147, v145, s73\n" \
  "v_perm_b32 v41, v151, v149, s73\n" \
  "v_perm_b32 v42, v155, v153, s73\n" \
  "v_perm_b32 v43, v159, v157, s73\n" \
  "v_perm_b32 v44, v147, v145, s74\n" \
  "v_perm_b32 v45, v151, v149, s74\n" \
  "v_perm_b32 v46, v155, v153, s74\n" \
  "v_perm_b32 v47, v159, v157, s74\n" \
  "v_accvgpr_write_b32 a192, v32\n" \
  "v_accvgpr_write_b32 a193, v33\n" \
  "v_accvgpr_write_b32 a194, v34\n" \
  "v_accvgpr_write_b32 a195, v35\n" \
  "v_accvgpr_write_b32 a196, v36\n" \
  "v_accvgpr_write_b32 a197, v37\n" \
  "v_accvgpr_write_b32 a198, v38\n" \
  "v_accvgpr_write_b32 a199, v39\n" \
  "v_accvgpr_write_b32 a200, v40\n" \
  "v_accvgpr_write_b32 a201, v41\n" \
  "v_accvgpr_write_b32 a202, v42\n" \
  "v_accvgpr_write_b32 a203, v43\n" \
  "v_accvgpr_write_b32 a204, v44\n" \
  "v_accvgpr_write_b32 a205, v45\n" \
  "v_accvgpr_write_b32 a206, v46\n" \
  "v_accvgpr_write_b32 a207, v47\n" \
  "s_waitcnt vmcnt(22)\n" \
  "v_perm_b32 v32, v162, v160, s73\n" \
  "s_waitcnt vmcnt(20)\n" \
  "v_perm_b32 v33, v166, v164, s73\n" \
  "s_waitcnt vmcnt(18)\n" \
  "v_perm_b32 v34, v170, v168, s73\n" \
  "s_waitcnt vmcnt(16)\n" \
  "v_perm_b32 v35, v174, v172, s73\n" \
  "v_perm_b32 v36, v162, v160, s74\n" \
  "v_perm_b32 v37, v166, v164, s74\n" \
  "v_perm_b32 v38, v170, v168, s74\n" \
  "v_perm_b32 v39, v174, v172, s74\n" \
  "v_perm_b32 v40, v163, v161, s73\n" \
  "v_perm_b32 v41, v167, v165, s73\n" \
  "v_perm_b32 v42, v171, v169, s73\n" \
  "v_perm_b32 v43, v175, v173, s73\n" \
  "v_perm_b32 v44, v163, v161, s74\n" \
  "v_perm_b32 v45, v167, v165, s74\n" \
  "v_perm_b32 v46, v171, v169, s74\n" \
  "v_perm_b32 v47, v175, v173, s74\n" \
  "v_accvgpr_write_b32 a208, v32\n" \
  "v_accvgpr_write_b32 a209, v33\n" \
  "v_accvgpr_write_b32 a210, v34\n" \
  "v_accvgpr_write_b32 a211, v35\n" \
  "v_accvgpr_write_b32 a212, v36\n" \
  "v_accvgpr_write_b32 a213, v37\n" \
  "v_accvgpr_write_b32 a214, v38\n" \
  "v_accvgpr_write_b32 a215, v39\n" \
  "v_accvgpr_write_b32 a216, v40\n" \
  "v_accvgpr_write_b32 a217, v41\n" \
  "v_accvgpr_write_b32 a218, v42\n" \
  "v_accvgpr_write_b32 a219, v43\n" \
  "v_accvgpr_write_b32 a220, v44\n" \
  "v_accvgpr_write_b32 a221, v45\n" \
  "v_accvgpr_write_b32 a222, v46\n" \
  "v_accvgpr_write_b32 a223, v47\n" \
  "s_waitcnt vmcnt(14)\n" \
  "v_perm_b32 v32, v66, v64, s73\n" \
  "s_waitcnt vmcnt(12)\n" \
  "v_perm_b32 v33, v70, v68, s73\n" \
  "s_waitcnt vmcnt(10)\n" \
  "v_perm_b32 v34, v74, v72, s73\n" \
  "s_waitcnt vmcnt(8)\n" \
  "v_perm_b32 v35, v78, v76, s73\n" \
  "v_perm_b32 v36, v66, v64, s74\n" \
  "v_perm_b32 v37, v70, v68, s74\n" \
  "v_perm_b32 v38, v74, v72, s74\n" \
  "v_perm_b32 v39, v78, v76, s74\n" \
  "v_perm_b32 v40, v67, v65, s73\n" \
  "v_perm_b32 v41, v71, v69, s73\n" \
  "v_perm_b32 v42, v75, v73, s73\n" \
  "v_perm_b32 v43, v79, v77, s73\n" \
  "v_perm_b32 v44, v67, v65, s74\n" \
  "v_perm_b32 v45, v71, v69, s74\n" \
  "v_perm_b32 v46, v75, v73, s74\n" \
  "v_perm_b32 v47, v79, v77, s74\n" \
  "v_accvgpr_write_b32 a224, v32\n" \
  "v_accvgpr_write_b32 a225, v33\n" \
  "v_accvgpr_write_b32 a226, v34\n" \
  "v_accvgpr_write_b32 a227, v35\n" \
  "v_accvgpr_write_b32 a228, v36\n" \
  "v_accvgpr_write_b32 a229, v37\n" \
  "v_accvgpr_write_b32 a230, v38\n" \
  "v_accvgpr_write_b32 a231, v39\n" \
  "v_accvgpr_write_b32 a232, v40\n" \
  "v_accvgpr_write_b32 a233, v41\n" \
  "v_accvgpr_write_b32 a234, v42\n" \
  "v_accvgpr_write_b32 a235, v43\n" \
  "v_accvgpr_write_b32 a236, v44\n" \
  "v_accvgpr_write_b32 a237, v45\n" \
  "v_accvgpr_write_b32 a238, v46\n" \
  "v_accvgpr_write_b32 a239, v47\n" \
  "s_waitcnt vmcnt(6)\n" \
  "v_perm_b32 v32, v82, v80, s73\n" \
  "s_waitcnt vmcnt(4)\n" \
  "v_perm_b32 v33, v86, v84, s73\n" \
  "s_waitcnt vmcnt(2)\n" \
  "v_perm_b32 v34, v90, v88, s73\n" \
  "s_waitcnt vmcnt(0)\n" \
  "v_perm_b32 v35, v94, v92, s73\n" \
  "v_perm_b32 v36, v82, v80, s74\n" \
  "v_perm_b32 v37, v86, v84, s74\n" \
  "v_perm_b32 v38, v90, v88, s74\n" \
  "v_perm_b32 v39, v94, v92, s74\n" \
  "v_perm_b32 v40, v83, v81, s73\n" \
  "v_perm_b32 v41, v87, v85, s73\n" \
  "v_perm_b32 v42, v91, v89, s73\n" \
  "v_perm_b32 v43, v95, v93, s73\n" \
  "v_perm_b32 v44, v83, v81, s74\n" \
  "v_perm_b32 v45, v87, v85, s74\n" \
  "v_perm_b32 v46, v91, v89, s74\n" \
  "v_perm_b32 v47, v95, v93, s74\n" \
  "v_accvgpr_write_b32 a240, v32\n" \
  "v_accvgpr_write_b32 a241, v33\n" \
  "v_accvgpr_write_b32 a242, v34\n" \
  "v_accvgpr_write_b32 a243, v35\n" \
  "v_accvgpr_write_b32 a244, v36\n" \
  "v_accvgpr_write_b32 a245, v37\n" \
  "v_accvgpr_write_b32 a246, v38\n" \
  "v_accvgpr_write_b32 a247, v39\n" \
  "v_accvgpr_write_b32 a248, v40\n" \
  "v_accvgpr_write_b32 a249, v41\n" \
  "v_accvgpr_write_b32 a250, v42\n" \
  "v_accvgpr_write_b32 a251, v43\n" \
  "v_accvgpr_write_b32 a252, v44\n" \
  "v_accvgpr_write_b32 a253, v45\n" \
  "v_accvgpr_write_b32 a254, v46\n" \
  "v_accvgpr_write_b32 a255, v47\n" \
  "s_waitcnt vmcnt(0) lgkmcnt(0)\n" \
  "s_branch .Lwo_sel\n" \
  ".Lwo_q:\n" \
  "ds_read_b128 v[64:67], v8 offset:32768\n" \
  "ds_read_b128 v[68:71], v8 offset:33792\n" \
  "ds_read_b128 v[72:75], v8 offset:34816\n" \
  "ds_read_b128 v[76:79], v8 offset:35840\n" \
  "ds_read_b128 v[80:83], v8 offset:40960\n" \
  "ds_read_b128 v[84:87], v8 offset:41984\n" \
  "ds_read_b128 v[88:91], v8 offset:43008\n" \
  "ds_read_b128 v[92:95], v8 offset:44032\n" \
  "ds_read_b128 v[96:99], v8 offset:49152\n" \
  "ds_read_b128 v[100:103], v8 offset:50176\n" \
  "ds_read_b128 v[104:107], v8 offset:51200\n" \
  "ds_read_b128 v[108:111], v8 offset:52224\n" \
  "ds_read_b128 v[112:115], v8 offset:57344\n" \
  "ds_read_b128 v[116:119], v8 offset:58368\n" \
  "ds_read_b128 v[120:123], v8 offset:59392\n" \
  "ds_read_b128 v[124:127], v8 offset:60416\n" \
  "s_mov_b32 m0, s40\n" \
  "s_branch .Lwo_pq_c\n" \
  ".Lwo_pq:\n" \
  ".Lwo_pq_f:\n" \
  "buffer_load_dword v62, v30, s[36:39], 0 offen sc1\n" \
  "s_sub_u32 s70, s70, 1\n" \
  "s_cselect_b32 s77, -1, s77\n" \
  "s_cselect_b32 s70, 0, s70\n" \
  "s_waitcnt vmcnt(0)\n" \
  ".Lwo_pq_c:\n" \
  "v_cmp_gt_i32 vcc, s77, v62\n" \
  "s_and_b32 vcc_lo, vcc_lo, s29\n" \
  "s_cbranch_scc1 .Lwo_pq\n" \
  "s_barrier\n" \
  "buffer_load_dwordx4 v0, s[36:39], s61 offen sc1 lds\n" \
  "s_waitcnt lgkmcnt(14)\n" \
  "v_mfma_f32_16x16x32_bf16 v[32:35], v[64:67], a[0:3], 0\n" \
  "v_mfma_f32_16x16x32_bf16 v[36:39], v[64:67], a[4:7], 0\n" \
  "s_mov_b32 m0, s41\n" \
  "v_mfma_f32_16x16x32_bf16 v[32:35], v[68:71], a[16:19], v[32:35]\n" \
  "buffer_load_dwordx4 v1, s[36:39], s61 offen sc1 lds\n" \
  "v_mfma_f32_16x16x32_bf16 v[36:39], v[68:71], a[20:23], v[36:39]\n" \
  "s_waitcnt lgkmcnt(13)\n" \
  "v_mfma_f32_16x16x32_bf16 v[32:35], v[72:75], a[32:35], v[32:35]\n" \
  "s_mov_b32 m0, s42\n" \
  "v_mfma_f32_16x16x32_bf16 v[36:39], v[72:75], a[36:39], v[36:39]\n" \
  "buffer_load_dwordx4 v2, s[36:39], s61 offen sc1 lds\n" \
  "s_waitcnt lgkmcnt(12)\n" \
  "v_mfma_f32_16x16x32_bf16 v[32:35], v[76:79], a[48:51], v[32:35]\n" \
  "v_mfma_f32_16x16x32_bf16 v[36:39], v[76:79], a[52:55], v[36:39]\n" \
  "s_mov_b32 m0, s43\n" \
  "s_waitcnt lgkmcnt(11)\n" \
  "v_mfma_f32_16x16x32_bf16 v[32:35], v[80:83], a[64:67], v[32:35]\n" \
  "buffer_load_dwordx4 v3, s[36:39], s61 offen sc1 lds\n" \
  "v_mfma_f32_16x16x32_bf16 v[36:39], v[80:83], a[68:71], v[36:39]\n" \
  "s_waitcnt lgkmcnt(10)\n" \
  "v_mfma_f32_16x16x32_bf16 v[32:35], v[84:87], a[80:83], v[32:35]\n" \
  "s_mov_b32 m0, s44\n" \
  "v_mfma_f32_16x16x32_bf16 v[36:39], v[84:87], a[84:87], v[36:39]\n" \
  "buffer_load_dwordx4 v4, s[36:39], s61 offen sc1 lds\n" \
  "s_waitcnt lgkmcnt(9)\n" \
  "v_mfma_f32_16x16x32_bf16 v[32:35], v[88:91], a[96:99], v[32:35]\n" \
  "v_mfma_f32_16x16x32_bf16 v[36:39], v[88:91], a[100:103], v[36:39]\n" \
  "s_mov_b32 m0, s45\n" \
  "s_waitcnt lgkmcnt(8)\n" \
  "v_mfma_f32_16x16x32_bf16 v[32:35], v[92:95], a[112:115], v[32:35]\n" \
  "buffer_load_dwordx4 v5, s[36:39], s61 offen sc1 lds\n" \
  "v_mfma_f32_16x16x32_bf16 v[36:39], v[92:95], a[116:119], v[36:39]\n" \
  "s_waitcnt lgkmcnt(7)\n" \
  "v_mfma_f32_16x16x32_bf16 v[32:35], v[96:99], a[128:131], v[32:35]\n" \
  "s_mov_b32 m0, s46\n" \
  "v_mfma_f32_16x16x32_bf16 v[36:39], v[96:99], a[132:135], v[36:39]\n" \
  "buffer_load_dwordx4 v6, s[36:39], s61 offen sc1 lds\n" \
  "s_waitcnt lgkmcnt(6)\n" \
  "v_mfma_f32_16x16x32_bf16 v[32:35], v[100:103], a[144:147], v[32:35]\n" \
  "v_mfma_f32_16x16x32_bf16 v[36:39], v[100:103], a[148:151], v[36:39]\n" \
  "s_mov_b32 m0, s47\n" \
  "s_waitcnt lgkmcnt(5)\n" \
  "v_mfma_f32_16x16x32_bf16 v[32:35], v[104:107], a[160:163], v[32:35]\n" \
  "buffer_load_dwordx4 v7, s[36:39], s61 offen sc1 lds\n" \
  "v_mfma_f32_16x16x32_bf16 v[36:39], v[104:107], a[164:167], v[36:39]\n" \
  "s_waitcnt lgkmcnt(4)\n" \
  "v_mfma_f32_16x16x32_bf16 v[32:35], v[108:111], a[176:179], v[32:35]\n" \
  "v_mfma_f32_16x16x32_bf16 v[36:39], v[108:111], a[180:183], v[36:39]\n" \
  "s_waitcnt lgkmcnt(3)\n" \
  "v_mfma_f32_16x16x32_bf16 v[32:35], v[112:115], a[192:195], v[32:35]\n" \
  "v_cmp_gt_i32 vcc, s63, v62\n" \
  "s_cmp_eq_u32 vcc_hi, 0\n" \
  "v_mfma_f32_16x16x32_bf16 v[36:39], v[112:115], a[196:199], v[36:39]\n" \
  "s_cselect_b32 s72, s64, 0x40000000\n" \
  "v_cmp_gt_i32 vcc, s31, v62\n" \
  "s_cmp_lg_u32 vcc_hi, 0\n" \
  "s_addc_u32 s72, s72, 0\n" \
  "v_mov_b32 v31, s72\n" \
  "s_waitcnt lgkmcnt(2)\n" \
  "v_mfma_f32_16x16x32_bf16 v[32:35], v[116:119], a[208:211], v[32:35]\n" \
  "ds_write_b32 v15, v31\n" \
  "v_mfma_f32_16x16x32_bf16 v[36:39], v[116:119], a[212:215], v[36:39]\n" \
  "s_waitcnt lgkmcnt(2)\n" \
  "v_mfma_f32_16x16x32_bf16 v[32:35], v[120:123], a[224:227], v[32:35]\n" \
  "v_mfma_f32_16x16x32_bf16 v[36:39], v[120:123], a[228:231], v[36:39]\n" \
  "s_waitcnt lgkmcnt(1)\n" \
  "v_mfma_f32_16x16x32_bf16 v[32:35], v[124:127], a[240:243], v[32:35]\n" \
  "v_mfma_f32_16x16x32_bf16 v[36:39], v[124:127], a[244:247], v[36:39]\n" \
  "v_mfma_f32_16x16x32_bf16 v[40:43], v[64:67], a[8:11], 0\n" \
  "v_mfma_f32_16x16x32_bf16 v[44:47], v[64:67], a[12:15], 0\n" \
  "v_mfma_f32_16x16x32_bf16 v[40:43], v[68:71], a[24:27], v[40:43]\n" \
  "v_cvt_pk_bf16_f32 v56, v32, v36\n" \
  "v_cvt_pk_bf16_f32 v50, v33, v37\n" \
  "v_mfma_f32_16x16x32_bf16 v[44:47], v[68:71], a[28:31], v[44:47]\n" \
  "v_cvt_pk_bf16_f32 v48, v32, v36\n" \
  "v_cvt_pk_bf16_f32 v57, v34, v38\n" \
  "v_mfma_f32_16x16x32_bf16 v[40:43], v[72:75], a[40:43], v[40:43]\n" \
  "v_cvt_pk_bf16_f32 v54, v35, v39\n" \
  "v_cvt_pk_bf16_f32 v52, v34, v38\n" \
  "v_mfma_f32_16x16x32_bf16 v[44:47], v[72:75], a[44:47], v[44:47]\n" \
  "v_mfma_f32_16x16x32_bf16 v[40:43], v[76:79], a[56:59], v[40:43]\n" \
  "v_mov_b32_dpp v48, v50 row_ror:8 row_mask:0xf bank_mask:0xc\n" \
  "v_mov_b32_dpp v50, v56 row_ror:8 row_mask:0xf bank_mask:0x3\n" \
  "v_mfma_f32_16x16x32_bf16 v[44:47], v[76:79], a[60:63], v[44:47]\n" \
  "v_mov_b32_dpp v52, v54 row_ror:8 row_mask:0xf bank_mask:0xc\n" \
  "v_mov_b32_dpp v54, v57 row_ror:8 row_mask:0xf bank_mask:0x3\n" \
  "v_mfma_f32_16x16x32_bf16 v[40:43], v[80:83], a[72:75], v[40:43]\n" \
  "v_mfma_f32_16x16x32_bf16 v[44:47], v[80:83], a[76:79], v[44:47]\n" \
  "v_mfma_f32_16x16x32_bf16 v[40:43], v[84:87], a[88:91], v[40:43]\n" \
  "v_mfma_f32_16x16x32_bf16 v[44:47], v[84:87], a[92:95], v[44:47]\n" \
  "v_mfma_f32_16x16x32_bf16 v[40:43], v[88:91], a[104:107], v[40:43]\n" \
  "v_mfma_f32_16x16x32_bf16 v[44:47], v[88:91], a[108:111], v[44:47]\n" \
  "v_mfma_f32_16x16x32_bf16 v[40:43], v[92:95], a[120:123], v[40:43]\n" \
  "v_mfma_f32_16x16x32_bf16 v[44:47], v[92:95], a[124:127], v[44:47]\n" \
  "v_mfma_f32_16x16x32_bf16 v[40:43], v[96:99], a[136:139], v[40:43]\n" \
  "v_mfma_f32_16x16x32_bf16 v[44:47], v[96:99], a[140:143], v[44:47]\n" \
  "v_mfma_f32_16x16x32_bf16 v[40:43], v[100:103], a[152:155], v[40:43]\n" \
  "v_mfma_f32_16x16x32_bf16 v[44:47], v[100:103], a[156:159], v[44:47]\n" \
  "v_mfma_f32_16x16x32_bf16 v[40:43], v[104:107], a[168:171], v[40:43]\n" \
  "v_mfma_f32_16x16x32_bf16 v[44:47], v[104:107], a[172:175], v[44:47]\n" \
  "v_mfma_f32_16x16x32_bf16 v[40:43], v[108:111], a[184:187], v[40:43]\n" \
  "v_mfma_f32_16x16x32_bf16 v[44:47], v[108:111], a[188:191], v[44:47]\n" \
  "v_mfma_f32_16x16x32_bf16 v[40:43], v[112:115], a[200:203], v[40:43]\n" \
  "v_mfma_f32_16x16x32_bf16 v[44:47], v[112:115], a[204:207], v[44:47]\n" \
  "v_mfma_f32_16x16x32_bf16 v[40:43], v[116:119], a[216:219], v[40:43]\n" \
  "v_mfma_f32_16x16x32_bf16 v[44:47], v[116:119], a[220:223], v[44:47]\n" \
  "v_mfma_f32_16x16x32_bf16 v[40:43], v[120:123], a[232:235], v[40:43]\n" \
  "v_mfma_f32_16x16x32_bf16 v[44:47], v[120:123], a[236:239], v[44:47]\n" \
  "v_mfma_f32_16x16x32_bf16 v[40:43], v[124:127], a[248:251], v[40:43]\n" \
  "v_mfma_f32_16x16x32_bf16 v[44:47], v[124:127], a[252:255], v[44:47]\n" \
  "s_nop 7\n" \
  "v_cvt_pk_bf16_f32 v56, v40, v44\n" \
  "v_cvt_pk_bf16_f32 v51, v41, v45\n" \
  "v_cvt_pk_bf16_f32 v49, v40, v44\n" \
  "v_cvt_pk_bf16_f32 v57, v42, v46\n" \
  "v_cvt_pk_bf16_f32 v55, v43, v47\n" \
  "v_cvt_pk_bf16_f32 v53, v42, v46\n" \
  "v_mov_b32_dpp v49, v51 row_ror:8 row_mask:0xf bank_mask:0xc\n" \
  "v_mov_b32_dpp v51, v56 row_ror:8 row_mask:0xf bank_mask:0x3\n" \
  "buffer_store_dwordx4 v[48:51], v12, s[36:39], s66 offen offset:0 sc1\n" \
  "v_mov_b32_dpp v53, v55 row_ror:8 row_mask:0xf bank_mask:0xc\n" \
  "v_mov_b32_dpp v55, v57 row_ror:8 row_mask:0xf bank_mask:0x3\n" \
  "buffer_store_dwordx4 v[52:55], v12, s[36:39], s66 offen offset:2048 sc1\n" \
  "s_cmp_ge_u32 s60, 64\n" \
  "s_cbranch_scc1 .Lwo_last\n" \
  "s_waitcnt vmcnt(2) lgkmcnt(0)\n" \
  ".Lwo_p:\n" \
  "s_barrier\n" \
  "ds_read_b128 v[64:67], v8 offset:0\n" \
  "ds_read_b32 v31, v28\n" \
  "ds_read_b128 v[68:71], v8 offset:1024\n" \
  "ds_read_b128 v[72:75], v8 offset:2048\n" \
  "ds_read_b128 v[76:79], v8 offset:3072\n" \
  "s_waitcnt lgkmcnt(4)\n" \
  "v_mfma_f32_16x16x32_bf16 v[32:35], v[64:67], a[0:3], 0\n" \
  "ds_read_b128 v[80:83], v8 offset:8192\n" \
  "v_mfma_f32_16x16x32_bf16 v[36:39], v[64:67], a[4:7], 0\n" \
  "s_waitcnt lgkmcnt(4)\n" \
  "v_readfirstlane_b32 s69, v31\n" \
  "s_and_b32 s30, s69, 1\n" \
  "s_bitset0_b32 s69, 0\n" \
  "s_waitcnt lgkmcnt(3)\n" \
  "v_mfma_f32_16x16x32_bf16 v[32:35], v[68:71], a[16:19], v[32:35]\n" \
  "ds_read_b128 v[84:87], v8 offset:9216\n" \
  "s_add_u32 m0, s40, 0x8000\n" \
  "v_mfma_f32_16x16x32_bf16 v[36:39], v[68:71], a[20:23], v[36:39]\n" \
  "buffer_load_dwordx4 v0, s[36:39], s69 offen sc1 lds\n" \
  "s_waitcnt lgkmcnt(3)\n" \
  "v_mfma_f32_16x16x32_bf16 v[32:35], v[72:75], a[32:35], v[32:35]\n" \
  "ds_read_b128 v[88:91], v8 offset:10240\n" \
  "v_mfma_f32_16x16x32_bf16 v[36:39], v[72:75], a[36:39], v[36:39]\n" \
  "s_waitcnt lgkmcnt(3)\n" \
  "v_mfma_f32_16x16x32_bf16 v[32:35], v[76:79], a[48:51], v[32:35]\n" \
  "ds_read_b128 v[92:95], v8 offset:11264\n" \
  "s_add_u32 m0, s41, 0x8000\n" \
  "v_mfma_f32_16x16x32_bf16 v[36:39], v[76:79], a[52:55], v[36:39]\n" \
  "buffer_load_dwordx4 v1, s[36:39], s69 offen sc1 lds\n" \
  "s_waitcnt lgkmcnt(3)\n" \
  "v_mfma_f32_16x16x32_bf16 v[32:35], v[80:83], a[64:67], v[32:35]\n" \
  "ds_read_b128 v[96:99], v8 offset:16384\n" \
  "v_mfma_f32_16x16x32_bf16 v[36:39], v[80:83], a[68:71], v[36:39]\n" \
  "s_waitcnt lgkmcnt(3)\n" \
  "v_mfma_f32_16x16x32_bf16 v[32:35], v[84:87], a[80:83], v[32:35]\n" \
  "ds_read_b128 v[100:103], v8 offset:17408\n" \
  "s_add_u32 m0, s42, 0x8000\n" \
  "v_mfma_f32_16x16x32_bf16 v[36:39], v[84:87], a[84:87], v[36:39]\n" \
  "buffer_load_dwordx4 v2, s[36:39], s69 offen sc1 lds\n" \
  "s_waitcnt lgkmcnt(3)\n" \
  "v_mfma_f32_16x16x32_bf16 v[32:35], v[88:91], a[96:99], v[32:35]\n" \
  "ds_read_b128 v[104:107], v8 offset:18432\n" \
  "v_mfma_f32_16x16x32_bf16 v[36:39], v[88:91], a[100:103], v[36:39]\n" \
  "s_waitcnt lgkmcnt(3)\n" \
  "v_mfma_f32_16x16x32_bf16 v[32:35], v[92:95], a[112:115], v[32:35]\n" \
  "ds_read_b128 v[108:111], v8 offset:19456\n" \
  "s_add_u32 m0, s43, 0x8000\n" \
  "v_mfma_f32_16x16x32_bf16 v[36:39], v[92:95], a[116:119], v[36:39]\n" \
  "buffer_load_dwordx4 v3, s[36:39], s69 offen sc1 lds\n" \
  "s_waitcnt lgkmcnt(3)\n" \
  "v_mfma_f32_16x16x32_bf16 v[32:35], v[96:99], a[128:131], v[32:35]\n" \
  "ds_read_b128 v[112:115], v8 offset:24576\n" \
  "v_mfma_f32_16x16x32_bf16 v[36:39], v[96:99], a[132:135], v[36:39]\n" \
  "s_waitcnt lgkmcnt(3)\n" \
  "v_mfma_f32_16x16x32_bf16 v[32:35], v[100:103], a[144:147], v[32:35]\n" \
  "ds_read_b128 v[116:119], v8 offset:25600\n" \
  "s_add_u32 m0, s44, 0x8000\n" \
  "v_mfma_f32_16x16x32_bf16 v[36:39], v[100:103], a[148:151], v[36:39]\n" \
  "buffer_load_dwordx4 v4, s[36:39], s69 offen sc1 lds\n" \
  "s_waitcnt lgkmcnt(3)\n" \
  "v_mfma_f32_16x16x32_bf16 v[32:35], v[104:107], a[160:163], v[32:35]\n" \
  "ds_read_b128 v[120:123], v8 offset:26624\n" \
  "v_mfma_f32_16x16x32_bf16 v[36:39], v[104:107], a[164:167], v[36:39]\n" \
  "s_waitcnt lgkmcnt(3)\n" \
  "v_mfma_f32_16x16x32_bf16 v[32:35], v[108:111], a[176:179], v[32:35]\n" \
  "ds_read_b128 v[124:127], v8 offset:27648\n" \
  "s_add_u32 m0, s45, 0x8000\n" \
  "v_mfma_f32_16x16x32_bf16 v[36:39], v[108:111], a[180:183], v[36:39]\n" \
  "buffer_load_dwordx4 v5, s[36:39], s69 offen sc1 lds\n" \
  "s_waitcnt lgkmcnt(3)\n" \
  "v_mfma_f32_16x16x32_bf16 v[32:35], v[112:115], a[192:195], v[32:35]\n" \
  "v_mfma_f32_16x16x32_bf16 v[36:39], v[112:115], a[196:199], v[36:39]\n" \
  "s_waitcnt lgkmcnt(2)\n" \
  "v_mfma_f32_16x16x32_bf16 v[32:35], v[116:119], a[208:211], v[32:35]\n" \
  "s_add_u32 m0, s46, 0x8000\n" \
  "v_mfma_f32_16x16x32_bf16 v[36:39], v[116:119], a[212:215], v[36:39]\n" \
  "buffer_load_dwordx4 v6, s[36:39], s69 offen sc1 lds\n" \
  "s_waitcnt lgkmcnt(1)\n" \
  "v_mfma_f32_16x16x32_bf16 v[32:35], v[120:123], a[224:227], v[32:35]\n" \
  "v_mfma_f32_16x16x32_bf16 v[36:39], v[120:123], a[228:231], v[36:39]\n" \
  "s_waitcnt lgkmcnt(0)\n" \
  "v_mfma_f32_16x16x32_bf16 v[32:35], v[124:127], a[240:243], v[32:35]\n" \
  "s_add_u32 m0, s47, 0x8000\n" \
  "v_mfma_f32_16x16x32_bf16 v[36:39], v[124:127], a[244:247], v[36:39]\n" \
  "buffer_load_dwordx4 v7, s[36:39], s69 offen sc1 lds\n" \
  "v_mfma_f32_16x16x32_bf16 v[40:43], v[64:67], a[8:11], 0\n" \
  "v_mfma_f32_16x16x32_bf16 v[44:47], v[64:67], a[12:15], 0\n" \
  "v_mfma_f32_16x16x32_bf16 v[40:43], v[68:71], a[24:27], v[40:43]\n" \
  "v_cvt_pk_bf16_f32 v56, v32, v36\n" \
  "v_cvt_pk_bf16_f32 v50, v33, v37\n" \
  "v_mfma_f32_16x16x32_bf16 v[44:47], v[68:71], a[28:31], v[44:47]\n" \
  "v_cvt_pk_bf16_f32 v48, v32, v36\n" \
  "v_cvt_pk_bf16_f32 v57, v34, v38\n" \
  "v_mfma_f32_16x16x32_bf16 v[40:43], v[72:75], a[40:43], v[40:43]\n" \
  "v_cvt_pk_bf16_f32 v54, v35, v39\n" \
  "v_cvt_pk_bf16_f32 v52, v34, v38\n" \
  "v_mfma_f32_16x16x32_bf16 v[44:47], v[72:75], a[44:47], v[44:47]\n" \
  "v_mfma_f32_16x16x32_bf16 v[40:43], v[76:79], a[56:59], v[40:43]\n" \
  "v_mov_b32_dpp v48, v50 row_ror:8 row_mask:0xf bank_mask:0xc\n" \
  "v_mov_b32_dpp v50, v56 row_ror:8 row_mask:0xf bank_mask:0x3\n" \
  "v_mfma_f32_16x16x32_bf16 v[44:47], v[76:79], a[60:63], v[44:47]\n" \
  "v_mov_b32_dpp v52, v54 row_ror:8 row_mask:0xf bank_mask:0xc\n" \
  "v_mov_b32_dpp v54, v57 row_ror:8 row_mask:0xf bank_mask:0x3\n" \
  "v_mfma_f32_16x16x32_bf16 v[40:43], v[80:83], a[72:75], v[40:43]\n" \
  "v_mfma_f32_16x16x32_bf16 v[44:47], v[80:83], a[76:79], v[44:47]\n" \
  "v_mfma_f32_16x16x32_bf16 v[40:43], v[84:87], a[88:91], v[40:43]\n" \
  "v_mfma_f32_16x16x32_bf16 v[44:47], v[84:87], a[92:95], v[44:47]\n" \
  "v_mfma_f32_16x16x32_bf16 v[40:43], v[88:91], a[104:107], v[40:43]\n" \
  "v_mfma_f32_16x16x32_bf16 v[44:47], v[88:91], a[108:111], v[44:47]\n" \
  "v_mfma_f32_16x16x32_bf16 v[40:43], v[92:95], a[120:123], v[40:43]\n" \
  "v_mfma_f32_16x16x32_bf16 v[44:47], v[92:95], a[124:127], v[44:47]\n" \
  "v_mfma_f32_16x16x32_bf16 v[40:43], v[96:99], a[136:139], v[40:43]\n" \
  "v_mfma_f32_16x16x32_bf16 v[44:47], v[96:99], a[140:143], v[44:47]\n" \
  "v_mfma_f32_16x16x32_bf16 v[40:43], v[100:103], a[152:155], v[40:43]\n" \
  "v_mfma_f32_16x16x32_bf16 v[44:47], v[100:103], a[156:159], v[44:47]\n" \
  "v_mfma_f32_16x16x32_bf16 v[40:43], v[104:107], a[168:171], v[40:43]\n" \
  "v_mfma_f32_16x16x32_bf16 v[44:47], v[104:107], a[172:175], v[44:47]\n" \
  "v_mfma_f32_16x16x32_bf16 v[40:43], v[108:111], a[184:187], v[40:43]\n" \
  "v_mfma_f32_16x16x32_bf16 v[44:47], v[108:111], a[188:191], v[44:47]\n" \
  "v_mfma_f32_16x16x32_bf16 v[40:43], v[112:115], a[200:203], v[40:43]\n" \
  "v_mfma_f32_16x16x32_bf16 v[44:47], v[112:115], a[204:207], v[44:47]\n" \
  "v_mfma_f32_16x16x32_bf16 v[40:43], v[116:119], a[216:219], v[40:43]\n" \
  "v_mfma_f32_16x16x32_bf16 v[44:47], v[116:119], a[220:223], v[44:47]\n" \
  "v_mfma_f32_16x16x32_bf16 v[40:43], v[120:123], a[232:235], v[40:43]\n" \
  "v_mfma_f32_16x16x32_bf16 v[44:47], v[120:123], a[236:239], v[44:47]\n" \
  "v_mfma_f32_16x16x32_bf16 v[40:43], v[124:127], a[248:251], v[40:43]\n" \
  "v_mfma_f32_16x16x32_bf16 v[44:47], v[124:127], a[252:255], v[44:47]\n" \
  "s_nop 7\n" \
  "v_cvt_pk_bf16_f32 v56, v40, v44\n" \
  "v_cvt_pk_bf16_f32 v51, v41, v45\n" \
  "v_cvt_pk_bf16_f32 v49, v40, v44\n" \
  "v_cvt_pk_bf16_f32 v57, v42, v46\n" \
  "v_cvt_pk_bf16_f32 v55, v43, v47\n" \
  "v_cvt_pk_bf16_f32 v53, v42, v46\n" \
  "v_mov_b32_dpp v49, v51 row_ror:8 row_mask:0xf bank_mask:0xc\n" \
  "v_mov_b32_dpp v51, v56 row_ror:8 row_mask:0xf bank_mask:0x3\n" \
  "buffer_store_dwordx4 v[48:51], v12, s[36:39], s62 offen offset:0 sc1\n" \
  "v_mov_b32_dpp v53, v55 row_ror:8 row_mask:0xf bank_mask:0xc\n" \
  "v_mov_b32_dpp v55, v57 row_ror:8 row_mask:0xf bank_mask:0x3\n" \
  "buffer_store_dwordx4 v[52:55], v12, s[36:39], s62 offen offset:2048 sc1\n" \
  ".Lwo_sel:\n" \
  "s_mov_b32 s76, s59\n" \
  "s_cmp_lg_u32 s69, 0x40000000\n" \
  "s_cselect_b32 s30, s30, 0\n" \
  "s_addc_u32 s59, s59, 0\n" \
  "s_cmp_lt_u32 s58, 64\n" \
  "s_cselect_b64 s[78:79], -1, 0\n" \
  "v_cndmask_b32_e64 v30, v14, v13, s[78:79]\n" \
  "buffer_load_dword v62, v30, s[36:39], 0 offen sc1\n" \
  "s_cselect_b32 s60, s58, s59\n" \
  "s_cselect_b32 s75, s48, s49\n" \
  "s_cselect_b32 s71, 0, 1\n" \
  "v_cndmask_b32_e64 v32, v17, v16, s[78:79]\n" \
  "v_cndmask_b32_e64 v33, v19, v18, s[78:79]\n" \
  "s_addc_u32 s58, s58, 0\n" \
  "s_cmp_lg_u32 s30, 0\n" \
  "s_cselect_b32 s60, 64, s60\n" \
  "s_cselect_b32 s71, 0, s71\n" \
  "s_add_u32 s59, s59, s71\n" \
  "s_mov_b32 s66, s65\n" \
  "v_readlane_b32 s61, v32, s60\n" \
  "v_readlane_b32 s62, v33, s60\n" \
  "v_readlane_b32 s64, v17, s59\n" \
  "v_readlane_b32 s65, v19, s59\n" \
  "s_mov_b32 s63, s59\n" \
  "s_add_u32 s31, s59, 1\n" \
  "s_cmp_lt_u32 s58, 64\n" \
  "s_cselect_b32 s31, -1, s31\n" \
  "s_cmp_lt_u32 s60, 64\n" \
  "s_cselect_b32 s77, s60, -1\n" \
  "s_cselect_b32 s61, s61, 0x40000000\n" \
  "s_cselect_b32 s75, s75, 0x40000000\n" \
  "s_cselect_b32 s71, s71, 0\n" \
  "s_cmp_eq_u32 s71, 0\n" \
  "s_cselect_b32 s72, s49, 0x40000000\n" \
  "s_waitcnt vmcnt(1)\n" \
  "s_barrier\n" \
  "buffer_store_dword v60, v29, s[36:39], s67 offen sc1\n" \
  "buffer_store_dword v61, v29, s[36:39], s68 offen sc1\n" \
  "s_waitcnt vmcnt(2)\n" \
  "v_mov_b32 v60, s60\n" \
  "v_mov_b32 v61, s76\n" \
  "s_mov_b32 s67, s75\n" \
  "s_mov_b32 s68, s72\n" \
  "s_cmp_lg_u32 s69, 0x40000000\n" \
  "s_cbranch_scc1 .Lwo_q\n" \
  "s_mov_b32 s68, 0x40000000\n" \
  "s_cmp_ge_u32 s60, 64\n" \
  "s_cbranch_scc1 .Lwo_end\n" \
  "s_mov_b32 m0, s40\n" \
  "s_branch .Lwo_pn_c\n" \
  ".Lwo_pn:\n" \
  ".Lwo_pn_f:\n" \
  "buffer_load_dword v62, v30, s[36:39], 0 offen sc1\n" \
  "s_sub_u32 s70, s70, 1\n" \
  "s_cselect_b32 s77, -1, s77\n" \
  "s_cselect_b32 s70, 0, s70\n" \
  "s_waitcnt vmcnt(0)\n" \
  ".Lwo_pn_c:\n" \
  "v_cmp_gt_i32 vcc, s77, v62\n" \
  "s_and_b32 vcc_lo, vcc_lo, s29\n" \
  "s_cbranch_scc1 .Lwo_pn\n" \
  "s_barrier\n" \
  "buffer_load_dwordx4 v0, s[36:39], s61 offen sc1 lds\n" \
  "s_mov_b32 m0, s41\n" \
  "v_cmp_gt_i32 vcc, s63, v62\n" \
  "buffer_load_dwordx4 v1, s[36:39], s61 offen sc1 lds\n" \
  "s_mov_b32 m0, s42\n" \
  "s_cmp_eq_u32 vcc_hi, 0\n" \
  "buffer_load_dwordx4 v2, s[36:39], s61 offen sc1 lds\n" \
  "s_mov_b32 m0, s43\n" \
  "s_cselect_b32 s72, s64, 0x40000000\n" \
  "buffer_load_dwordx4 v3, s[36:39], s61 offen sc1 lds\n" \
  "s_mov_b32 m0, s44\n" \
  "v_cmp_gt_i32 vcc, s31, v62\n" \
  "s_cmp_lg_u32 vcc_hi, 0\n" \
  "s_addc_u32 s72, s72, 0\n" \
  "v_mov_b32 v31, s72\n" \
  "buffer_load_dwordx4 v4, s[36:39], s61 offen sc1 lds\n" \
  "s_mov_b32 m0, s45\n" \
  "ds_write_b32 v15, v31\n" \
  "buffer_load_dwordx4 v5, s[36:39], s61 offen sc1 lds\n" \
  "s_mov_b32 m0, s46\n" \
  "s_nop 0\n" \
  "buffer_load_dwordx4 v6, s[36:39], s61 offen sc1 lds\n" \
  "s_mov_b32 m0, s47\n" \
  "s_nop 0\n" \
  "buffer_load_dwordx4 v7, s[36:39], s61 offen sc1 lds\n" \
  "s_waitcnt vmcnt(0) lgkmcnt(0)\n" \
  "s_branch .Lwo_p\n" \
  ".Lwo_last:\n" \
  "s_mov_b32 s69, 0x40000000\n" \
  "s_branch .Lwo_sel\n" \
  ".Lwo_end:\n" \
  "s_endpgm\n"
#define ASM_FF \
  "s_nop 4\n" \
  "s_mov_b32 s58, 0\n" \
  "s_mov_b32 s59, 0\n" \
  "s_mov_b32 s70, 100000\n" \
  "s_mov_b32 s69, 0x40000000\n" \
  "s_mov_b32 s67, 0x40000000\n" \
  "s_mov_b32 s68, 0x40000000\n" \
  "v_mov_b32 v60, 0\n" \
  "v_mov_b32 v61, 0\n" \
  "v_cmp_eq_u32 vcc, v15, v28\n" \
  "s_mov_b32 s29, vcc_lo\n" \
  "ds_read_b64_tr_b16 a[0:1], v20 offset:0\n" \
  "ds_read_b64_tr_b16 a[2:3], v21 offset:0\n" \
  "ds_read_b64_tr_b16 a[4:5], v22 offset:0\n" \
  "ds_read_b64_tr_b16 a[6:7], v23 offset:0\n" \
  "ds_read_b64_tr_b16 a[8:9], v24 offset:0\n" \
  "ds_read_b64_tr_b16 a[10:11], v25 offset:0\n" \
  "ds_read_b64_tr_b16 a[12:13], v26 offset:0\n" \
  "ds_read_b64_tr_b16 a[14:15], v27 offset:0\n" \
  "ds_read_b64_tr_b16 a[16:17], v20 offset:8192\n" \
  "ds_read_b64_tr_b16 a[18:19], v21 offset:8192\n" \
  "ds_read_b64_tr_b16 a[20:21], v22 offset:8192\n" \
  "ds_read_b64_tr_b16 a[22:23], v23 offset:8192\n" \
  "ds_read_b64_tr_b16 a[24:25], v24 offset:8192\n" \
  "ds_read_b64_tr_b16 a[26:27], v25 offset:8192\n" \
  "ds_read_b64_tr_b16 a[28:29], v26 offset:8192\n" \
  "ds_read_b64_tr_b16 a[30:31], v27 offset:8192\n" \
  "ds_read_b64_tr_b16 a[32:33], v20 offset:16384\n" \
  "ds_read_b64_tr_b16 a[34:35], v21 offset:16384\n" \
  "ds_read_b64_tr_b16 a[36:37], v22 offset:16384\n" \
  "ds_read_b64_tr_b16 a[38:39], v23 offset:16384\n" \
  "ds_read_b64_tr_b16 a[40:41], v24 offset:16384\n" \
  "ds_read_b64_tr_b16 a[42:43], v25 offset:16384\n" \
  "ds_read_b64_tr_b16 a[44:45], v26 offset:16384\n" \
  "ds_read_b64_tr_b16 a[46:47], v27 offset:16384\n" \
  "ds_read_b64_tr_b16 a[48:49], v20 offset:24576\n" \
  "ds_read_b64_tr_b16 a[50:51], v21 offset:24576\n" \
  "ds_read_b64_tr_b16 a[52:53], v22 offset:24576\n" \
  "ds_read_b64_tr_b16 a[54:55], v23 offset:24576\n" \
  "ds_read_b64_tr_b16 a[56:57], v24 offset:24576\n" \
  "ds_read_b64_tr_b16 a[58:59], v25 offset:24576\n" \
  "ds_read_b64_tr_b16 a[60:61], v26 offset:24576\n" \
  "ds_read_b64_tr_b16 a[62:63], v27 offset:24576\n" \
  "ds_read_b64_tr_b16 a[64:65], v20 offset:32768\n" \
  "ds_read_b64_tr_b16 a[66:67], v21 offset:32768\n" \
  "ds_read_b64_tr_b16 a[68:69], v22 offset:32768\n" \
  "ds_read_b64_tr_b16 a[70:71], v23 offset:32768\n" \
  "ds_read_b64_tr_b16 a[72:73], v24 offset:32768\n" \
  "ds_read_b64_tr_b16 a[74:75], v25 offset:32768\n" \
  "ds_read_b64_tr_b16 a[76:77], v26 offset:32768\n" \
  "ds_read_b64_tr_b16 a[78:79], v27 offset:32768\n" \
  "ds_read_b64_tr_b16 a[80:81], v20 offset:40960\n" \
  "ds_read_b64_tr_b16 a[82:83], v21 offset:40960\n" \
  "ds_read_b64_tr_b16 a[84:85], v22 offset:40960\n" \
  "ds_read_b64_tr_b16 a[86:87], v23 offset:40960\n" \
  "ds_read_b64_tr_b16 a[88:89], v24 offset:40960\n" \
  "ds_read_b64_tr_b16 a[90:91], v25 offset:40960\n" \
  "ds_read_b64_tr_b16 a[92:93], v26 offset:40960\n" \
  "ds_read_b64_tr_b16 a[94:95], v27 offset:40960\n" \
  "ds_read_b64_tr_b16 a[96:97], v20 offset:49152\n" \
  "ds_read_b64_tr_b16 a[98:99], v21 offset:49152\n" \
  "ds_read_b64_tr_b16 a[100:101], v22 offset:49152\n" \
  "ds_read_b64_tr_b16 a[102:103], v23 offset:49152\n" \
  "ds_read_b64_tr_b16 a[104:105], v24 offset:49152\n" \
  "ds_read_b64_tr_b16 a[106:107], v25 offset:49152\n" \
  "ds_read_b64_tr_b16 a[108:109], v26 offset:49152\n" \
  "ds_read_b64_tr_b16 a[110:111], v27 offset:49152\n" \
  "ds_read_b64_tr_b16 a[112:113], v20 offset:57344\n" \
  "ds_read_b64_tr_b16 a[114:115], v21 offset:57344\n" \
  "ds_read_b64_tr_b16 a[116:117], v22 offset:57344\n" \
  "ds_read_b64_tr_b16 a[118:119], v23 offset:57344\n" \
  "ds_read_b64_tr_b16 a[120:121], v24 offset:57344\n" \
  "ds_read_b64_tr_b16 a[122:123], v25 offset:57344\n" \
  "ds_read_b64_tr_b16 a[124:125], v26 offset:57344\n" \
  "ds_read_b64_tr_b16 a[126:127], v27 offset:57344\n" \
  "v_add_u32 v20, 0x10000, v20\n" \
  "v_add_u32 v21, 0x10000, v21\n" \
  "v_add_u32 v22, 0x10000, v22\n" \
  "v_add_u32 v23, 0x10000, v23\n" \
  "v_add_u32 v24, 0x10000, v24\n" \
  "v_add_u32 v25, 0x10000, v25\n" \
  "v_add_u32 v26, 0x10000, v26\n" \
  "v_add_u32 v27, 0x10000, v27\n" \
  "ds_read_b64_tr_b16 a[128:129], v20 offset:0\n" \
  "ds_read_b64_tr_b16 a[130:131], v21 offset:0\n" \
  "ds_read_b64_tr_b16 a[132:133], v22 offset:0\n" \
  "ds_read_b64_tr_b16 a[134:135], v23 offset:0\n" \
  "ds_read_b64_tr_b16 a[136:137], v24 offset:0\n" \
  "ds_read_b64_tr_b16 a[138:139], v25 offset:0\n" \
  "ds_read_b64_tr_b16 a[140:141], v26 offset:0\n" \
  "ds_read_b64_tr_b16 a[142:143], v27 offset:0\n" \
  "ds_read_b64_tr_b16 a[144:145], v20 offset:8192\n" \
  "ds_read_b64_tr_b16 a[146:147], v21 offset:8192\n" \
  "ds_read_b64_tr_b16 a[148:149], v22 offset:8192\n" \
  "ds_read_b64_tr_b16 a[150:151], v23 offset:8192\n" \
  "ds_read_b64_tr_b16 a[152:153], v24 offset:8192\n" \
  "ds_read_b64_tr_b16 a[154:155], v25 offset:8192\n" \
  "ds_read_b64_tr_b16 a[156:157], v26 offset:8192\n" \
  "ds_read_b64_tr_b16 a[158:159], v27 offset:8192\n" \
  "ds_read_b64_tr_b16 a[160:161], v20 offset:16384\n" \
  "ds_read_b64_tr_b16 a[162:163], v21 offset:16384\n" \
  "ds_read_b64_tr_b16 a[164:165], v22 offset:16384\n" \
  "ds_read_b64_tr_b16 a[166:167], v23 offset:16384\n" \
  "ds_read_b64_tr_b16 a[168:169], v24 offset:16384\n" \
  "ds_read_b64_tr_b16 a[170:171], v25 offset:16384\n" \
  "ds_read_b64_tr_b16 a[172:173], v26 offset:16384\n" \
  "ds_read_b64_tr_b16 a[174:175], v27 offset:16384\n" \
  "ds_read_b64_tr_b16 a[176:177], v20 offset:24576\n" \
  "ds_read_b64_tr_b16 a[178:179], v21 offset:24576\n" \
  "ds_read_b64_tr_b16 a[180:181], v22 offset:24576\n" \
  "ds_read_b64_tr_b16 a[182:183], v23 offset:24576\n" \
  "ds_read_b64_tr_b16 a[184:185], v24 offset:24576\n" \
  "ds_read_b64_tr_b16 a[186:187], v25 offset:24576\n" \
  "ds_read_b64_tr_b16 a[188:189], v26 offset:24576\n" \
  "ds_read_b64_tr_b16 a[190:191], v27 offset:24576\n" \
  "ds_read_b64_tr_b16 a[192:193], v20 offset:32768\n" \
  "ds_read_b64_tr_b16 a[194:195], v21 offset:32768\n" \
  "ds_read_b64_tr_b16 a[196:197], v22 offset:32768\n" \
  "ds_read_b64_tr_b16 a[198:199], v23 offset:32768\n" \
  "ds_read_b64_tr_b16 a[200:201], v24 offset:32768\n" \
  "ds_read_b64_tr_b16 a[202:203], v25 offset:32768\n" \
  "ds_read_b64_tr_b16 a[204:205], v26 offset:32768\n" \
  "ds_read_b64_tr_b16 a[206:207], v27 offset:32768\n" \
  "ds_read_b64_tr_b16 a[208:209], v20 offset:40960\n" \
  "ds_read_b64_tr_b16 a[210:211], v21 offset:40960\n" \
  "ds_read_b64_tr_b16 a[212:213], v22 offset:40960\n" \
  "ds_read_b64_tr_b16 a[214:215], v23 offset:40960\n" \
  "ds_read_b64_tr_b16 a[216:217], v24 offset:40960\n" \
  "ds_read_b64_tr_b16 a[218:219], v25 offset:40960\n" \
  "ds_read_b64_tr_b16 a[220:221], v26 offset:40960\n" \
  "ds_read_b64_tr_b16 a[222:223], v27 offset:40960\n" \
  "ds_read_b64_tr_b16 a[224:225], v20 offset:49152\n" \
  "ds_read_b64_tr_b16 a[226:227], v21 offset:49152\n" \
  "ds_read_b64_tr_b16 a[228:229], v22 offset:49152\n" \
  "ds_read_b64_tr_b16 a[230:231], v23 offset:49152\n" \
  "ds_read_b64_tr_b16 a[232:233], v24 offset:49152\n" \
  "ds_read_b64_tr_b16 a[234:235], v25 offset:49152\n" \
  "ds_read_b64_tr_b16 a[236:237], v26 offset:49152\n" \
  "ds_read_b64_tr_b16 a[238:239], v27 offset:49152\n" \
  "ds_read_b64_tr_b16 a[240:241], v20 offset:57344\n" \
  "ds_read_b64_tr_b16 a[242:243], v21 offset:57344\n" \
  "ds_read_b64_tr_b16 a[244:245], v22 offset:57344\n" \
  "ds_read_b64_tr_b16 a[246:247], v23 offset:57344\n" \
  "ds_read_b64_tr_b16 a[248:249], v24 offset:57344\n" \
  "ds_read_b64_tr_b16 a[250:251], v25 offset:57344\n" \
  "ds_read_b64_tr_b16 a[252:253], v26 offset:57344\n" \
  "ds_read_b64_tr_b16 a[254:255], v27 offset:57344\n" \
  "s_mov_b32 s73, 0xbfb8aa3b\n" \
  "s_waitcnt vmcnt(0) lgkmcnt(0)\n" \
  "s_branch .Lff_sel\n" \
  ".Lff_q:\n" \
  "ds_read_b128 v[64:67], v8 offset:32768\n" \
  "ds_read_b128 v[68:71], v9 offset:32768\n" \
  "ds_read_b128 v[72:75], v10 offset:32768\n" \
  "ds_read_b128 v[76:79], v11 offset:32768\n" \
  "ds_read_b128 v[80:83], v8 offset:33024\n" \
  "ds_read_b128 v[84:87], v9 offset:33024\n" \
  "ds_read_b128 v[88:91], v10 offset:33024\n" \
  "ds_read_b128 v[92:95], v11 offset:33024\n" \
  "ds_read_b128 v[96:99], v8 offset:33280\n" \
  "ds_read_b128 v[100:103], v9 offset:33280\n" \
  "ds_read_b128 v[104:107], v10 offset:33280\n" \
  "ds_read_b128 v[108:111], v11 offset:33280\n" \
  "ds_read_b128 v[112:115], v8 offset:33536\n" \
  "ds_read_b128 v[116:119], v9 offset:33536\n" \
  "ds_read_b128 v[120:123], v10 offset:33536\n" \
  "ds_read_b128 v[124:127], v11 offset:33536\n" \
  "s_mov_b32 m0, s40\n" \
  "s_branch .Lff_pq_c\n" \
  ".Lff_pq:\n" \
  ".Lff_pq_f:\n" \
  "buffer_load_dword v62, v30, s[36:39], 0 offen sc1\n" \
  "s_sub_u32 s70, s70, 1\n" \
  "s_cselect_b32 s77, -1, s77\n" \
  "s_cselect_b32 s70, 0, s70\n" \
  "s_waitcnt vmcnt(0)\n" \
  ".Lff_pq_c:\n" \
  "v_cmp_gt_i32 vcc, s77, v62\n" \
  "s_and_b32 vcc_lo, vcc_lo, s29\n" \
  "s_cbranch_scc1 .Lff_pq\n" \
  "s_barrier\n" \
  "buffer_load_dwordx4 v0, s[36:39], s61 offen sc1 lds\n" \
  "s_waitcnt lgkmcnt(14)\n" \
  "v_mfma_f32_16x16x32_bf16 v[32:35], a[0:3], v[64:67], 0\n" \
  "v_mfma_f32_16x16x32_bf16 v[36:39], a[4:7], v[64:67], 0\n" \
  "s_mov_b32 m0, s41\n" \
  "v_mfma_f32_16x16x32_bf16 v[32:35], a[16:19], v[68:71], v[32:35]\n" \
  "buffer_load_dwordx4 v1, s[36:39], s61 offen sc1 lds\n" \
  "v_mfma_f32_16x16x32_bf16 v[36:39], a[20:23], v[68:71], v[36:39]\n" \
  "s_waitcnt lgkmcnt(13)\n" \
  "v_mfma_f32_16x16x32_bf16 v[32:35], a[32:35], v[72:75], v[32:35]\n" \
  "s_mov_b32 m0, s42\n" \
  "v_mfma_f32_16x16x32_bf16 v[36:39], a[36:39], v[72:75], v[36:39]\n" \
  "buffer_load_dwordx4 v2, s[36:39], s61 offen sc1 lds\n" \
  "s_waitcnt lgkmcnt(12)\n" \
  "v_mfma_f32_16x16x32_bf16 v[32:35], a[48:51], v[76:79], v[32:35]\n" \
  "v_mfma_f32_16x16x32_bf16 v[36:39], a[52:55], v[76:79], v[36:39]\n" \
  "s_mov_b32 m0, s43\n" \
  "s_waitcnt lgkmcnt(11)\n" \
  "v_mfma_f32_16x16x32_bf16 v[32:35], a[64:67], v[80:83], v[32:35]\n" \
  "buffer_load_dwordx4 v3, s[36:39], s61 offen sc1 lds\n" \
  "v_mfma_f32_16x16x32_bf16 v[36:39], a[68:71], v[80:83], v[36:39]\n" \
  "s_waitcnt lgkmcnt(10)\n" \
  "v_mfma_f32_16x16x32_bf16 v[32:35], a[80:83], v[84:87], v[32:35]\n" \
  "s_mov_b32 m0, s44\n" \
  "v_mfma_f32_16x16x32_bf16 v[36:39], a[84:87], v[84:87], v[36:39]\n" \
  "buffer_load_dwordx4 v4, s[36:39], s61 offen sc1 lds\n" \
  "s_waitcnt lgkmcnt(9)\n" \
  "v_mfma_f32_16x16x32_bf16 v[32:35], a[96:99], v[88:91], v[32:35]\n" \
  "v_mfma_f32_16x16x32_bf16 v[36:39], a[100:103], v[88:91], v[36:39]\n" \
  "s_mov_b32 m0, s45\n" \
  "s_waitcnt lgkmcnt(8)\n" \
  "v_mfma_f32_16x16x32_bf16 v[32:35], a[112:115], v[92:95], v[32:35]\n" \
  "buffer_load_dwordx4 v5, s[36:39], s61 offen sc1 lds\n" \
  "v_mfma_f32_16x16x32_bf16 v[36:39], a[116:119], v[92:95], v[36:39]\n" \
  "s_waitcnt lgkmcnt(7)\n" \
  "v_mfma_f32_16x16x32_bf16 v[32:35], a[128:131], v[96:99], v[32:35]\n" \
  "s_mov_b32 m0, s46\n" \
  "v_mfma_f32_16x16x32_bf16 v[36:39], a[132:135], v[96:99], v[36:39]\n" \
  "buffer_load_dwordx4 v6, s[36:39], s61 offen sc1 lds\n" \
  "s_waitcnt lgkmcnt(6)\n" \
  "v_mfma_f32_16x16x32_bf16 v[32:35], a[144:147], v[100:103], v[32:35]\n" \
  "v_mfma_f32_16x16x32_bf16 v[36:39], a[148:151], v[100:103], v[36:39]\n" \
  "s_mov_b32 m0, s47\n" \
  "s_waitcnt lgkmcnt(5)\n" \
  "v_mfma_f32_16x16x32_bf16 v[32:35], a[160:163], v[104:107], v[32:35]\n" \
  "buffer_load_dwordx4 v7, s[36:39], s61 offen sc1 lds\n" \
  "v_mfma_f32_16x16x32_bf16 v[36:39], a[164:167], v[104:107], v[36:39]\n" \
  "s_waitcnt lgkmcnt(4)\n" \
  "v_mfma_f32_16x16x32_bf16 v[32:35], a[176:179], v[108:111], v[32:35]\n" \
  "v_mfma_f32_16x16x32_bf16 v[36:39], a[180:183], v[108:111], v[36:39]\n" \
  "s_waitcnt lgkmcnt(3)\n" \
  "v_mfma_f32_16x16x32_bf16 v[32:35], a[192:195], v[112:115], v[32:35]\n" \
  "v_cmp_gt_i32 vcc, s63, v62\n" \
  "s_cmp_eq_u32 vcc_hi, 0\n" \
  "v_mfma_f32_16x16x32_bf16 v[36:39], a[196:199], v[112:115], v[36:39]\n" \
  "s_cselect_b32 s72, s64, 0x40000000\n" \
  "v_cmp_gt_i32 vcc, s31, v62\n" \
  "s_cmp_lg_u32 vcc_hi, 0\n" \
  "s_addc_u32 s72, s72, 0\n" \
  "v_mov_b32 v31, s72\n" \
  "s_waitcnt lgkmcnt(2)\n" \
  "v_mfma_f32_16x16x32_bf16 v[32:35], a[208:211], v[116:119], v[32:35]\n" \
  "ds_write_b32 v15, v31\n" \
  "v_mfma_f32_16x16x32_bf16 v[36:39], a[212:215], v[116:119], v[36:39]\n" \
  "s_waitcnt lgkmcnt(2)\n" \
  "v_mfma_f32_16x16x32_bf16 v[32:35], a[224:227], v[120:123], v[32:35]\n" \
  "v_mfma_f32_16x16x32_bf16 v[36:39], a[228:231], v[120:123], v[36:39]\n" \
  "s_waitcnt lgkmcnt(1)\n" \
  "v_mfma_f32_16x16x32_bf16 v[32:35], a[240:243], v[124:127], v[32:35]\n" \
  "v_mfma_f32_16x16x32_bf16 v[36:39], a[244:247], v[124:127], v[36:39]\n" \
  "v_mfma_f32_16x16x32_bf16 v[40:43], a[8:11], v[64:67], 0\n" \
  "v_mfma_f32_16x16x32_bf16 v[40:43], a[24:27], v[68:71], v[40:43]\n" \
  "v_mul_f32 v56, s73, v32\n" \
  "v_mul_f32 v57, s73, v33\n" \
  "v_mfma_f32_16x16x32_bf16 v[40:43], a[40:43], v[72:75], v[40:43]\n" \
  "v_mul_f32 v58, s73, v34\n" \
  "v_mul_f32 v59, s73, v35\n" \
  "v_mfma_f32_16x16x32_bf16 v[40:43], a[56:59], v[76:79], v[40:43]\n" \
  "v_exp_f32 v56, v56\n" \
  "v_mfma_f32_16x16x32_bf16 v[40:43], a[72:75], v[80:83], v[40:43]\n" \
  "v_exp_f32 v57, v57\n" \
  "v_mfma_f32_16x16x32_bf16 v[40:43], a[88:91], v[84:87], v[40:43]\n" \
  "v_exp_f32 v58, v58\n" \
  "v_mfma_f32_16x16x32_bf16 v[40:43], a[104:107], v[88:91], v[40:43]\n" \
  "v_exp_f32 v59, v59\n" \
  "v_mfma_f32_16x16x32_bf16 v[40:43], a[120:123], v[92:95], v[40:43]\n" \
  "v_add_f32 v56, 1.0, v56\n" \
  "v_add_f32 v57, 1.0, v57\n" \
  "v_mfma_f32_16x16x32_bf16 v[40:43], a[136:139], v[96:99], v[40:43]\n" \
  "v_add_f32 v58, 1.0, v58\n" \
  "v_add_f32 v59, 1.0, v59\n" \
  "v_mfma_f32_16x16x32_bf16 v[40:43], a[152:155], v[100:103], v[40:43]\n" \
  "v_rcp_f32 v56, v56\n" \
  "v_mfma_f32_16x16x32_bf16 v[40:43], a[168:171], v[104:107], v[40:43]\n" \
  "v_rcp_f32 v57, v57\n" \
  "v_mfma_f32_16x16x32_bf16 v[40:43], a[184:187], v[108:111], v[40:43]\n" \
  "v_rcp_f32 v58, v58\n" \
  "v_mfma_f32_16x16x32_bf16 v[40:43], a[200:203], v[112:115], v[40:43]\n" \
  "v_rcp_f32 v59, v59\n" \
  "v_mfma_f32_16x16x32_bf16 v[40:43], a[216:219], v[116:119], v[40:43]\n" \
  "v_mul_f32 v56, v56, v32\n" \
  "v_mul_f32 v57, v57, v33\n" \
  "v_mfma_f32_16x16x32_bf16 v[40:43], a[232:235], v[120:123], v[40:43]\n" \
  "v_mul_f32 v58, v58, v34\n" \
  "v_mul_f32 v59, v59, v35\n" \
  "v_mfma_f32_16x16x32_bf16 v[40:43], a[248:251], v[124:127], v[40:43]\n" \
  "v_mul_f32 v56, v56, v36\n" \
  "v_mul_f32 v57, v57, v37\n" \
  "v_mfma_f32_16x16x32_bf16 v[44:47], a[12:15], v[64:67], 0\n" \
  "v_mul_f32 v58, v58, v38\n" \
  "v_mul_f32 v59, v59, v39\n" \
  "v_mfma_f32_16x16x32_bf16 v[44:47], a[28:31], v[68:71], v[44:47]\n" \
  "v_cvt_pk_bf16_f32 v48, v56, v57\n" \
  "v_cvt_pk_bf16_f32 v49, v58, v59\n" \
  "v_mfma_f32_16x16x32_bf16 v[44:47], a[44:47], v[72:75], v[44:47]\n" \
  "v_mul_f32 v56, s73, v40\n" \
  "v_mul_f32 v57, s73, v41\n" \
  "v_mfma_f32_16x16x32_bf16 v[44:47], a[60:63], v[76:79], v[44:47]\n" \
  "v_mul_f32 v58, s73, v42\n" \
  "v_mul_f32 v59, s73, v43\n" \
  "v_mfma_f32_16x16x32_bf16 v[44:47], a[76:79], v[80:83], v[44:47]\n" \
  "v_exp_f32 v56, v56\n" \
  "v_mfma_f32_16x16x32_bf16 v[44:47], a[92:95], v[84:87], v[44:47]\n" \
  "v_exp_f32 v57, v57\n" \
  "v_mfma_f32_16x16x32_bf16 v[44:47], a[108:111], v[88:91], v[44:47]\n" \
  "v_exp_f32 v58, v58\n" \
  "v_mfma_f32_16x16x32_bf16 v[44:47], a[124:127], v[92:95], v[44:47]\n" \
  "v_exp_f32 v59, v59\n" \
  "v_mfma_f32_16x16x32_bf16 v[44:47], a[140:143], v[96:99], v[44:47]\n" \
  "v_add_f32 v56, 1.0, v56\n" \
  "v_add_f32 v57, 1.0, v57\n" \
  "v_mfma_f32_16x16x32_bf16 v[44:47], a[156:159], v[100:103], v[44:47]\n" \
  "v_add_f32 v58, 1.0, v58\n" \
  "v_add_f32 v59, 1.0, v59\n" \
  "v_mfma_f32_16x16x32_bf16 v[44:47], a[172:175], v[104:107], v[44:47]\n" \
  "v_rcp_f32 v56, v56\n" \
  "v_mfma_f32_16x16x32_bf16 v[44:47], a[188:191], v[108:111], v[44:47]\n" \
  "v_rcp_f32 v57, v57\n" \
  "v_mfma_f32_16x16x32_bf16 v[44:47], a[204:207], v[112:115], v[44:47]\n" \
  "v_rcp_f32 v58, v58\n" \
  "v_mfma_f32_16x16x32_bf16 v[44:47], a[220:223], v[116:119], v[44:47]\n" \
  "v_rcp_f32 v59, v59\n" \
  "v_mfma_f32_16x16x32_bf16 v[44:47], a[236:239], v[120:123], v[44:47]\n" \
  "v_mul_f32 v56, v56, v40\n" \
  "v_mul_f32 v57, v57, v41\n" \
  "v_mfma_f32_16x16x32_bf16 v[44:47], a[252:255], v[124:127], v[44:47]\n" \
  "v_mul_f32 v58, v58, v42\n" \
  "v_mul_f32 v59, v59, v43\n" \
  "s_nop 5\n" \
  "v_mul_f32 v56, v56, v44\n" \
  "v_mul_f32 v57, v57, v45\n" \
  "v_mul_f32 v58, v58, v46\n" \
  "v_mul_f32 v59, v59, v47\n" \
  "v_cvt_pk_bf16_f32 v50, v56, v57\n" \
  "v_cvt_pk_bf16_f32 v51, v58, v59\n" \
  "buffer_store_dwordx4 v[48:51], v12, s[36:39], s66 offen offset:0 sc1\n" \
  "s_cmp_ge_u32 s60, 64\n" \
  "s_cbranch_scc1 .Lff_last\n" \
  "s_waitcnt vmcnt(1) lgkmcnt(0)\n" \
  ".Lff_p:\n" \
  "s_barrier\n" \
  "ds_read_b128 v[64:67], v8 offset:0\n" \
  "ds_read_b32 v31, v28\n" \
  "ds_read_b128 v[68:71], v9 offset:0\n" \
  "ds_read_b128 v[72:75], v10 offset:0\n" \
  "ds_read_b128 v[76:79], v11 offset:0\n" \
  "s_waitcnt lgkmcnt(4)\n" \
  "v_mfma_f32_16x16x32_bf16 v[32:35], a[0:3], v[64:67], 0\n" \
  "ds_read_b128 v[80:83], v8 offset:256\n" \
  "v_mfma_f32_16x16x32_bf16 v[36:39], a[4:7], v[64:67], 0\n" \
  "s_waitcnt lgkmcnt(4)\n" \
  "v_readfirstlane_b32 s69, v31\n" \
  "s_and_b32 s30, s69, 1\n" \
  "s_bitset0_b32 s69, 0\n" \
  "s_waitcnt lgkmcnt(3)\n" \
  "v_mfma_f32_16x16x32_bf16 v[32:35], a[16:19], v[68:71], v[32:35]\n" \
  "ds_read_b128 v[84:87], v9 offset:256\n" \
  "s_add_u32 m0, s40, 0x8000\n" \
  "v_mfma_f32_16x16x32_bf16 v[36:39], a[20:23], v[68:71], v[36:39]\n" \
  "buffer_load_dwordx4 v0, s[36:39], s69 offen sc1 lds\n" \
  "s_waitcnt lgkmcnt(3)\n" \
  "v_mfma_f32_16x16x32_bf16 v[32:35], a[32:35], v[72:75], v[32:35]\n" \
  "ds_read_b128 v[88:91], v10 offset:256\n" \
  "v_mfma_f32_16x16x32_bf16 v[36:39], a[36:39], v[72:75], v[36:39]\n" \
  "s_waitcnt lgkmcnt(3)\n" \
  "v_mfma_f32_16x16x32_bf16 v[32:35], a[48:51], v[76:79], v[32:35]\n" \
  "ds_read_b128 v[92:95], v11 offset:256\n" \
  "s_add_u32 m0, s41, 0x8000\n" \
  "v_mfma_f32_16x16x32_bf16 v[36:39], a[52:55], v[76:79], v[36:39]\n" \
  "buffer_load_dwordx4 v1, s[36:39], s69 offen sc1 lds\n" \
  "s_waitcnt lgkmcnt(3)\n" \
  "v_mfma_f32_16x16x32_bf16 v[32:35], a[64:67], v[80:83], v[32:35]\n" \
  "ds_read_b128 v[96:99], v8 offset:512\n" \
  "v_mfma_f32_16x16x32_bf16 v[36:39], a[68:71], v[80:83], v[36:39]\n" \
  "s_waitcnt lgkmcnt(3)\n" \
  "v_mfma_f32_16x16x32_bf16 v[32:35], a[80:83], v[84:87], v[32:35]\n" \
  "ds_read_b128 v[100:103], v9 offset:512\n" \
  "s_add_u32 m0, s42, 0x8000\n" \
  "v_mfma_f32_16x16x32_bf16 v[36:39], a[84:87], v[84:87], v[36:39]\n" \
  "buffer_load_dwordx4 v2, s[36:39], s69 offen sc1 lds\n" \
  "s_waitcnt lgkmcnt(3)\n" \
  "v_mfma_f32_16x16x32_bf16 v[32:35], a[96:99], v[88:91], v[32:35]\n" \
  "ds_read_b128 v[104:107], v10 offset:512\n" \
  "v_mfma_f32_16x16x32_bf16 v[36:39], a[100:103], v[88:91], v[36:39]\n" \
  "s_waitcnt lgkmcnt(3)\n" \
  "v_mfma_f32_16x16x32_bf16 v[32:35], a[112:115], v[92:95], v[32:35]\n" \
  "ds_read_b128 v[108:111], v11 offset:512\n" \
  "s_add_u32 m0, s43, 0x8000\n" \
  "v_mfma_f32_16x16x32_bf16 v[36:39], a[116:119], v[92:95], v[36:39]\n" \
  "buffer_load_dwordx4 v3, s[36:39], s69 offen sc1 lds\n" \
  "s_waitcnt lgkmcnt(3)\n" \
  "v_mfma_f32_16x16x32_bf16 v[32:35], a[128:131], v[96:99], v[32:35]\n" \
  "ds_read_b128 v[112:115], v8 offset:768\n" \
  "v_mfma_f32_16x16x32_bf16 v[36:39], a[132:135], v[96:99], v[36:39]\n" \
  "s_waitcnt lgkmcnt(3)\n" \
  "v_mfma_f32_16x16x32_bf16 v[32:35], a[144:147], v[100:103], v[32:35]\n" \
  "ds_read_b128 v[116:119], v9 offset:768\n" \
  "s_add_u32 m0, s44, 0x8000\n" \
  "v_mfma_f32_16x16x32_bf16 v[36:39], a[148:151], v[100:103], v[36:39]\n" \
  "buffer_load_dwordx4 v4, s[36:39], s69 offen sc1 lds\n" \
  "s_waitcnt lgkmcnt(3)\n" \
  "v_mfma_f32_16x16x32_bf16 v[32:35], a[160:163], v[104:107], v[32:35]\n" \
  "ds_read_b128 v[120:123], v10 offset:768\n" \
  "v_mfma_f32_16x16x32_bf16 v[36:39], a[164:167], v[104:107], v[36:39]\n" \
  "s_waitcnt lgkmcnt(3)\n" \
  "v_mfma_f32_16x16x32_bf16 v[32:35], a[176:179], v[108:111], v[32:35]\n" \
  "ds_read_b128 v[124:127], v11 offset:768\n" \
  "s_add_u32 m0, s45, 0x8000\n" \
  "v_mfma_f32_16x16x32_bf16 v[36:39], a[180:183], v[108:111], v[36:39]\n" \
  "buffer_load_dwordx4 v5, s[36:39], s69 offen sc1 lds\n" \
  "s_waitcnt lgkmcnt(3)\n" \
  "v_mfma_f32_16x16x32_bf16 v[32:35], a[192:195], v[112:115], v[32:35]\n" \
  "v_mfma_f32_16x16x32_bf16 v[36:39], a[196:199], v[112:115], v[36:39]\n" \
  "s_waitcnt lgkmcnt(2)\n" \
  "v_mfma_f32_16x16x32_bf16 v[32:35], a[208:211], v[116:119], v[32:35]\n" \
  "s_add_u32 m0, s46, 0x8000\n" \
  "v_mfma_f32_16x16x32_bf16 v[36:39], a[212:215], v[116:119], v[36:39]\n" \
  "buffer_load_dwordx4 v6, s[36:39], s69 offen sc1 lds\n" \
  "s_waitcnt lgkmcnt(1)\n" \
  "v_mfma_f32_16x16x32_bf16 v[32:35], a[224:227], v[120:123], v[32:35]\n" \
  "v_mfma_f32_16x16x32_bf16 v[36:39], a[228:231], v[120:123], v[36:39]\n" \
  "s_waitcnt lgkmcnt(0)\n" \
  "v_mfma_f32_16x16x32_bf16 v[32:35], a[240:243], v[124:127], v[32:35]\n" \
  "s_add_u32 m0, s47, 0x8000\n" \
  "v_mfma_f32_16x16x32_bf16 v[36:39], a[244:247], v[124:127], v[36:39]\n" \
  "buffer_load_dwordx4 v7, s[36:39], s69 offen sc1 lds\n" \
  "v_mfma_f32_16x16x32_bf16 v[40:43], a[8:11], v[64:67], 0\n" \
  "v_mfma_f32_16x16x32_bf16 v[40:43], a[24:27], v[68:71], v[40:43]\n" \
  "v_mul_f32 v56, s73, v32\n" \
  "v_mul_f32 v57, s73, v33\n" \
  "v_mfma_f32_16x16x32_bf16 v[40:43], a[40:43], v[72:75], v[40:43]\n" \
  "v_mul_f32 v58, s73, v34\n" \
  "v_mul_f32 v59, s73, v35\n" \
  "v_mfma_f32_16x16x32_bf16 v[40:43], a[56:59], v[76:79], v[40:43]\n" \
  "v_exp_f32 v56, v56\n" \
  "v_mfma_f32_16x16x32_bf16 v[40:43], a[72:75], v[80:83], v[40:43]\n" \
  "v_exp_f32 v57, v57\n" \
  "v_mfma_f32_16x16x32_bf16 v[40:43], a[88:91], v[84:87], v[40:43]\n" \
  "v_exp_f32 v58, v58\n" \
  "v_mfma_f32_16x16x32_bf16 v[40:43], a[104:107], v[88:91], v[40:43]\n" \
  "v_exp_f32 v59, v59\n" \
  "v_mfma_f32_16x16x32_bf16 v[40:43], a[120:123], v[92:95], v[40:43]\n" \
  "v_add_f32 v56, 1.0, v56\n" \
  "v_add_f32 v57, 1.0, v57\n" \
  "v_mfma_f32_16x16x32_bf16 v[40:43], a[136:139], v[96:99], v[40:43]\n" \
  "v_add_f32 v58, 1.0, v58\n" \
  "v_add_f32 v59, 1.0, v59\n" \
  "v_mfma_f32_16x16x32_bf16 v[40:43], a[152:155], v[100:103], v[40:43]\n" \
  "v_rcp_f32 v56, v56\n" \
  "v_mfma_f32_16x16x32_bf16 v[40:43], a[168:171], v[104:107], v[40:43]\n" \
  "v_rcp_f32 v57, v57\n" \
  "v_mfma_f32_16x16x32_bf16 v[40:43], a[184:187], v[108:111], v[40:43]\n" \
  "v_rcp_f32 v58, v58\n" \
  "v_mfma_f32_16x16x32_bf16 v[40:43], a[200:203], v[112:115], v[40:43]\n" \
  "v_rcp_f32 v59, v59\n" \
  "v_mfma_f32_16x16x32_bf16 v[40:43], a[216:219], v[116:119], v[40:43]\n" \
  "v_mul_f32 v56, v56, v32\n" \
  "v_mul_f32 v57, v57, v33\n" \
  "v_mfma_f32_16x16x32_bf16 v[40:43], a[232:235], v[120:123], v[40:43]\n" \
  "v_mul_f32 v58, v58, v34\n" \
  "v_mul_f32 v59, v59, v35\n" \
  "v_mfma_f32_16x16x32_bf16 v[40:43], a[248:251], v[124:127], v[40:43]\n" \
  "v_mul_f32 v56, v56, v36\n" \
  "v_mul_f32 v57, v57, v37\n" \
  "v_mfma_f32_16x16x32_bf16 v[44:47], a[12:15], v[64:67], 0\n" \
  "v_mul_f32 v58, v58, v38\n" \
  "v_mul_f32 v59, v59, v39\n" \
  "v_mfma_f32_16x16x32_bf16 v[44:47], a[28:31], v[68:71], v[44:47]\n" \
  "v_cvt_pk_bf16_f32 v48, v56, v57\n" \
  "v_cvt_pk_bf16_f32 v49, v58, v59\n" \
  "v_mfma_f32_16x16x32_bf16 v[44:47], a[44:47], v[72:75], v[44:47]\n" \
  "v_mul_f32 v56, s73, v40\n" \
  "v_mul_f32 v57, s73, v41\n" \
  "v_mfma_f32_16x16x32_bf16 v[44:47], a[60:63], v[76:79], v[44:47]\n" \
  "v_mul_f32 v58, s73, v42\n" \
  "v_mul_f32 v59, s73, v43\n" \
  "v_mfma_f32_16x16x32_bf16 v[44:47], a[76:79], v[80:83], v[44:47]\n" \
  "v_exp_f32 v56, v56\n" \
  "v_mfma_f32_16x16x32_bf16 v[44:47], a[92:95], v[84:87], v[44:47]\n" \
  "v_exp_f32 v57, v57\n" \
  "v_mfma_f32_16x16x32_bf16 v[44:47], a[108:111], v[88:91], v[44:47]\n" \
  "v_exp_f32 v58, v58\n" \
  "v_mfma_f32_16x16x32_bf16 v[44:47], a[124:127], v[92:95], v[44:47]\n" \
  "v_exp_f32 v59, v59\n" \
  "v_mfma_f32_16x16x32_bf16 v[44:47], a[140:143], v[96:99], v[44:47]\n" \
  "v_add_f32 v56, 1.0, v56\n" \
  "v_add_f32 v57, 1.0, v57\n" \
  "v_mfma_f32_16x16x32_bf16 v[44:47], a[156:159], v[100:103], v[44:47]\n" \
  "v_add_f32 v58, 1.0, v58\n" \
  "v_add_f32 v59, 1.0, v59\n" \
  "v_mfma_f32_16x16x32_bf16 v[44:47], a[172:175], v[104:107], v[44:47]\n" \
  "v_rcp_f32 v56, v56\n" \
  "v_mfma_f32_16x16x32_bf16 v[44:47], a[188:191], v[108:111], v[44:47]\n" \
  "v_rcp_f32 v57, v57\n" \
  "v_mfma_f32_16x16x32_bf16 v[44:47], a[204:207], v[112:115], v[44:47]\n" \
  "v_rcp_f32 v58, v58\n" \
  "v_mfma_f32_16x16x32_bf16 v[44:47], a[220:223], v[116:119], v[44:47]\n" \
  "v_rcp_f32 v59, v59\n" \
  "v_mfma_f32_16x16x32_bf16 v[44:47], a[236:239], v[120:123], v[44:47]\n" \
  "v_mul_f32 v56, v56, v40\n" \
  "v_mul_f32 v57, v57, v41\n" \
  "v_mfma_f32_16x16x32_bf16 v[44:47], a[252:255], v[124:127], v[44:47]\n" \
  "v_mul_f32 v58, v58, v42\n" \
  "v_mul_f32 v59, v59, v43\n" \
  "s_nop 5\n" \
  "v_mul_f32 v56, v56, v44\n" \
  "v_mul_f32 v57, v57, v45\n" \
  "v_mul_f32 v58, v58, v46\n" \
  "v_mul_f32 v59, v59, v47\n" \
  "v_cvt_pk_bf16_f32 v50, v56, v57\n" \
  "v_cvt_pk_bf16_f32 v51, v58, v59\n" \
  "buffer_store_dwordx4 v[48:51], v12, s[36:39], s62 offen offset:0 sc1\n" \
  ".Lff_sel:\n" \
  "s_mov_b32 s76, s59\n" \
  "s_cmp_lg_u32 s69, 0x40000000\n" \
  "s_cselect_b32 s30, s30, 0\n" \
  "s_addc_u32 s59, s59, 0\n" \
  "s_cmp_lt_u32 s58, 64\n" \
  "s_cselect_b64 s[78:79], -1, 0\n" \
  "v_cndmask_b32_e64 v30, v14, v13, s[78:79]\n" \
  "buffer_load_dword v62, v30, s[36:39], 0 offen sc1\n" \
  "s_cselect_b32 s60, s58, s59\n" \
  "s_cselect_b32 s75, s48, s49\n" \
  "s_cselect_b32 s71, 0, 1\n" \
  "v_cndmask_b32_e64 v32, v17, v16, s[78:79]\n" \
  "v_cndmask_b32_e64 v33, v19, v18, s[78:79]\n" \
  "s_addc_u32 s58, s58, 0\n" \
  "s_cmp_lg_u32 s30, 0\n" \
  "s_cselect_b32 s60, 64, s60\n" \
  "s_cselect_b32 s71, 0, s71\n" \
  "s_add_u32 s59, s59, s71\n" \
  "s_mov_b32 s66, s65\n" \
  "v_readlane_b32 s61, v32, s60\n" \
  "v_readlane_b32 s62, v33, s60\n" \
  "v_readlane_b32 s64, v17, s59\n" \
  "v_readlane_b32 s65, v19, s59\n" \
  "s_mov_b32 s63, s59\n" \
  "s_add_u32 s31, s59, 1\n" \
  "s_cmp_lt_u32 s58, 64\n" \
  "s_cselect_b32 s31, -1, s31\n" \
  "s_cmp_lt_u32 s60, 64\n" \
  "s_cselect_b32 s77, s60, -1\n" \
  "s_cselect_b32 s61, s61, 0x40000000\n" \
  "s_cselect_b32 s75, s75, 0x40000000\n" \
  "s_cselect_b32 s71, s71, 0\n" \
  "s_cmp_eq_u32 s71, 0\n" \
  "s_cselect_b32 s72, s49, 0x40000000\n" \
  "s_waitcnt vmcnt(1)\n" \
  "s_barrier\n" \
  "buffer_store_dword v60, v29, s[36:39], s67 offen sc1\n" \
  "buffer_store_dword v61, v29, s[36:39], s68 offen sc1\n" \
  "s_waitcnt vmcnt(2)\n" \
  "v_mov_b32 v60, s60\n" \
  "v_mov_b32 v61, s76\n" \
  "s_mov_b32 s67, s75\n" \
  "s_mov_b32 s68, s72\n" \
  "s_cmp_lg_u32 s69, 0x40000000\n" \
  "s_cbranch_scc1 .Lff_q\n" \
  "s_mov_b32 s68, 0x40000000\n" \
  "s_cmp_ge_u32 s60, 64\n" \
  "s_cbranch_scc1 .Lff_end\n" \
  "s_mov_b32 m0, s40\n" \
  "s_branch .Lff_pn_c\n" \
  ".Lff_pn:\n" \
  ".Lff_pn_f:\n" \
  "buffer_load_dword v62, v30, s[36:39], 0 offen sc1\n" \
  "s_sub_u32 s70, s70, 1\n" \
  "s_cselect_b32 s77, -1, s77\n" \
  "s_cselect_b32 s70, 0, s70\n" \
  "s_waitcnt vmcnt(0)\n" \
  ".Lff_pn_c:\n" \
  "v_cmp_gt_i32 vcc, s77, v62\n" \
  "s_and_b32 vcc_lo, vcc_lo, s29\n" \
  "s_cbranch_scc1 .Lff_pn\n" \
  "s_barrier\n" \
  "buffer_load_dwordx4 v0, s[36:39], s61 offen sc1 lds\n" \
  "s_mov_b32 m0, s41\n" \
  "v_cmp_gt_i32 vcc, s63, v62\n" \
  "buffer_load_dwordx4 v1, s[36:39], s61 offen sc1 lds\n" \
  "s_mov_b32 m0, s42\n" \
  "s_cmp_eq_u32 vcc_hi, 0\n" \
  "buffer_load_dwordx4 v2, s[36:39], s61 offen sc1 lds\n" \
  "s_mov_b32 m0, s43\n" \
  "s_cselect_b32 s72, s64, 0x40000000\n" \
  "buffer_load_dwordx4 v3, s[36:39], s61 offen sc1 lds\n" \
  "s_mov_b32 m0, s44\n" \
  "v_cmp_gt_i32 vcc, s31, v62\n" \
  "s_cmp_lg_u32 vcc_hi, 0\n" \
  "s_addc_u32 s72, s72, 0\n" \
  "v_mov_b32 v31, s72\n" \
  "buffer_load_dwordx4 v4, s[36:39], s61 offen sc1 lds\n" \
  "s_mov_b32 m0, s45\n" \
  "ds_write_b32 v15, v31\n" \
  "buffer_load_dwordx4 v5, s[36:39], s61 offen sc1 lds\n" \
  "s_mov_b32 m0, s46\n" \
  "s_nop 0\n" \
  "buffer_load_dwordx4 v6, s[36:39], s61 offen sc1 lds\n" \
  "s_mov_b32 m0, s47\n" \
  "s_nop 0\n" \
  "buffer_load_dwordx4 v7, s[36:39], s61 offen sc1 lds\n" \
  "s_waitcnt vmcnt(0) lgkmcnt(0)\n" \
  "s_branch .Lff_p\n" \
  ".Lff_last:\n" \
  "s_mov_b32 s69, 0x40000000\n" \
  "s_branch .Lff_sel\n" \
  ".Lff_end:\n" \
  "s_endpgm\n"
#define ASM_DN \
  "s_nop 4\n" \
  "s_mov_b32 s58, 0\n" \
  "s_mov_b32 s59, s57\n" \
  "s_mov_b32 s70, 100000\n" \
  "s_mov_b32 s69, 0x40000000\n" \
  "s_mov_b32 s67, 0x40000000\n" \
  "s_mov_b32 s68, 0x40000000\n" \
  "v_mov_b32 v60, 0\n" \
  "v_mov_b32 v61, 0\n" \
  "v_cmp_eq_u32 vcc, v15, v28\n" \
  "s_mov_b32 s29, vcc_lo\n" \
  "s_mov_b32 s73, 0x05040100\n" \
  "s_mov_b32 s74, 0x07060302\n" \
  "s_mov_b32 s71, 0\n" \
  "s_mov_b32 s72, 16384\n" \
  "buffer_load_dwordx2 v[64:65], v20, s[52:55], s71 offen offset:0\n" \
  "buffer_load_dwordx2 v[66:67], v20, s[52:55], s71 offen offset:1024\n" \
  "buffer_load_dwordx2 v[68:69], v20, s[52:55], s71 offen offset:2048\n" \
  "buffer_load_dwordx2 v[70:71], v20, s[52:55], s71 offen offset:3072\n" \
  "buffer_load_dwordx2 v[72:73], v20, s[52:55], s72 offen offset:0\n" \
  "buffer_load_dwordx2 v[74:75], v20, s[52:55], s72 offen offset:1024\n" \
  "buffer_load_dwordx2 v[76:77], v20, s[52:55], s72 offen offset:2048\n" \
  "buffer_load_dwordx2 v[78:79], v20, s[52:55], s72 offen offset:3072\n" \
  "s_mov_b32 s71, 32768\n" \
  "s_mov_b32 s72, 49152\n" \
  "buffer_load_dwordx2 v[80:81], v20, s[52:55], s71 offen offset:0\n" \
  "buffer_load_dwordx2 v[82:83], v20, s[52:55], s71 offen offset:1024\n" \
  "buffer_load_dwordx2 v[84:85], v20, s[52:55], s71 offen offset:2048\n" \
  "buffer_load_dwordx2 v[86:87], v20, s[52:55], s71 offen offset:3072\n" \
  "buffer_load_dwordx2 v[88:89], v20, s[52:55], s72 offen offset:0\n" \
  "buffer_load_dwordx2 v[90:91], v20, s[52:55], s72 offen offset:1024\n" \
  "buffer_load_dwordx2 v[92:93], v20, s[52:55], s72 offen offset:2048\n" \
  "buffer_load_dwordx2 v[94:95], v20, s[52:55], s72 offen offset:3072\n" \
  "s_mov_b32 s71, 65536\n" \
  "s_mov_b32 s72, 81920\n" \
  "buffer_load_dwordx2 v[96:97], v20, s[52:55], s71 offen offset:0\n" \
  "buffer_load_dwordx2 v[98:99], v20, s[52:55], s71 offen offset:1024\n" \
  "buffer_load_dwordx2 v[100:101], v20, s[52:55], s71 offen offset:2048\n" \
  "buffer_load_dwordx2 v[102:103], v20, s[52:55], s71 offen offset:3072\n" \
  "buffer_load_dwordx2 v[104:105], v20, s[52:55], s72 offen offset:0\n" \
  "buffer_load_dwordx2 v[106:107], v20, s[52:55], s72 offen offset:1024\n" \
  "buffer_load_dwordx2 v[108:109], v20, s[52:55], s72 offen offset:2048\n" \
  "buffer_load_dwordx2 v[110:111], v20, s[52:55], s72 offen offset:3072\n" \
  "s_mov_b32 s71, 98304\n" \
  "s_mov_b32 s72, 114688\n" \
  "buffer_load_dwordx2 v[112:113], v20, s[52:55], s71 offen offset:0\n" \
  "buffer_load_dwordx2 v[114:115], v20, s[52:55], s71 offen offset:1024\n" \
  "buffer_load_dwordx2 v[116:117], v20, s[52:55], s71 offen offset:2048\n" \
  "buffer_load_dwordx2 v[118:119], v20, s[52:55], s71 offen offset:3072\n" \
  "buffer_load_dwordx2 v[120:121], v20, s[52:55], s72 offen offset:0\n" \
  "buffer_load_dwordx2 v[122:123], v20, s[52:55], s72 offen offset:1024\n" \
  "buffer_load_dwordx2 v[124:125], v20, s[52:55], s72 offen offset:2048\n" \
  "buffer_load_dwordx2 v[126:127], v20, s[52:55], s72 offen offset:3072\n" \
  "s_mov_b32 s71, 131072\n" \
  "s_mov_b32 s72, 147456\n" \
  "buffer_load_dwordx2 v[128:129], v20, s[52:55], s71 offen offset:0\n" \
  "buffer_load_dwordx2 v[130:131], v20, s[52:55], s71 offen offset:1024\n" \
  "buffer_load_dwordx2 v[132:133], v20, s[52:55], s71 offen offset:2048\n" \
  "buffer_load_dwordx2 v[134:135], v20, s[52:55], s71 offen offset:3072\n" \
  "buffer_load_dwordx2 v[136:137], v20, s[52:55], s72 offen offset:0\n" \
  "buffer_load_dwordx2 v[138:139], v20, s[52:55], s72 offen offset:1024\n" \
  "buffer_load_dwordx2 v[140:141], v20, s[52:55], s72 offen offset:2048\n" \
  "buffer_load_dwordx2 v[142:143], v20, s[52:55], s72 offen offset:3072\n" \
  "s_mov_b32 s71, 163840\n" \
  "s_mov_b32 s72, 180224\n" \
  "buffer_load_dwordx2 v[144:145], v20, s[52:55], s71 offen offset:0\n" \
  "buffer_load_dwordx2 v[146:147], v20, s[52:55], s71 offen offset:1024\n" \
  "buffer_load_dwordx2 v[148:149], v20, s[52:55], s71 offen offset:2048\n" \
  "buffer_load_dwordx2 v[150:151], v20, s[52:55], s71 offen offset:3072\n" \
  "buffer_load_dwordx2 v[152:153], v20, s[52:55], s72 offen offset:0\n" \
  "buffer_load_dwordx2 v[154:155], v20, s[52:55], s72 offen offset:1024\n" \
  "buffer_load_dwordx2 v[156:157], v20, s[52:55], s72 offen offset:2048\n" \
  "buffer_load_dwordx2 v[158:159], v20, s[52:55], s72 offen offset:3072\n" \
  "s_mov_b32 s71, 196608\n" \
  "s_mov_b32 s72, 212992\n" \
  "buffer_load_dwordx2 v[160:161], v20, s[52:55], s71 offen offset:0\n" \
  "buffer_load_dwordx2 v[162:163], v20, s[52:55], s71 offen offset:1024\n" \
  "buffer_load_dwordx2 v[164:165], v20, s[52:55], s71 offen offset:2048\n" \
  "buffer_load_dwordx2 v[166:167], v20, s[52:55], s71 offen offset:3072\n" \
  "buffer_load_dwordx2 v[168:169], v20, s[52:55], s72 offen offset:0\n" \
  "buffer_load_dwordx2 v[170:171], v20, s[52:55], s72 offen offset:1024\n" \
  "buffer_load_dwordx2 v[172:173], v20, s[52:55], s72 offen offset:2048\n" \
  "buffer_load_dwordx2 v[174:175], v20, s[52:55], s72 offen offset:3072\n" \
  "s_waitcnt vmcnt(54)\n" \
  "v_perm_b32 v32, v66, v64, s73\n" \
  "s_waitcnt vmcnt(52)\n" \
  "v_perm_b32 v33, v70, v68, s73\n" \
  "s_waitcnt vmcnt(50)\n" \
  "v_perm_b32 v34, v74, v72, s73\n" \
  "s_waitcnt vmcnt(48)\n" \
  "v_perm_b32 v35, v78, v76, s73\n" \
  "v_perm_b32 v36, v66, v64, s74\n" \
  "v_perm_b32 v37, v70, v68, s74\n" \
  "v_perm_b32 v38, v74, v72, s74\n" \
  "v_perm_b32 v39, v78, v76, s74\n" \
  "v_perm_b32 v40, v67, v65, s73\n" \
  "v_perm_b32 v41, v71, v69, s73\n" \
  "v_perm_b32 v42, v75, v73, s73\n" \
  "v_perm_b32 v43, v79, v77, s73\n" \
  "v_perm_b32 v44, v67, v65, s74\n" \
  "v_perm_b32 v45, v71, v69, s74\n" \
  "v_perm_b32 v46, v75, v73, s74\n" \
  "v_perm_b32 v47, v79, v77, s74\n" \
  "v_accvgpr_write_b32 a0, v32\n" \
  "v_accvgpr_write_b32 a1, v33\n" \
  "v_accvgpr_write_b32 a2, v34\n" \
  "v_accvgpr_write_b32 a3, v35\n" \
  "v_accvgpr_write_b32 a4, v36\n" \
  "v_accvgpr_write_b32 a5, v37\n" \
  "v_accvgpr_write_b32 a6, v38\n" \
  "v_accvgpr_write_b32 a7, v39\n" \
  "v_accvgpr_write_b32 a8, v40\n" \
  "v_accvgpr_write_b32 a9, v41\n" \
  "v_accvgpr_write_b32 a10, v42\n" \
  "v_accvgpr_write_b32 a11, v43\n" \
  "v_accvgpr_write_b32 a12, v44\n" \
  "v_accvgpr_write_b32 a13, v45\n" \
  "v_accvgpr_write_b32 a14, v46\n" \
  "v_accvgpr_write_b32 a15, v47\n" \
  "s_mov_b32 s71, 229376\n" \
  "s_mov_b32 s72, 245760\n" \
  "buffer_load_dwordx2 v[64:65], v20, s[52:55], s71 offen offset:0\n" \
  "buffer_load_dwordx2 v[66:67], v20, s[52:55], s71 offen offset:1024\n" \
  "buffer_load_dwordx2 v[68:69], v20, s[52:55], s71 offen offset:2048\n" \
  "buffer_load_dwordx2 v[70:71], v20, s[52:55], s71 offen offset:3072\n" \
  "buffer_load_dwordx2 v[72:73], v20, s[52:55], s72 offen offset:0\n" \
  "buffer_load_dwordx2 v[74:75], v20, s[52:55], s72 offen offset:1024\n" \
  "buffer_load_dwordx2 v[76:77], v20, s[52:55], s72 offen offset:2048\n" \
  "buffer_load_dwordx2 v[78:79], v20, s[52:55], s72 offen offset:3072\n" \
  "s_waitcnt vmcnt(54)\n" \
  "v_perm_b32 v32, v82, v80, s73\n" \
  "s_waitcnt vmcnt(52)\n" \
  "v_perm_b32 v33, v86, v84, s73\n" \
  "s_waitcnt vmcnt(50)\n" \
  "v_perm_b32 v34, v90, v88, s73\n" \
  "s_waitcnt vmcnt(48)\n" \
  "v_perm_b32 v35, v94, v92, s73\n" \
  "v_perm_b32 v36, v82, v80, s74\n" \
  "v_perm_b32 v37, v86, v84, s74\n" \
  "v_perm_b32 v38, v90, v88, s74\n" \
  "v_perm_b32 v39, v94, v92, s74\n" \
  "v_perm_b32 v40, v83, v81, s73\n" \
  "v_perm_b32 v41, v87, v85, s73\n" \
  "v_perm_b32 v42, v91, v89, s73\n" \
  "v_perm_b32 v43, v95, v93, s73\n" \
  "v_perm_b32 v44, v83, v81, s74\n" \
  "v_perm_b32 v45, v87, v85, s74\n" \
  "v_perm_b32 v46, v91, v89, s74\n" \
  "v_perm_b32 v47, v95, v93, s74\n" \
  "v_accvgpr_write_b32 a16, v32\n" \
  "v_accvgpr_write_b32 a17, v33\n" \
  "v_accvgpr_write_b32 a18, v34\n" \
  "v_accvgpr_write_b32 a19, v35\n" \
  "v_accvgpr_write_b32 a20, v36\n" \
  "v_accvgpr_write_b32 a21, v37\n" \
  "v_accvgpr_write_b32 a22, v38\n" \
  "v_accvgpr_write_b32 a23, v39\n" \
  "v_accvgpr_write_b32 a24, v40\n" \
  "v_accvgpr_write_b32 a25, v41\n" \
  "v_accvgpr_write_b32 a26, v42\n" \
  "v_accvgpr_write_b32 a27, v43\n" \
  "v_accvgpr_write_b32 a28, v44\n" \
  "v_accvgpr_write_b32 a29, v45\n" \
  "v_accvgpr_write_b32 a30, v46\n" \
  "v_accvgpr_write_b32 a31, v47\n" \
  "s_mov_b32 s71, 262144\n" \
  "s_mov_b32 s72, 278528\n" \
  "buffer_load_dwordx2 v[80:81], v20, s[52:55], s71 offen offset:0\n" \
  "buffer_load_dwordx2 v[82:83], v20, s[52:55], s71 offen offset:1024\n" \
  "buffer_load_dwordx2 v[84:85], v20, s[52:55], s71 offen offset:2048\n" \
  "buffer_load_dwordx2 v[86:87], v20, s[52:55], s71 offen offset:3072\n" \
  "buffer_load_dwordx2 v[88:89], v20, s[52:55], s72 offen offset:0\n" \
  "buffer_load_dwordx2 v[90:91], v20, s[52:55], s72 offen offset:1024\n" \
  "buffer_load_dwordx2 v[92:93], v20, s[52:55], s72 offen offset:2048\n" \
  "buffer_load_dwordx2 v[94:95], v20, s[52:55], s72 offen offset:3072\n" \
  "s_waitcnt vmcnt(54)\n" \
  "v_perm_b32 v32, v98, v96, s73\n" \
  "s_waitcnt vmcnt(52)\n" \
  "v_perm_b32 v33, v102, v100, s73\n" \
  "s_waitcnt vmcnt(50)\n" \
  "v_perm_b32 v34, v106, v104, s73\n" \
  "s_waitcnt vmcnt(48)\n" \
  "v_perm_b32 v35, v110, v108, s73\n" \
  "v_perm_b32 v36, v98, v96, s74\n" \
  "v_perm_b32 v37, v102, v100, s74\n" \
  "v_perm_b32 v38, v106, v104, s74\n" \
  "v_perm_b32 v39, v110, v108, s74\n" \
  "v_perm_b32 v40, v99, v97, s73\n" \
  "v_perm_b32 v41, v103, v101, s73\n" \
  "v_perm_b32 v42, v107, v105, s73\n" \
  "v_perm_b32 v43, v111, v109, s73\n" \
  "v_perm_b32 v44, v99, v97, s74\n" \
  "v_perm_b32 v45, v103, v101, s74\n" \
  "v_perm_b32 v46, v107, v105, s74\n" \
  "v_perm_b32 v47, v111, v109, s74\n" \
  "v_accvgpr_write_b32 a32, v32\n" \
  "v_accvgpr_write_b32 a33, v33\n" \
  "v_accvgpr_write_b32 a34, v34\n" \
  "v_accvgpr_write_b32 a35, v35\n" \
  "v_accvgpr_write_b32 a36, v36\n" \
  "v_accvgpr_write_b32 a37, v37\n" \
  "v_accvgpr_write_b32 a38, v38\n" \
  "v_accvgpr_write_b32 a39, v39\n" \
  "v_accvgpr_write_b32 a40, v40\n" \
  "v_accvgpr_write_b32 a41, v41\n" \
  "v_accvgpr_write_b32 a42, v42\n" \
  "v_accvgpr_write_b32 a43, v43\n" \
  "v_accvgpr_write_b32 a44, v44\n" \
  "v_accvgpr_write_b32 a45, v45\n" \
  "v_accvgpr_write_b32 a46, v46\n" \
  "v_accvgpr_write_b32 a47, v47\n" \
  "s_mov_b32 s71, 294912\n" \
  "s_mov_b32 s72, 311296\n" \
  "buffer_load_dwordx2 v[96:97], v20, s[52:55], s71 offen offset:0\n" \
  "buffer_load_dwordx2 v[98:99], v20, s[52:55], s71 offen offset:1024\n" \
  "buffer_load_dwordx2 v[100:101], v20, s[52:55], s71 offen offset:2048\n" \
  "buffer_load_dwordx2 v[102:103], v20, s[52:55], s71 offen offset:3072\n" \
  "buffer_load_dwordx2 v[104:105], v20, s[52:55], s72 offen offset:0\n" \
  "buffer_load_dwordx2 v[106:107], v20, s[52:55], s72 offen offset:1024\n" \
  "buffer_load_dwordx2 v[108:109], v20, s[52:55], s72 offen offset:2048\n" \
  "buffer_load_dwordx2 v[110:111], v20, s[52:55], s72 offen offset:3072\n" \
  "s_waitcnt vmcnt(54)\n" \
  "v_perm_b32 v32, v114, v112, s73\n" \
  "s_waitcnt vmcnt(52)\n" \
  "v_perm_b32 v33, v118, v116, s73\n" \
  "s_waitcnt vmcnt(50)\n" \
  "v_perm_b32 v34, v122, v120, s73\n" \
  "s_waitcnt vmcnt(48)\n" \
  "v_perm_b32 v35, v126, v124, s73\n" \
  "v_perm_b32 v36, v114, v112, s74\n" \
  "v_perm_b32 v37, v118, v116, s74\n" \
  "v_perm_b32 v38, v122, v120, s74\n" \
  "v_perm_b32 v39, v126, v124, s74\n" \
  "v_perm_b32 v40, v115, v113, s73\n" \
  "v_perm_b32 v41, v119, v117, s73\n" \
  "v_perm_b32 v42, v123, v121, s73\n" \
  "v_perm_b32 v43, v127, v125, s73\n" \
  "v_perm_b32 v44, v115, v113, s74\n" \
  "v_perm_b32 v45, v119, v117, s74\n" \
  "v_perm_b32 v46, v123, v121, s74\n" \
  "v_perm_b32 v47, v127, v125, s74\n" \
  "v_accvgpr_write_b32 a48, v32\n" \
  "v_accvgpr_write_b32 a49, v33\n" \
  "v_accvgpr_write_b32 a50, v34\n" \
  "v_accvgpr_write_b32 a51, v35\n" \
  "v_accvgpr_write_b32 a52, v36\n" \
  "v_accvgpr_write_b32 a53, v37\n" \
  "v_accvgpr_write_b32 a54, v38\n" \
  "v_accvgpr_write_b32 a55, v39\n" \
  "v_accvgpr_write_b32 a56, v40\n" \
  "v_accvgpr_write_b32 a57, v41\n" \
  "v_accvgpr_write_b32 a58, v42\n" \
  "v_accvgpr_write_b32 a59, v43\n" \
  "v_accvgpr_write_b32 a60, v44\n" \
  "v_accvgpr_write_b32 a61, v45\n" \
  "v_accvgpr_write_b32 a62, v46\n" \
  "v_accvgpr_write_b32 a63, v47\n" \
  "s_mov_b32 s71, 327680\n" \
  "s_mov_b32 s72, 344064\n" \
  "buffer_load_dwordx2 v[112:113], v20, s[52:55], s71 offen offset:0\n" \
  "buffer_load_dwordx2 v[114:115], v20, s[52:55], s71 offen offset:1024\n" \
  "buffer_load_dwordx2 v[116:117], v20, s[52:55], s71 offen offset:2048\n" \
  "buffer_load_dwordx2 v[118:119], v20, s[52:55], s71 offen offset:3072\n" \
  "buffer_load_dwordx2 v[120:121], v20, s[52:55], s72 offen offset:0\n" \
  "buffer_load_dwordx2 v[122:123], v20, s[52:55], s72 offen offset:1024\n" \
  "buffer_load_dwordx2 v[124:125], v20, s[52:55], s72 offen offset:2048\n" \
  "buffer_load_dwordx2 v[126:127], v20, s[52:55], s72 offen offset:3072\n" \
  "s_waitcnt vmcnt(54)\n" \
  "v_perm_b32 v32, v130, v128, s73\n" \
  "s_waitcnt vmcnt(52)\n" \
  "v_perm_b32 v33, v134, v132, s73\n" \
  "s_waitcnt vmcnt(50)\n" \
  "v_perm_b32 v34, v138, v136, s73\n" \
  "s_waitcnt vmcnt(48)\n" \
  "v_perm_b32 v35, v142, v140, s73\n" \
  "v_perm_b32 v36, v130, v128, s74\n" \
  "v_perm_b32 v37, v134, v132, s74\n" \
  "v_perm_b32 v38, v138, v136, s74\n" \
  "v_perm_b32 v39, v142, v140, s74\n" \
  "v_perm_b32 v40, v131, v129, s73\n" \
  "v_perm_b32 v41, v135, v133, s73\n" \
  "v_perm_b32 v42, v139, v137, s73\n" \
  "v_perm_b32 v43, v143, v141, s73\n" \
  "v_perm_b32 v44, v131, v129, s74\n" \
  "v_perm_b32 v45, v135, v133, s74\n" \
  "v_perm_b32 v46, v139, v137, s74\n" \
  "v_perm_b32 v47, v143, v141, s74\n" \
  "v_accvgpr_write_b32 a64, v32\n" \
  "v_accvgpr_write_b32 a65, v33\n" \
  "v_accvgpr_write_b32 a66, v34\n" \
  "v_accvgpr_write_b32 a67, v35\n" \
  "v_accvgpr_write_b32 a68, v36\n" \
  "v_accvgpr_write_b32 a69, v37\n" \
  "v_accvgpr_write_b32 a70, v38\n" \
  "v_accvgpr_write_b32 a71, v39\n" \
  "v_accvgpr_write_b32 a72, v40\n" \
  "v_accvgpr_write_b32 a73, v41\n" \
  "v_accvgpr_write_b32 a74, v42\n" \
  "v_accvgpr_write_b32 a75, v43\n" \
  "v_accvgpr_write_b32 a76, v44\n" \
  "v_accvgpr_write_b32 a77, v45\n" \
  "v_accvgpr_write_b32 a78, v46\n" \
  "v_accvgpr_write_b32 a79, v47\n" \
  "s_waitcnt vmcnt(46)\n" \
  "v_perm_b32 v32, v146, v144, s73\n" \
  "s_waitcnt vmcnt(44)\n" \
  "v_perm_b32 v33, v150, v148, s73\n" \
  "s_waitcnt vmcnt(42)\n" \
  "v_perm_b32 v34, v154, v152, s73\n" \
  "s_waitcnt vmcnt(40)\n" \
  "v_perm_b32 v35, v158, v156, s73\n" \
  "v_perm_b32 v36, v146, v144, s74\n" \
  "v_perm_b32 v37, v150, v148, s74\n" \
  "v_perm_b32 v38, v154, v152, s74\n" \
  "v_perm_b32 v39, v158, v156, s74\n" \
  "v_perm_b32 v40, v147, v145, s73\n" \
  "v_perm_b32 v41, v151, v149, s73\n" \
  "v_perm_b32 v42, v155, v153, s73\n" \
  "v_perm_b32 v43, v159, v157, s73\n" \
  "v_perm_b32 v44, v147, v145, s74\n" \
  "v_perm_b32 v45, v151, v149, s74\n" \
  "v_perm_b32 v46, v155, v153, s74\n" \
  "v_perm_b32 v47, v159, v157, s74\n" \
  "v_accvgpr_write_b32 a80, v32\n" \
  "v_accvgpr_write_b32 a81, v33\n" \
  "v_accvgpr_write_b32 a82, v34\n" \
  "v_accvgpr_write_b32 a83, v35\n" \
  "v_accvgpr_write_b32 a84, v36\n" \
  "v_accvgpr_write_b32 a85, v37\n" \
  "v_accvgpr_write_b32 a86, v38\n" \
  "v_accvgpr_write_b32 a87, v39\n" \
  "v_accvgpr_write_b32 a88, v40\n" \
  "v_accvgpr_write_b32 a89, v41\n" \
  "v_accvgpr_write_b32 a90, v42\n" \
  "v_accvgpr_write_b32 a91, v43\n" \
  "v_accvgpr_write_b32 a92, v44\n" \
  "v_accvgpr_write_b32 a93, v45\n" \
  "v_accvgpr_write_b32 a94, v46\n" \
  "v_accvgpr_write_b32 a95, v47\n" \
  "s_waitcnt vmcnt(38)\n" \
  "v_perm_b32 v32, v162, v160, s73\n" \
  "s_waitcnt vmcnt(36)\n" \
  "v_perm_b32 v33, v166, v164, s73\n" \
  "s_waitcnt vmcnt(34)\n" \
  "v_perm_b32 v34, v170, v168, s73\n" \
  "s_waitcnt vmcnt(32)\n" \
  "v_perm_b32 v35, v174, v172, s73\n" \
  "v_perm_b32 v36, v162, v160, s74\n" \
  "v_perm_b32 v37, v166, v164, s74\n" \
  "v_perm_b32 v38, v170, v168, s74\n" \
  "v_perm_b32 v39, v174, v172, s74\n" \
  "v_perm_b32 v40, v163, v161, s73\n" \
  "v_perm_b32 v41, v167, v165, s73\n" \
  "v_perm_b32 v42, v171, v169, s73\n" \
  "v_perm_b32 v43, v175, v173, s73\n" \
  "v_perm_b32 v44, v163, v161, s74\n" \
  "v_perm_b32 v45, v167, v165, s74\n" \
  "v_perm_b32 v46, v171, v169, s74\n" \
  "v_perm_b32 v47, v175, v173, s74\n" \
  "v_accvgpr_write_b32 a96, v32\n" \
  "v_accvgpr_write_b32 a97, v33\n" \
  "v_accvgpr_write_b32 a98, v34\n" \
  "v_accvgpr_write_b32 a99, v35\n" \
  "v_accvgpr_write_b32 a100, v36\n" \
  "v_accvgpr_write_b32 a101, v37\n" \
  "v_accvgpr_write_b32 a102, v38\n" \
  "v_accvgpr_write_b32 a103, v39\n" \
  "v_accvgpr_write_b32 a104, v40\n" \
  "v_accvgpr_write_b32 a105, v41\n" \
  "v_accvgpr_write_b32 a106, v42\n" \
  "v_accvgpr_write_b32 a107, v43\n" \
  "v_accvgpr_write_b32 a108, v44\n" \
  "v_accvgpr_write_b32 a109, v45\n" \
  "v_accvgpr_write_b32 a110, v46\n" \
  "v_accvgpr_write_b32 a111, v47\n" \
  "s_waitcnt vmcnt(30)\n" \
  "v_perm_b32 v32, v66, v64, s73\n" \
  "s_waitcnt vmcnt(28)\n" \
  "v_perm_b32 v33, v70, v68, s73\n" \
  "s_waitcnt vmcnt(26)\n" \
  "v_perm_b32 v34, v74, v72, s73\n" \
  "s_waitcnt vmcnt(24)\n" \
  "v_perm_b32 v35, v78, v76, s73\n" \
  "v_perm_b32 v36, v66, v64, s74\n" \
  "v_perm_b32 v37, v70, v68, s74\n" \
  "v_perm_b32 v38, v74, v72, s74\n" \
  "v_perm_b32 v39, v78, v76, s74\n" \
  "v_perm_b32 v40, v67, v65, s73\n" \
  "v_perm_b32 v41, v71, v69, s73\n" \
  "v_perm_b32 v42, v75, v73, s73\n" \
  "v_perm_b32 v43, v79, v77, s73\n" \
  "v_perm_b32 v44, v67, v65, s74\n" \
  "v_perm_b32 v45, v71, v69, s74\n" \
  "v_perm_b32 v46, v75, v73, s74\n" \
  "v_perm_b32 v47, v79, v77, s74\n" \
  "v_accvgpr_write_b32 a112, v32\n" \
  "v_accvgpr_write_b32 a113, v33\n" \
  "v_accvgpr_write_b32 a114, v34\n" \
  "v_accvgpr_write_b32 a115, v35\n" \
  "v_accvgpr_write_b32 a116, v36\n" \
  "v_accvgpr_write_b32 a117, v37\n" \
  "v_accvgpr_write_b32 a118, v38\n" \
  "v_accvgpr_write_b32 a119, v39\n" \
  "v_accvgpr_write_b32 a120, v40\n" \
  "v_accvgpr_write_b32 a121, v41\n" \
  "v_accvgpr_write_b32 a122, v42\n" \
  "v_accvgpr_write_b32 a123, v43\n" \
  "v_accvgpr_write_b32 a124, v44\n" \
  "v_accvgpr_write_b32 a125, v45\n" \
  "v_accvgpr_write_b32 a126, v46\n" \
  "v_accvgpr_write_b32 a127, v47\n" \
  "s_waitcnt vmcnt(22)\n" \
  "v_perm_b32 v32, v82, v80, s73\n" \
  "s_waitcnt vmcnt(20)\n" \
  "v_perm_b32 v33, v86, v84, s73\n" \
  "s_waitcnt vmcnt(18)\n" \
  "v_perm_b32 v34, v90, v88, s73\n" \
  "s_waitcnt vmcnt(16)\n" \
  "v_perm_b32 v35, v94, v92, s73\n" \
  "v_perm_b32 v36, v82, v80, s74\n" \
  "v_perm_b32 v37, v86, v84, s74\n" \
  "v_perm_b32 v38, v90, v88, s74\n" \
  "v_perm_b32 v39, v94, v92, s74\n" \
  "v_perm_b32 v40, v83, v81, s73\n" \
  "v_perm_b32 v41, v87, v85, s73\n" \
  "v_perm_b32 v42, v91, v89, s73\n" \
  "v_perm_b32 v43, v95, v93, s73\n" \
  "v_perm_b32 v44, v83, v81, s74\n" \
  "v_perm_b32 v45, v87, v85, s74\n" \
  "v_perm_b32 v46, v91, v89, s74\n" \
  "v_perm_b32 v47, v95, v93, s74\n" \
  "v_accvgpr_write_b32 a128, v32\n" \
  "v_accvgpr_write_b32 a129, v33\n" \
  "v_accvgpr_write_b32 a130, v34\n" \
  "v_accvgpr_write_b32 a131, v35\n" \
  "v_accvgpr_write_b32 a132, v36\n" \
  "v_accvgpr_write_b32 a133, v37\n" \
  "v_accvgpr_write_b32 a134, v38\n" \
  "v_accvgpr_write_b32 a135, v39\n" \
  "v_accvgpr_write_b32 a136, v40\n" \
  "v_accvgpr_write_b32 a137, v41\n" \
  "v_accvgpr_write_b32 a138, v42\n" \
  "v_accvgpr_write_b32 a139, v43\n" \
  "v_accvgpr_write_b32 a140, v44\n" \
  "v_accvgpr_write_b32 a141, v45\n" \
  "v_accvgpr_write_b32 a142, v46\n" \
  "v_accvgpr_write_b32 a143, v47\n" \
  "s_waitcnt vmcnt(14)\n" \
  "v_perm_b32 v32, v98, v96, s73\n" \
  "s_waitcnt vmcnt(12)\n" \
  "v_perm_b32 v33, v102, v100, s73\n" \
  "s_waitcnt vmcnt(10)\n" \
  "v_perm_b32 v34, v106, v104, s73\n" \
  "s_waitcnt vmcnt(8)\n" \
  "v_perm_b32 v35, v110, v108, s73\n" \
  "v_perm_b32 v36, v98, v96, s74\n" \
  "v_perm_b32 v37, v102, v100, s74\n" \
  "v_perm_b32 v38, v106, v104, s74\n" \
  "v_perm_b32 v39, v110, v108, s74\n" \
  "v_perm_b32 v40, v99, v97, s73\n" \
  "v_perm_b32 v41, v103, v101, s73\n" \
  "v_perm_b32 v42, v107, v105, s73\n" \
  "v_perm_b32 v43, v111, v109, s73\n" \
  "v_perm_b32 v44, v99, v97, s74\n" \
  "v_perm_b32 v45, v103, v101, s74\n" \
  "v_perm_b32 v46, v107, v105, s74\n" \
  "v_perm_b32 v47, v111, v109, s74\n" \
  "v_accvgpr_write_b32 a144, v32\n" \
  "v_accvgpr_write_b32 a145, v33\n" \
  "v_accvgpr_write_b32 a146, v34\n" \
  "v_accvgpr_write_b32 a147, v35\n" \
  "v_accvgpr_write_b32 a148, v36\n" \
  "v_accvgpr_write_b32 a149, v37\n" \
  "v_accvgpr_write_b32 a150, v38\n" \
  "v_accvgpr_write_b32 a151, v39\n" \
  "v_accvgpr_write_b32 a152, v40\n" \
  "v_accvgpr_write_b32 a153, v41\n" \
  "v_accvgpr_write_b32 a154, v42\n" \
  "v_accvgpr_write_b32 a155, v43\n" \
  "v_accvgpr_write_b32 a156, v44\n" \
  "v_accvgpr_write_b32 a157, v45\n" \
  "v_accvgpr_write_b32 a158, v46\n" \
  "v_accvgpr_write_b32 a159, v47\n" \
  "s_waitcnt vmcnt(6)\n" \
  "v_perm_b32 v32, v114, v112, s73\n" \
  "s_waitcnt vmcnt(4)\n" \
  "v_perm_b32 v33, v118, v116, s73\n" \
  "s_waitcnt vmcnt(2)\n" \
  "v_perm_b32 v34, v122, v120, s73\n" \
  "s_waitcnt vmcnt(0)\n" \
  "v_perm_b32 v35, v126, v124, s73\n" \
  "v_perm_b32 v36, v114, v112, s74\n" \
  "v_perm_b32 v37, v118, v116, s74\n" \
  "v_perm_b32 v38, v122, v120, s74\n" \
  "v_perm_b32 v39, v126, v124, s74\n" \
  "v_perm_b32 v40, v115, v113, s73\n" \
  "v_perm_b32 v41, v119, v117, s73\n" \
  "v_perm_b32 v42, v123, v121, s73\n" \
  "v_perm_b32 v43, v127, v125, s73\n" \
  "v_perm_b32 v44, v115, v113, s74\n" \
  "v_perm_b32 v45, v119, v117, s74\n" \
  "v_perm_b32 v46, v123, v121, s74\n" \
  "v_perm_b32 v47, v127, v125, s74\n" \
  "v_accvgpr_write_b32 a160, v32\n" \
  "v_accvgpr_write_b32 a161, v33\n" \
  "v_accvgpr_write_b32 a162, v34\n" \
  "v_accvgpr_write_b32 a163, v35\n" \
  "v_accvgpr_write_b32 a164, v36\n" \
  "v_accvgpr_write_b32 a165, v37\n" \
  "v_accvgpr_write_b32 a166, v38\n" \
  "v_accvgpr_write_b32 a167, v39\n" \
  "v_accvgpr_write_b32 a168, v40\n" \
  "v_accvgpr_write_b32 a169, v41\n" \
  "v_accvgpr_write_b32 a170, v42\n" \
  "v_accvgpr_write_b32 a171, v43\n" \
  "v_accvgpr_write_b32 a172, v44\n" \
  "v_accvgpr_write_b32 a173, v45\n" \
  "v_accvgpr_write_b32 a174, v46\n" \
  "v_accvgpr_write_b32 a175, v47\n" \
  "s_waitcnt vmcnt(0) lgkmcnt(0)\n" \
  "s_branch .Ldn_sel\n" \
  ".Ldn_q:\n" \
  "s_branch .Ldn_pq_c\n" \
  ".Ldn_pq:\n" \
  ".Ldn_pq_f:\n" \
  "buffer_load_dword v62, v30, s[36:39], 0 offen sc1\n" \
  "s_sub_u32 s70, s70, 1\n" \
  "s_cselect_b32 s77, -1, s77\n" \
  "s_cselect_b32 s70, 0, s70\n" \
  "s_waitcnt vmcnt(0)\n" \
  ".Ldn_pq_c:\n" \
  "v_cmp_gt_i32 vcc, s77, v62\n" \
  "s_and_b32 vcc_lo, vcc_lo, s29\n" \
  "s_cbranch_scc1 .Ldn_pq\n" \
  "s_barrier\n" \
  "buffer_load_dwordx4 v[64:67], v0, s[36:39], s61 offen sc1\n" \
  "v_mfma_f32_16x16x32_bf16 v[32:35], a[0:3], v[112:115], 0\n" \
  "buffer_load_dwordx4 v[68:71], v1, s[36:39], s61 offen sc1\n" \
  "v_mfma_f32_16x16x32_bf16 v[36:39], a[4:7], v[112:115], 0\n" \
  "buffer_load_dwordx4 v[72:75], v2, s[36:39], s61 offen sc1\n" \
  "v_mfma_f32_16x16x32_bf16 v[40:43], a[8:11], v[112:115], 0\n" \
  "buffer_load_dwordx4 v[76:79], v3, s[36:39], s61 offen sc1\n" \
  "v_mfma_f32_16x16x32_bf16 v[44:47], a[12:15], v[112:115], 0\n" \
  "buffer_load_dwordx4 v[80:83], v4, s[36:39], s61 offen sc1\n" \
  "v_mfma_f32_16x16x32_bf16 v[32:35], a[16:19], v[116:119], v[32:35]\n" \
  "buffer_load_dwordx4 v[84:87], v5, s[36:39], s61 offen sc1\n" \
  "v_mfma_f32_16x16x32_bf16 v[36:39], a[20:23], v[116:119], v[36:39]\n" \
  "buffer_load_dwordx4 v[88:91], v6, s[36:39], s61 offen sc1\n" \
  "v_mfma_f32_16x16x32_bf16 v[40:43], a[24:27], v[116:119], v[40:43]\n" \
  "buffer_load_dwordx4 v[92:95], v7, s[36:39], s61 offen sc1\n" \
  "v_mfma_f32_16x16x32_bf16 v[44:47], a[28:31], v[116:119], v[44:47]\n" \
  "buffer_load_dwordx4 v[96:99], v21, s[36:39], s61 offen sc1\n" \
  "v_mfma_f32_16x16x32_bf16 v[32:35], a[32:35], v[120:123], v[32:35]\n" \
  "buffer_load_dwordx4 v[100:103], v22, s[36:39], s61 offen sc1\n" \
  "v_mfma_f32_16x16x32_bf16 v[36:39], a[36:39], v[120:123], v[36:39]\n" \
  "buffer_load_dwordx4 v[104:107], v23, s[36:39], s61 offen sc1\n" \
  "v_mfma_f32_16x16x32_bf16 v[40:43], a[40:43], v[120:123], v[40:43]\n" \
  "buffer_load_dwordx2 v[108:109], v24, s[36:39], s61 offen sc1\n" \
  "v_mfma_f32_16x16x32_bf16 v[44:47], a[44:47], v[120:123], v[44:47]\n" \
  "v_mfma_f32_16x16x32_bf16 v[32:35], a[48:51], v[124:127], v[32:35]\n" \
  "v_cmp_gt_i32 vcc, s63, v62\n" \
  "s_cmp_eq_u32 vcc_hi, 0\n" \
  "v_mfma_f32_16x16x32_bf16 v[36:39], a[52:55], v[124:127], v[36:39]\n" \
  "s_cselect_b32 s72, s64, 0x40000000\n" \
  "v_cmp_gt_i32 vcc, s31, v62\n" \
  "s_cmp_lg_u32 vcc_hi, 0\n" \
  "s_addc_u32 s72, s72, 0\n" \
  "v_mov_b32 v31, s72\n" \
  "v_mfma_f32_16x16x32_bf16 v[40:43], a[56:59], v[124:127], v[40:43]\n" \
  "ds_write_b32 v15, v31\n" \
  "v_mfma_f32_16x16x32_bf16 v[44:47], a[60:63], v[124:127], v[44:47]\n" \
  "v_mfma_f32_16x16x32_bf16 v[32:35], a[64:67], v[128:131], v[32:35]\n" \
  "v_mfma_f32_16x16x32_bf16 v[36:39], a[68:71], v[128:131], v[36:39]\n" \
  "v_mfma_f32_16x16x32_bf16 v[40:43], a[72:75], v[128:131], v[40:43]\n" \
  "v_mfma_f32_16x16x32_bf16 v[44:47], a[76:79], v[128:131], v[44:47]\n" \
  "v_mfma_f32_16x16x32_bf16 v[32:35], a[80:83], v[132:135], v[32:35]\n" \
  "v_mfma_f32_16x16x32_bf16 v[36:39], a[84:87], v[132:135], v[36:39]\n" \
  "v_mfma_f32_16x16x32_bf16 v[40:43], a[88:91], v[132:135], v[40:43]\n" \
  "v_mfma_f32_16x16x32_bf16 v[44:47], a[92:95], v[132:135], v[44:47]\n" \
  "v_mfma_f32_16x16x32_bf16 v[32:35], a[96:99], v[136:139], v[32:35]\n" \
  "v_mfma_f32_16x16x32_bf16 v[36:39], a[100:103], v[136:139], v[36:39]\n" \
  "v_mfma_f32_16x16x32_bf16 v[40:43], a[104:107], v[136:139], v[40:43]\n" \
  "v_mfma_f32_16x16x32_bf16 v[44:47], a[108:111], v[136:139], v[44:47]\n" \
  "v_mfma_f32_16x16x32_bf16 v[32:35], a[112:115], v[140:143], v[32:35]\n" \
  "v_mfma_f32_16x16x32_bf16 v[36:39], a[116:119], v[140:143], v[36:39]\n" \
  "v_mfma_f32_16x16x32_bf16 v[40:43], a[120:123], v[140:143], v[40:43]\n" \
  "v_mfma_f32_16x16x32_bf16 v[44:47], a[124:127], v[140:143], v[44:47]\n" \
  "v_mfma_f32_16x16x32_bf16 v[32:35], a[128:131], v[144:147], v[32:35]\n" \
  "v_mfma_f32_16x16x32_bf16 v[36:39], a[132:135], v[144:147], v[36:39]\n" \
  "v_mfma_f32_16x16x32_bf16 v[40:43], a[136:139], v[144:147], v[40:43]\n" \
  "v_mfma_f32_16x16x32_bf16 v[44:47], a[140:143], v[144:147], v[44:47]\n" \
  "v_mfma_f32_16x16x32_bf16 v[32:35], a[144:147], v[148:151], v[32:35]\n" \
  "v_mfma_f32_16x16x32_bf16 v[36:39], a[148:151], v[148:151], v[36:39]\n" \
  "v_mfma_f32_16x16x32_bf16 v[40:43], a[152:155], v[148:151], v[40:43]\n" \
  "v_mfma_f32_16x16x32_bf16 v[44:47], a[156:159], v[148:151], v[44:47]\n" \
  "v_mfma_f32_16x16x32_bf16 v[32:35], a[160:163], v[152:155], v[32:35]\n" \
  "v_mfma_f32_16x16x32_bf16 v[36:39], a[164:167], v[152:155], v[36:39]\n" \
  "v_mfma_f32_16x16x32_bf16 v[40:43], a[168:171], v[152:155], v[40:43]\n" \
  "v_mfma_f32_16x16x32_bf16 v[44:47], a[172:175], v[152:155], v[44:47]\n" \
  "v_pk_mov_b32 v[176:177], v[32:33], v[36:37] op_sel:[0,0]\n" \
  "v_pk_mov_b32 v[180:181], v[32:33], v[36:37] op_sel:[1,1]\n" \
  "v_pk_mov_b32 v[184:185], v[34:35], v[38:39] op_sel:[0,0]\n" \
  "v_pk_mov_b32 v[188:189], v[34:35], v[38:39] op_sel:[1,1]\n" \
  "s_nop 3\n" \
  "v_pk_mov_b32 v[178:179], v[40:41], v[44:45] op_sel:[0,0]\n" \
  "ds_write_b128 v25, v[176:179] offset:0\n" \
  "v_pk_mov_b32 v[182:183], v[40:41], v[44:45] op_sel:[1,1]\n" \
  "ds_write_b128 v25, v[180:183] offset:16\n" \
  "v_pk_mov_b32 v[186:187], v[42:43], v[46:47] op_sel:[0,0]\n" \
  "ds_write_b128 v25, v[184:187] offset:32\n" \
  "v_pk_mov_b32 v[190:191], v[42:43], v[46:47] op_sel:[1,1]\n" \
  "ds_write_b128 v25, v[188:191] offset:48\n" \
  "s_waitcnt lgkmcnt(0)\n" \
  "s_barrier\n" \
  "ds_read_b128 v[32:35], v26 offset:0\n" \
  "ds_read_b128 v[36:39], v26 offset:4352\n" \
  "ds_read_b128 v[40:43], v26 offset:8704\n" \
  "ds_read_b128 v[44:47], v26 offset:13056\n" \
  "v_lshlrev_b32 v176, 16, v156\n" \
  "v_and_b32 v177, 0xffff0000, v156\n" \
  "v_lshlrev_b32 v178, 16, v157\n" \
  "v_and_b32 v179, 0xffff0000, v157\n" \
  "s_waitcnt lgkmcnt(3)\n" \
  "v_pk_add_f32 v[176:177], v[176:177], v[32:33]\n" \
  "v_pk_add_f32 v[178:179], v[178:179], v[34:35]\n" \
  "s_waitcnt lgkmcnt(2)\n" \
  "v_pk_add_f32 v[176:177], v[176:177], v[36:37]\n" \
  "v_pk_add_f32 v[178:179], v[178:179], v[38:39]\n" \
  "s_waitcnt lgkmcnt(1)\n" \
  "v_pk_add_f32 v[176:177], v[176:177], v[40:41]\n" \
  "v_pk_add_f32 v[178:179], v[178:179], v[42:43]\n" \
  "s_waitcnt lgkmcnt(0)\n" \
  "v_pk_add_f32 v[176:177], v[176:177], v[44:45]\n" \
  "v_pk_add_f32 v[178:179], v[178:179], v[46:47]\n" \
  "v_cvt_pk_bf16_f32 v48, v176, v177\n" \
  "v_cvt_pk_bf16_f32 v49, v178, v179\n" \
  "s_nop 0\n" \
  "v_mov_b32_dpp v50, v48 row_ror:8 row_mask:0xf bank_mask:0xf\n" \
  "v_mov_b32_dpp v51, v49 row_ror:8 row_mask:0xf bank_mask:0xf\n" \
  "buffer_store_dwordx4 v[48:51], v12, s[40:43], s66 offen sc1\n" \
  "s_cmp_ge_u32 s60, s50\n" \
  "s_cbranch_scc1 .Ldn_last\n" \
  ".Ldn_p:\n" \
  "s_barrier\n" \
  "ds_read_b32 v31, v28\n" \
  "s_waitcnt vmcnt(12)\n" \
  "v_mfma_f32_16x16x32_bf16 v[32:35], a[0:3], v[64:67], 0\n" \
  "v_mfma_f32_16x16x32_bf16 v[36:39], a[4:7], v[64:67], 0\n" \
  "v_mfma_f32_16x16x32_bf16 v[40:43], a[8:11], v[64:67], 0\n" \
  "v_mfma_f32_16x16x32_bf16 v[44:47], a[12:15], v[64:67], 0\n" \
  "s_waitcnt vmcnt(11)\n" \
  "v_mfma_f32_16x16x32_bf16 v[32:35], a[16:19], v[68:71], v[32:35]\n" \
  "s_waitcnt lgkmcnt(0)\n" \
  "v_readfirstlane_b32 s69, v31\n" \
  "s_and_b32 s30, s69, 1\n" \
  "s_bitset0_b32 s69, 0\n" \
  "v_mfma_f32_16x16x32_bf16 v[36:39], a[20:23], v[68:71], v[36:39]\n" \
  "v_mfma_f32_16x16x32_bf16 v[40:43], a[24:27], v[68:71], v[40:43]\n" \
  "s_nop 0\n" \
  "buffer_load_dwordx4 v[112:115], v0, s[36:39], s69 offen sc1\n" \
  "v_mfma_f32_16x16x32_bf16 v[44:47], a[28:31], v[68:71], v[44:47]\n" \
  "s_waitcnt vmcnt(11)\n" \
  "v_mfma_f32_16x16x32_bf16 v[32:35], a[32:35], v[72:75], v[32:35]\n" \
  "v_mfma_f32_16x16x32_bf16 v[36:39], a[36:39], v[72:75], v[36:39]\n" \
  "buffer_load_dwordx4 v[116:119], v1, s[36:39], s69 offen sc1\n" \
  "v_mfma_f32_16x16x32_bf16 v[40:43], a[40:43], v[72:75], v[40:43]\n" \
  "v_mfma_f32_16x16x32_bf16 v[44:47], a[44:47], v[72:75], v[44:47]\n" \
  "s_waitcnt vmcnt(11)\n" \
  "v_mfma_f32_16x16x32_bf16 v[32:35], a[48:51], v[76:79], v[32:35]\n" \
  "buffer_load_dwordx4 v[120:123], v2, s[36:39], s69 offen sc1\n" \
  "v_mfma_f32_16x16x32_bf16 v[36:39], a[52:55], v[76:79], v[36:39]\n" \
  "v_mfma_f32_16x16x32_bf16 v[40:43], a[56:59], v[76:79], v[40:43]\n" \
  "v_mfma_f32_16x16x32_bf16 v[44:47], a[60:63], v[76:79], v[44:47]\n" \
  "buffer_load_dwordx4 v[124:127], v3, s[36:39], s69 offen sc1\n" \
  "s_waitcnt vmcnt(12)\n" \
  "v_mfma_f32_16x16x32_bf16 v[32:35], a[64:67], v[80:83], v[32:35]\n" \
  "v_mfma_f32_16x16x32_bf16 v[36:39], a[68:71], v[80:83], v[36:39]\n" \
  "v_mfma_f32_16x16x32_bf16 v[40:43], a[72:75], v[80:83], v[40:43]\n" \
  "buffer_load_dwordx4 v[128:131], v4, s[36:39], s69 offen sc1\n" \
  "v_mfma_f32_16x16x32_bf16 v[44:47], a[76:79], v[80:83], v[44:47]\n" \
  "s_waitcnt vmcnt(12)\n" \
  "v_mfma_f32_16x16x32_bf16 v[32:35], a[80:83], v[84:87], v[32:35]\n" \
  "v_mfma_f32_16x16x32_bf16 v[36:39], a[84:87], v[84:87], v[36:39]\n" \
  "buffer_load_dwordx4 v[132:135], v5, s[36:39], s69 offen sc1\n" \
  "v_mfma_f32_16x16x32_bf16 v[40:43], a[88:91], v[84:87], v[40:43]\n" \
  "v_mfma_f32_16x16x32_bf16 v[44:47], a[92:95], v[84:87], v[44:47]\n" \
  "s_waitcnt vmcnt(12)\n" \
  "v_mfma_f32_16x16x32_bf16 v[32:35], a[96:99], v[88:91], v[32:35]\n" \
  "buffer_load_dwordx4 v[136:139], v6, s[36:39], s69 offen sc1\n" \
  "v_mfma_f32_16x16x32_bf16 v[36:39], a[100:103], v[88:91], v[36:39]\n" \
  "v_mfma_f32_16x16x32_bf16 v[40:43], a[104:107], v[88:91], v[40:43]\n" \
  "v_mfma_f32_16x16x32_bf16 v[44:47], a[108:111], v[88:91], v[44:47]\n" \
  "buffer_load_dwordx4 v[140:143], v7, s[36:39], s69 offen sc1\n" \
  "s_waitcnt vmcnt(13)\n" \
  "v_mfma_f32_16x16x32_bf16 v[32:35], a[112:115], v[92:95], v[32:35]\n" \
  "v_mfma_f32_16x16x32_bf16 v[36:39], a[116:119], v[92:95], v[36:39]\n" \
  "v_mfma_f32_16x16x32_bf16 v[40:43], a[120:123], v[92:95], v[40:43]\n" \
  "buffer_load_dwordx4 v[144:147], v21, s[36:39], s69 offen sc1\n" \
  "v_mfma_f32_16x16x32_bf16 v[44:47], a[124:127], v[92:95], v[44:47]\n" \
  "s_waitcnt vmcnt(13)\n" \
  "v_mfma_f32_16x16x32_bf16 v[32:35], a[128:131], v[96:99], v[32:35]\n" \
  "v_mfma_f32_16x16x32_bf16 v[36:39], a[132:135], v[96:99], v[36:39]\n" \
  "buffer_load_dwordx4 v[148:151], v22, s[36:39], s69 offen sc1\n" \
  "v_mfma_f32_16x16x32_bf16 v[40:43], a[136:139], v[96:99], v[40:43]\n" \
  "v_mfma_f32_16x16x32_bf16 v[44:47], a[140:143], v[96:99], v[44:47]\n" \
  "s_waitcnt vmcnt(13)\n" \
  "v_mfma_f32_16x16x32_bf16 v[32:35], a[144:147], v[100:103], v[32:35]\n" \
  "buffer_load_dwordx4 v[152:155], v23, s[36:39], s69 offen sc1\n" \
  "v_mfma_f32_16x16x32_bf16 v[36:39], a[148:151], v[100:103], v[36:39]\n" \
  "v_mfma_f32_16x16x32_bf16 v[40:43], a[152:155], v[100:103], v[40:43]\n" \
  "v_mfma_f32_16x16x32_bf16 v[44:47], a[156:159], v[100:103], v[44:47]\n" \
  "buffer_load_dwordx2 v[156:157], v24, s[36:39], s69 offen sc1\n" \
  "s_waitcnt vmcnt(14)\n" \
  "v_mfma_f32_16x16x32_bf16 v[32:35], a[160:163], v[104:107], v[32:35]\n" \
  "v_mfma_f32_16x16x32_bf16 v[36:39], a[164:167], v[104:107], v[36:39]\n" \
  "v_mfma_f32_16x16x32_bf16 v[40:43], a[168:171], v[104:107], v[40:43]\n" \
  "v_mfma_f32_16x16x32_bf16 v[44:47], a[172:175], v[104:107], v[44:47]\n" \
  "v_pk_mov_b32 v[176:177], v[32:33], v[36:37] op_sel:[0,0]\n" \
  "v_pk_mov_b32 v[180:181], v[32:33], v[36:37] op_sel:[1,1]\n" \
  "v_pk_mov_b32 v[184:185], v[34:35], v[38:39] op_sel:[0,0]\n" \
  "v_pk_mov_b32 v[188:189], v[34:35], v[38:39] op_sel:[1,1]\n" \
  "s_nop 3\n" \
  "v_pk_mov_b32 v[178:179], v[40:41], v[44:45] op_sel:[0,0]\n" \
  "ds_write_b128 v25, v[176:179] offset:0\n" \
  "v_pk_mov_b32 v[182:183], v[40:41], v[44:45] op_sel:[1,1]\n" \
  "ds_write_b128 v25, v[180:183] offset:16\n" \
  "v_pk_mov_b32 v[186:187], v[42:43], v[46:47] op_sel:[0,0]\n" \
  "ds_write_b128 v25, v[184:187] offset:32\n" \
  "v_pk_mov_b32 v[190:191], v[42:43], v[46:47] op_sel:[1,1]\n" \
  "ds_write_b128 v25, v[188:191] offset:48\n" \
  "s_waitcnt lgkmcnt(0)\n" \
  "s_barrier\n" \
  "ds_read_b128 v[32:35], v26 offset:0\n" \
  "ds_read_b128 v[36:39], v26 offset:4352\n" \
  "ds_read_b128 v[40:43], v26 offset:8704\n" \
  "ds_read_b128 v[44:47], v26 offset:13056\n" \
  "s_waitcnt vmcnt(13)\n" \
  "v_lshlrev_b32 v176, 16, v108\n" \
  "v_and_b32 v177, 0xffff0000, v108\n" \
  "v_lshlrev_b32 v178, 16, v109\n" \
  "v_and_b32 v179, 0xffff0000, v109\n" \
  "s_waitcnt lgkmcnt(3)\n" \
  "v_pk_add_f32 v[176:177], v[176:177], v[32:33]\n" \
  "v_pk_add_f32 v[178:179], v[178:179], v[34:35]\n" \
  "s_waitcnt lgkmcnt(2)\n" \
  "v_pk_add_f32 v[176:177], v[176:177], v[36:37]\n" \
  "v_pk_add_f32 v[178:179], v[178:179], v[38:39]\n" \
  "s_waitcnt lgkmcnt(1)\n" \
  "v_pk_add_f32 v[176:177], v[176:177], v[40:41]\n" \
  "v_pk_add_f32 v[178:179], v[178:179], v[42:43]\n" \
  "s_waitcnt lgkmcnt(0)\n" \
  "v_pk_add_f32 v[176:177], v[176:177], v[44:45]\n" \
  "v_pk_add_f32 v[178:179], v[178:179], v[46:47]\n" \
  "v_cvt_pk_bf16_f32 v48, v176, v177\n" \
  "v_cvt_pk_bf16_f32 v49, v178, v179\n" \
  "s_nop 0\n" \
  "v_mov_b32_dpp v50, v48 row_ror:8 row_mask:0xf bank_mask:0xf\n" \
  "v_mov_b32_dpp v51, v49 row_ror:8 row_mask:0xf bank_mask:0xf\n" \
  "buffer_store_dwordx4 v[48:51], v12, s[52:55], s62 offen sc1\n" \
  "v_lshlrev_b32 v176, 16, v48\n" \
  "v_and_b32 v177, 0xffff0000, v48\n" \
  "v_lshlrev_b32 v178, 16, v49\n" \
  "v_and_b32 v179, 0xffff0000, v49\n" \
  "v_pk_mul_f32 v[180:181], v[176:177], v[176:177]\n" \
  "v_pk_add_f32 v[182:183], v[176:177], v[178:179]\n" \
  "v_pk_fma_f32 v[180:181], v[178:179], v[178:179], v[180:181]\n" \
  "v_add_f32 v176, v182, v183\n" \
  "v_add_f32 v177, v180, v181\n" \
  "s_nop 0\n" \
  "v_add_f32_dpp v176, v176, v176 row_ror:8 row_mask:0xf bank_mask:0xf\n" \
  "v_add_f32_dpp v177, v177, v177 row_ror:8 row_mask:0xf bank_mask:0xf\n" \
  "s_nop 0\n" \
  "v_add_f32_dpp v176, v176, v176 row_ror:4 row_mask:0xf bank_mask:0xf\n" \
  "v_add_f32_dpp v177, v177, v177 row_ror:4 row_mask:0xf bank_mask:0xf\n" \
  "s_nop 0\n" \
  "v_add_f32_dpp v176, v176, v176 row_ror:2 row_mask:0xf bank_mask:0xf\n" \
  "v_add_f32_dpp v177, v177, v177 row_ror:2 row_mask:0xf bank_mask:0xf\n" \
  "s_nop 0\n" \
  "v_add_f32_dpp v176, v176, v176 row_ror:1 row_mask:0xf bank_mask:0xf\n" \
  "v_add_f32_dpp v177, v177, v177 row_ror:1 row_mask:0xf bank_mask:0xf\n" \
  "ds_write_b64 v8, v[176:177]\n" \
  "s_waitcnt lgkmcnt(0)\n" \
  "s_barrier\n" \
  "ds_read_b128 v[176:179], v9\n" \
  "s_waitcnt lgkmcnt(0)\n" \
  "buffer_store_dwordx4 v[176:179], v27, s[36:39], s47 offen sc1\n" \
  ".Ldn_sel:\n" \
  "s_mov_b32 s76, s59\n" \
  "s_cmp_lg_u32 s69, 0x40000000\n" \
  "s_cselect_b32 s30, s30, 0\n" \
  "s_addc_u32 s59, s59, 0\n" \
  "s_cmp_lt_u32 s58, s50\n" \
  "s_cselect_b64 s[78:79], -1, 0\n" \
  "v_cndmask_b32_e64 v30, v14, v13, s[78:79]\n" \
  "buffer_load_dword v62, v30, s[36:39], 0 offen sc1\n" \
  "s_cselect_b32 s60, s58, s59\n" \
  "s_cselect_b64 s[52:53], s[44:45], s[40:41]\n" \
  "s_cselect_b32 s54, s46, s42\n" \
  "s_cselect_b32 s75, s48, 0x40000000\n" \
  "s_cselect_b32 s71, 0, 1\n" \
  "v_cndmask_b32_e64 v32, v17, v16, s[78:79]\n" \
  "v_cndmask_b32_e64 v33, v19, v18, s[78:79]\n" \
  "s_addc_u32 s58, s58, 0\n" \
  "s_cmp_lg_u32 s30, 0\n" \
  "s_cselect_b32 s60, 64, s60\n" \
  "s_cselect_b32 s71, 0, s71\n" \
  "s_add_u32 s59, s59, s71\n" \
  "s_mov_b32 s66, s65\n" \
  "v_readlane_b32 s61, v32, s60\n" \
  "v_readlane_b32 s62, v33, s60\n" \
  "v_readlane_b32 s64, v17, s59\n" \
  "v_readlane_b32 s65, v19, s59\n" \
  "s_mov_b32 s63, s59\n" \
  "s_add_u32 s31, s59, 1\n" \
  "s_cmp_lt_u32 s58, s50\n" \
  "s_cselect_b32 s31, -1, s31\n" \
  "s_cmp_eq_u32 s71, 0\n" \
  "s_cselect_b32 s47, s62, 0x40000000\n" \
  "s_cmp_lt_u32 s59, s50\n" \
  "s_cselect_b32 s63, s63, 64\n" \
  "s_cmp_lt_u32 s60, s50\n" \
  "s_cselect_b32 s77, s60, -1\n" \
  "s_cselect_b32 s61, s61, 0x40000000\n" \
  "s_cselect_b32 s75, s75, 0x40000000\n" \
  "s_cselect_b32 s71, s71, 0\n" \
  "s_cmp_eq_u32 s71, 0\n" \
  "s_cselect_b32 s72, s49, 0x40000000\n" \
  "s_mov_b32 s48, s49\n" \
  "s_mov_b32 s72, 0x40000000\n" \
  "s_waitcnt vmcnt(1)\n" \
  "s_barrier\n" \
  "buffer_store_dword v60, v29, s[36:39], s67 offen sc1\n" \
  "buffer_store_dword v61, v29, s[36:39], s68 offen sc1\n" \
  "s_waitcnt vmcnt(2)\n" \
  "v_mov_b32 v60, s60\n" \
  "v_mov_b32 v61, s76\n" \
  "s_mov_b32 s67, s75\n" \
  "s_mov_b32 s68, s72\n" \
  "s_cmp_lg_u32 s69, 0x40000000\n" \
  "s_cbranch_scc1 .Ldn_q\n" \
  "s_mov_b32 s68, 0x40000000\n" \
  "s_cmp_ge_u32 s60, s50\n" \
  "s_cbranch_scc1 .Ldn_end\n" \
  "s_branch .Ldn_pn_c\n" \
  ".Ldn_pn:\n" \
  ".Ldn_pn_f:\n" \
  "buffer_load_dword v62, v30, s[36:39], 0 offen sc1\n" \
  "s_sub_u32 s70, s70, 1\n" \
  "s_cselect_b32 s77, -1, s77\n" \
  "s_cselect_b32 s70, 0, s70\n" \
  "s_waitcnt vmcnt(0)\n" \
  ".Ldn_pn_c:\n" \
  "v_cmp_gt_i32 vcc, s77, v62\n" \
  "s_and_b32 vcc_lo, vcc_lo, s29\n" \
  "s_cbranch_scc1 .Ldn_pn\n" \
  "s_barrier\n" \
  "buffer_load_dwordx4 v[64:67], v0, s[36:39], s61 offen sc1\n" \
  "buffer_load_dwordx4 v[68:71], v1, s[36:39], s61 offen sc1\n" \
  "buffer_load_dwordx4 v[72:75], v2, s[36:39], s61 offen sc1\n" \
  "buffer_load_dwordx4 v[76:79], v3, s[36:39], s61 offen sc1\n" \
  "buffer_load_dwordx4 v[80:83], v4, s[36:39], s61 offen sc1\n" \
  "buffer_load_dwordx4 v[84:87], v5, s[36:39], s61 offen sc1\n" \
  "buffer_load_dwordx4 v[88:91], v6, s[36:39], s61 offen sc1\n" \
  "buffer_load_dwordx4 v[92:95], v7, s[36:39], s61 offen sc1\n" \
  "buffer_load_dwordx4 v[96:99], v21, s[36:39], s61 offen sc1\n" \
  "buffer_load_dwordx4 v[100:103], v22, s[36:39], s61 offen sc1\n" \
  "buffer_load_dwordx4 v[104:107], v23, s[36:39], s61 offen sc1\n" \
  "buffer_load_dwordx2 v[108:109], v24, s[36:39], s61 offen sc1\n" \
  "buffer_store_dword v60, v29, s[36:39], s68 offen sc1\n" \
  "v_cmp_gt_i32 vcc, s63, v62\n" \
  "s_cmp_eq_u32 vcc_hi, 0\n" \
  "s_cselect_b32 s72, s64, 0x40000000\n" \
  "v_cmp_gt_i32 vcc, s31, v62\n" \
  "s_cmp_lg_u32 vcc_hi, 0\n" \
  "s_addc_u32 s72, s72, 0\n" \
  "v_mov_b32 v31, s72\n" \
  "ds_write_b32 v15, v31\n" \
  "s_waitcnt lgkmcnt(0)\n" \
  "s_branch .Ldn_p\n" \
  ".Ldn_last:\n" \
  "s_mov_b32 s69, 0x40000000\n" \
  "s_branch .Ldn_sel\n" \
  ".Ldn_end:\n" \
  "s_endpgm\n"
#define HX 0
#define RX 0
#define DROP(b) 0
#define SWAP 0
#define SW0 18
#define WSW (SW0 >= 10 ? SWAP : 0)
#define LN1 0
#define DSW 58
DEVI u4 srd_of(const void* p, unsigned bytes) { return u4{lo32(p), hi32(p), bytes, 0x20000u}; }
DEVI void dma_g(const void* src, unsigned lds) {
  asm volatile("s_mov_b32 m0, %0\n\ts_nop 0\n\tglobal_load_lds_dwordx4 %1, off" ::"s"(lds), "v"(src) : "memory");
}
DEVI float blo(unsigned x) { return asf(x << 16); }
DEVI float bhi(unsigned x) { return asf(x & 0xffff0000u); }

constexpr int L_DEC = 140000 / 16 * 16;
DEVI int poll(u4 rs, int voff) {
  int v; asm volatile("buffer_load_dword %0, %1, %2, 0 offen sc1\n\ts_waitcnt vmcnt(0)" : "=v"(v) : "v"(voff), "s"(rs) : "memory"); return v;
}
DEVI unsigned get_go(const Params& p) {
  const rsrc_t rw = mkrs(p.ws, END_OFF);
  u4 v = ld16c(rw, 0, CTL_OFF);
  for (int i = 0; i < (1 << 14) && anyl((((v.x + 16) ^ v.z) >> 4 | ~(v.y ^ v.w)) != 0); ++i) { __builtin_amdgcn_s_sleep(4); MEMB(); v = ld16c(rw, 0, CTL_OFF); }
  return __builtin_amdgcn_readfirstlane(v.z);
}
DEVI int get_par(const Params& p) { return get_go(p) >> 2 & 1; }
DEVI void pause(unsigned t0, unsigned n) {
  for (int i = 0; i < 256 && (unsigned)__builtin_amdgcn_s_memrealtime() - t0 < n * 50; ++i) __builtin_amdgcn_s_sleep(4);
}
enum { K_QK, K_V, K_WO, K_FF, K_DN };
template <int KIND> DEVI void gemm_role(const Params& p, int id, int rot) {
  const int tid = threadIdx.x, l = tid & 63, w = __builtin_amdgcn_readfirstlane(tid >> 6);
  const int g = l >> 4, c = l & 15, tau = w & 1, hf = w >> 1;
  const int c8 = (c & 7) * 16 + (c >> 3) * 8;
  constexpr bool QKV = KIND <= K_V;
  constexpr int LA = QKV ? 1 : 0, LB = 1 - LA;
  constexpr bool LN = QKV && FK > 1;
  u8v tra = {}, vo, m0v; u4 wrd = {0, 0, 0, 0x20000u}, tab; u8v pv;
  if (KIND == K_WO) { wrd = srd_of((const char*)p.wo + (id >> 2) * 512 * 1024, 512 * 1024); tra[0] = g * 4096 + (id & 3) * 256 + hf * 128 + (W4 & 1 ? c8 : c * 8); }
  else if (KIND == K_DN) {
    const int r0 = (FK ? w : (id >> 1) & 3) * 352;
    wrd = srd_of((const char*)p.wd + r0 * 1024, (1376 - r0) * 1024); tra[0] = g * 4096 + (FK ? (id & 7) * 128 : (id & 1) * 512 + w * 128) + c * 8;
  } else {
    constexpr int ldw = QKV ? 8192 : 2752;
    const char* b0 = QKV ? (const char*)(id < 32 ? p.wq : id < 64 ? p.wk : p.wv) + (id & 31) * 256 : (const char*)p.wg;
#pragma unroll
    for (int i = 0; i < 32; ++i) {
      const int ch = c ^ ((g << 2) | (i & 3)), row = 128 * w + 4 * i + g;
      const char* s = QKV ? b0 + ch * 16 : (ch < 8 ? b0 : (const char*)p.wu) + min(id * 128 + (ch & 7) * 16, 2752 - 16);
      dma_g(s + row * ldw, (128 * w + 4 * i) * 256);
    }
#pragma unroll
    for (int i = 0; i < (KIND == K_V ? 4 : 8); ++i) {
      const int t = i >> 1, row = 8 * g + 4 * (i & 1) + (c >> 2);
      const int cb = (KIND == K_QK ? 128 * hf + 32 * t : KIND == K_V ? 64 * w + 32 * t : 128 * (t & 1) + 64 * hf + 32 * (t >> 1)) + 8 * (c & 3);
      tra[i] = 256 * row + 16 * ((cb >> 4) ^ (((row & 3) << 2) | ((row >> 2) & 3))) + (cb & 8);
    }
  }
  const int par = get_par(p);
  drain();
  wgbar();
#pragma unroll
  for (int i = 0; i < 8; ++i) {
    const int j = (i + rot) & 7, ck = ((id >> 1) & 3) * 11 + 3 * w + i;
    if (KIND == K_DN) { vo[i] = 3 * w + i < 11 && ck < 43 ? rowoff((id >> 3) * 43 + ck) + l * 16 : (int)OOB; m0v[i] = (3 * w + i) * 1024; }
    else { vo[i] = (LN ? 0 : w * BLK) + j * 1024 + (KIND == K_WO ? l : l ^ ((w & 1) * 8 + j)) * 16; m0v[i] = w * 8192 + j * 1024; }
  }
#pragma unroll
  for (int i = 0; i < 4; ++i) pv[i] = KIND == K_WO ? tau * 4096 + l * 16 : KIND == K_DN ? l * 16 : ((KIND == K_V ? 0 : 16 * tau) + c) * 1024 + (((i << 2 | g) ^ c) << 4);
  if (FK && KIND == K_DN) {
#pragma unroll
    for (int i = 0; i < 11; ++i) {
      const int ck = 11 * w + i, o = ck < 43 ? rowoff(ck) + l * 16 : (int)OOB;
      if (i < 8) vo[i] = o; else tra[i - 7] = o;
    }
    tra[4] = B_X1 - B_A + rowoff(4 * w + g) + (id & 7) * 128 + (W4 & 2 ? c8 : c * 8);
    tra[5] = w * 4352 + c * 272 + g * 64; tra[6] = (4 * w + g) * 272 + (W4 & 2 ? c8 * 2 : c * 16);
    tra[7] = tid >= 8 || id >= 16 ? OOB : (4 - 2 * (id >> 3)) * BLK + id * 128 + l * 16;
    pv[0] = 32768 + (4 * w + g) * 8; pv[1] = 32768 + (l & 7) * 16;
    m0v = u8v{lo32(p.out), hi32(p.out), 64 * 32768, 0x20000u, lo32(id < 16 ? p.ws : p.out), hi32(id < 16 ? p.ws : p.out), id < 16 ? END_OFF : 64 * 32768u, 0};
  }
  int own, all, fo;
  const int m = l & 31;
  if (QKV) {
    pv[4] = id * BLK + (KIND == K_V ? w : 4 * tau + 2 * hf) * 1024 + l * 16;
    own = (LN ? m & 15 : 8 * w + (m & 7)) * FS; all = m * FS; fo = (id >> 5) * FS;
  } else if (KIND == K_WO) {
    pv[4] = B_WP + ((4 * tau + g) * 4 + (id >> 3)) * BLK + ((id >> 2) & 1) * 4096 + (id & 3) * 256 + hf * 128 + (W4 & 1 ? (c >> 3) * 1024 + (c & 7) * 16 : c * 8);
    own = ((id & ~3) + w) * FS + (m & 1) * OFS; all = ((id & ~3) + ((m >> 1) & 3)) * FS + (m & 1) * OFS; fo = id * FS;
  } else if (KIND == K_FF) {
    pv[4] = 2 * id + hf < 43 ? B_A + rowoff(tau * AH + 2 * id + hf) + l * 16 : (int)OOB;
    own = (8 * w + (m & 7)) * FS; all = (RS > 1 ? m & 7 : m) * FS; fo = id * FS;
  } else if (FK) {
    pv[4] = W4 & 2 && c >= 8 ? OOB : ((4 * w + g) & 7) * 1024 + (id & 7) * 128 + (W4 & 2 ? c * 16 : c * 8);
    own = (min(11 * w + min(m, 10), 42) >> 1) * FS; all = min(m, 21) * FS; fo = id * FS;
  } else {
    const int kq = (id >> 1) & 3, c0 = min(kq * 11 + min(3 * w + min(m, 2), 10), 42), c1 = min(kq * 11 + min(m, 10), 42);
    pv[4] = ((4 * (id >> 3) + g) * 2 + (kq >> 1)) * BLK + (kq & 1) * 4096 + (id & 1) * 512 + w * 128 + c * 8;
    own = (c0 >> 1) * FS; all = (c1 >> 1) * FS; fo = id * FS;
  }
  if ((SPK >> KIND & 1) && !LN) own = all;
  constexpr int FIN = QKV ? F_XN : KIND == K_WO ? F_O : KIND == K_FF ? F_X1 : F_A, FOUT = KIND == K_WO ? F_WP : KIND == K_FF ? F_A : F_X2;
  const bool hi = w == 0 && l >= 32;
  pv[5] = hi ? flg(FIN, LB, par) + all : flg(FIN, LA, par) + own;
  pv[6] = flg(FIN, LB, par) + (hi ? all : own);
  pv[7] = L_DEC + (w ? 16 : 0);
#pragma unroll
  for (int i = 0; i < 2; ++i) {
    const int it = (i ? LB : LA) * 64 + l;
    tab[i] = QKV ? item_xn(it, par) : KIND == K_WO ? item_o(it, par) + (id >> 2) * 4 * BLK : item_b(it, par) + (KIND == K_FF ? B_XN1 : B_A);
    tab[2 + i] = QKV ? item_q(it, par) : KIND == K_DN ? item_c(it, par) : KIND == K_WO ? item_w(it, par) : item_b(it, par);
  }
  if (FK && KIND == K_DN) {
    const int th = id >> 3;
    tab[3] = l * 32768 + hf * 8192;
    if (th < 2) { tab[0] += th * 8 * BLK; tab[2] = item_x2(l, par) + (2 * th + hf) * BLK; }
    else { tab[0] = tab[1]; tab[2] = tab[3]; pv[5] = pv[6]; }
    if (th) { tab[1] += 8 * BLK; tab[3] += 16384; }
  }
  if (LN) { tab[0] += w * BLK; tab[1] = l * 32768; pv[6] = flg(FIN, LB, par) + all; wrd = srd_of((const char*)p.x + w * 8192, 64 * 32768 - w * 8192); }
  if ((SPK >> KIND & 1) && w) pv[5] = pv[6] = OOB;
  const unsigned st = KIND == K_V ? (c >> 2 & 1) * 1024 + (c >> 3) * 512 + (4 * g + (c & 3)) * 8 : tau * 1024 + g * 256 + c * 8;
  const u2 sta = {(4 - w) * BLK + st, ST0_OFF + st};
  u4 sf; u2 sg = {OOB, (unsigned)p.pad};
  sf[0] = __builtin_amdgcn_readfirstlane((QKV ? flg_qkv(LA, id & 31, par) : flg(FOUT, LA, par)) + fo);
  sf[1] = __builtin_amdgcn_readfirstlane((QKV ? flg_qkv(LB, id & 31, par) : flg(FOUT, LB, par)) + fo);
  sf[2] = p.lead; sf[3] = OOB;
  if (FK && KIND == K_DN) {
    sf[2] = id < 16 ? 64 : DSW; sg[1] = id < 16 && FK > 1 ? DSW : 0;
    if (FK > 1) sf[0] = id < 16 ? __builtin_amdgcn_readfirstlane(flg(F_XN, 1, par) + fo) : sf[1];
    if (id >= 16) sf[1] = OOB;
    else if (F1) sf[1] = sf[0];
  }
  const u4 rs = srd_of(p.ws, END_OFF);
#define GO(A) asm volatile(A : : "{v[0:7]}"(vo), "{v[8:15]}"(pv), "{v[16:19]}"(tab), "{v[20:27]}"(tra), "{v[28:29]}"(u2{L_DEC, tid == 64 ? 0 : OOB}), "{s[36:39]}"(rs), "{s[40:47]}"(m0v), \
                           "{s[48:51]}"(sf), "{s[52:55]}"(wrd), "{s[56:57]}"(sg), "{v[232:233]}"(sta) : "memory", "v255", "a255", "s101")
  if (KIND == K_QK) GO(ASM_QK); else if (KIND == K_V) GO(ASM_V); else if (KIND == K_WO) GO(ASM_WO); else if (KIND == K_FF) GO(ASM_FF); else GO(ASM_DN);
  __builtin_unreachable();
}

DEVI float wsum(float v) {
  v += asf(__builtin_amdgcn_update_dpp(0, asu(v), 0xB1, 0xf, 0xf, true));
  v += asf(__builtin_amdgcn_update_dpp(0, asu(v), 0x4E, 0xf, 0xf, true));
  v += asf(__builtin_amdgcn_update_dpp(0, asu(v), 0x124, 0xf, 0xf, true));
  v += asf(__builtin_amdgcn_update_dpp(0, asu(v), 0x128, 0xf, 0xf, true));
  return sum32(sum16(v));
}
DEVI u4 lnorm(u4 x) {
  float f[8];
#pragma unroll
  for (int i = 0; i < 4; ++i) { f[2 * i] = blo(x[i]); f[2 * i + 1] = bhi(x[i]); }
  if (LN1) {
    const float q = wsum(((f[0] * f[0] + f[1] * f[1]) + (f[2] * f[2] + f[3] * f[3])) + ((f[4] * f[4] + f[5] * f[5]) + (f[6] * f[6] + f[7] * f[7])));
    const float m = wsum(((f[0] + f[1]) + (f[2] + f[3])) + ((f[4] + f[5]) + (f[6] + f[7]))) * (1.f / 512);
    const float r = __builtin_amdgcn_rsqf(fmaxf(q * (1.f / 512) + 1e-5f - m * m, 1e-5f)), n = -m * r;
    return u4{pk(f[0] * r + n, f[1] * r + n), pk(f[2] * r + n, f[3] * r + n), pk(f[4] * r + n, f[5] * r + n), pk(f[6] * r + n, f[7] * r + n)};
  }
  const float m = wsum(((f[0] + f[1]) + (f[2] + f[3])) + ((f[4] + f[5]) + (f[6] + f[7]))) * (1.f / 512);
  float s = 0;
#pragma unroll
  for (int i = 0; i < 8; ++i) { f[i] -= m; s += f[i] * f[i]; }
  const float r = __builtin_amdgcn_rsqf(wsum(s) * (1.f / 512) + 1e-5f);
  return u4{pk(f[0] * r, f[1] * r), pk(f[2] * r, f[3] * r), pk(f[4] * r, f[5] * r), pk(f[6] * r, f[7] * r)};
}
DEVI u2 sums(u4 x) {
  float s = 0, q = 0;
#pragma unroll
  for (int i = 0; i < 4; ++i) { const float a = blo(x[i]), b = bhi(x[i]); s += a + b; q += a * a + b * b; }
#define STEP(c) s += asf(__builtin_amdgcn_update_dpp(0, asu(s), c, 0xf, 0xf, true)), q += asf(__builtin_amdgcn_update_dpp(0, asu(q), c, 0xf, 0xf, true))
  STEP(0xB1); STEP(0x4E); STEP(0x141);
  return u2{asu(s), asu(q)};
}
DEVI void input_sums(const Params& p, int v) {
  const rsrc_t rw = mkrs(p.ws, END_OFF), rx = mkrs(p.x, 64 * 32768);
  const int l = threadIdx.x & 63;
  for (int n = 0; n < 4; ++n) {
    u4 x[16], o;
#pragma unroll
    for (int i = 0; i < 16; ++i) x[i] = ld16(rx, l * 16 + i * 1024, (v + 32 * n) * 16384);
    SB();
#pragma unroll
    for (int i = 0; i < 8; ++i) {
      const u2 a = sums(x[2 * i]), b = sums(x[2 * i + 1]);
      if (i == 0 || (l & 7) == i) o = u4{a.x, a.y, b.x, b.y};
    }
    st16c(o, rw, l * 16, ST0_OFF + ((v >> 1) + 16 * n) * 32768 + (v & 1) * 1024);
    if (n == 0) get_go(p);
    drain();
    st4c(n == 3 ? 63 : (v >> 1) + 16 * n + 15, rw, l ? OOB : 0, flg(F_XN, 0, 0) + v * FS);
  }
}
DEVI int rchunk(int l) { return (l & 15) + 16 * (l >> 5) + 32 * ((l >> 4) & 1); }
struct Sel { u4 all, lo, hi; };
DEVI Sel mksel(int l) {
  const int g = l >> 4, c = l & 15;
  Sel s;
#pragma unroll
  for (int i = 0; i < 4; ++i) {
    s.all[i] = (c >> 3) == (g & 1) && ((c >> 1) & 3) == i ? 0x3f80u << (16 * (c & 1)) : 0u;
    s.lo[i] = g < 2 ? s.all[i] : 0u; s.hi[i] = g < 2 ? 0u : s.all[i];
  }
  return s;
}
template <int NP> DEVI u4 rowsum(const Sel& s, rsrc_t rx, int vx, int sx, rsrc_t rw, int vp, int sp) {
  u4 v[NP];
  const u4 x = ld16c(rx, vx, sx);
#pragma unroll
  for (int k = 0; k < NP; ++k) v[k] = ld16c(rw, vp + (k & 1) * 512, sp + (k >> 1) * BLK);
  SB();
  f4 a[2] = {mfma32(s.lo, x, f4{}), mfma32(s.hi, x, f4{})};
#pragma unroll
  for (int k = 0; k < NP; ++k) a[k & 1] = mfma32(s.all, v[k], a[k & 1]);
  const auto r0 = __builtin_amdgcn_permlane16_swap(pk(a[0][0], a[0][1]), pk(a[1][0], a[1][1]), false, false);
  const auto r1 = __builtin_amdgcn_permlane16_swap(pk(a[0][2], a[0][3]), pk(a[1][2], a[1][3]), false, false);
  return u4{r0[0], r1[0], r0[1], r1[1]};
}
#define NFIRST 24
template <bool REST> DEVI unsigned norm_input(const Params& p, int row) {
  const rsrc_t rw = mkrs(p.ws, END_OFF), rx = mkrs(p.x, 64 * 32768);
  const int l = threadIdx.x & 63, vn = rowoff(row) + l * 16, vx = row * 1024 + l * 16, fl = l < (REST ? 4 : 1) ? flg(F_XN, 0, 0) + (row + l) * FS : OOB;
  constexpr int S0 = REST ? NFIRST : 0, S1 = REST ? 64 : NFIRST, DS = REST ? 1 : 4, DI = REST ? 1024 : 32768;
  unsigned go = 0;
  u4 x[4], n[4];
#pragma unroll
  for (int i = 0; i < 4; ++i) x[i] = ld16(rx, vx, S0 * 32768 + i * DI);
  for (int s = S0; s < S1; s += DS) {
#pragma unroll
    for (int i = 0; i < 4; ++i) n[i] = ld16(rx, vx, (s + DS) * 32768 + i * DI);
    SB();
#pragma unroll
    for (int i = 0; i < 4; ++i) st16c(lnorm(x[i]), rw, vn, REST ? item_xn(s, 0) + i * 1024 : item_xn(s + i, 0));
    if (s == S0) {
      go = get_go(p);
      if (REST) for (int i = 0; i < (1 << 14) && anyl(l < 4 && (int)ld4c(rw, fl, 0) < S0 - 1); ++i) { __builtin_amdgcn_s_sleep(32); MEMB(); }
    }
    drain();
    st4c(s + DS - 1, rw, fl, 0);
#pragma unroll
    for (int i = 0; i < 4; ++i) x[i] = n[i];
  }
  return go;
}
DEVI void reduce_role(const Params& p, int layer, int row) {
  const rsrc_t rw = mkrs(p.ws, END_OFF), rx = layer ? rw : mkrs(p.x, 64 * 32768);
  const u4 rs = srd_of(p.ws, END_OFF);
  const int l = threadIdx.x & 63, vo = rowoff(row) + rchunk(l) * 16 + DROP(11), vx = (layer ? rowoff(row) : row * 1024) + l * 16;
  const unsigned go = get_go(p);
  const int par = go >> 2 & 1;
  const int vp = B_WP + (row >> 2) * 4 * BLK + (l >> 5) * 4096 + (row & 3) * 1024 + (l & 31) * 16 + DROP(10);
  const int fi = flg(F_WP, layer, par) + (l & 31) * FS, fo = flg(F_X1, layer, par) + (RS > 1 ? row >> 2 : row) * FS, l0 = l ? OOB : 0;
  const Sel sel = mksel(l);
  int budget = 1 << 17, v = -1;
  for (int s = 0; s < 64; ++s) {
    const int ib = item_b(layer * 64 + s, par), sx = layer ? item_x2(s, par) : s * 32768;
    if (!RS || !(row & 3)) while (anyl(v < s) && budget > 0) { --budget; if (SLP && (s < TS || !layer)) __builtin_amdgcn_s_sleep(SLP); v = poll(rs, fi); }
    if (RS) wgbar();
    const u4 x1 = rowsum<8>(sel, rx, vx, sx, rw, vp, item_w(layer * 64 + s, par));
    st16c(x1, rw, vo, ib + B_X1 + (FK ? (row >> 4) * 6 * BLK : 0));
    st16c(lnorm(x1), rw, vo, ib + B_XN1);
    drain();
    if (RS > 1) wgbar();
    st4c(s, rw, RS > 1 && (row & 3) != 1 ? OOB : l0, fo);
  }
  if (FK > 1 && layer && row == 0) {
    for (v = -1; anyl(v < 0) && budget > 0; --budget) v = poll(rs, flg(F_X2, 1, par) + (16 + (l & 7)) * FS);
    st4c(go, rw, l0, CTL_OFF);
  }
}
DEVI void norm_role(const Params& p, int row) {
  const rsrc_t rw = mkrs(p.ws, END_OFF), ro = mkrs(p.out, 64 * 32768);
  const u4 rs = srd_of(p.ws, END_OFF);
  const int l = threadIdx.x & 63, vn = rowoff(row) + l * 16, fr = row * FS, l0 = l ? OOB : 0;
  const unsigned go = norm_input<false>(p, row);
  const int par = go >> 2 & 1, vo = rowoff(row) + rchunk(l) * 16 + DROP(13), vq = row * 1024 + rchunk(l) * 16 + DROP(13);
  const int vp = (row >> 2) * 2 * BLK + (l >> 5) * 4096 + (row & 3) * 1024 + (l & 31) * 16 + DROP(12);
  const int fi = flg(F_X2, l >> 5, par) + ((row >> 4) * 8 + (l & 7)) * FS, fo = flg(F_XN, 1, par) + fr;
  const Sel sel = mksel(l);
  int budget = 1 << 15, v = -1, s0 = 0, s1 = 0;
  for (; FK && s0 < 64; ++s0) {
    while ((unsigned)__builtin_amdgcn_ballot_w64(v < s0) != 0 && budget > 0) { --budget; if (SLP) __builtin_amdgcn_s_sleep(SLP); v = poll(rs, fi); }
    st16c(lnorm(ld16c(rw, vn, item_x2(s0, par))), rw, vn, item_xn(64 + s0, par));
    drain();
    st4c(s0, rw, l0, fo);
  }
  while (!FK && s1 < 64) {
    const int b0 = item_b(s0, par), c0 = item_c(s0, par), b1 = item_b(64 + s1, par), c1 = item_c(64 + s1, par);
    bool r0;
    for (;;) {
      r0 = s0 < 64 && (unsigned)__builtin_amdgcn_ballot_w64(v < s0) == 0;
      if (r0 || (__builtin_amdgcn_ballot_w64(v < s1) >> 32) == 0) break;
      if (SLP) __builtin_amdgcn_s_sleep(SLP);
      v = poll(rs, fi);
      if (--budget < 0) { budget = 0; v = 63; }
    }
    if (r0) {
      const u4 x2 = rowsum<4>(sel, rw, vn, b0 + B_X1, rw, vp, c0);
      st16c(lnorm(x2), rw, vo, (b0 ^ XNH) + B_XN);
      st16c(x2, rw, vo, b0 + B_X2);
      drain();
      st4c(s0++, rw, l0, fo);
    } else {
      __builtin_amdgcn_raw_buffer_store_b128(rowsum<4>(sel, rw, vn, b1 + B_X1, rw, vp, c1), ro, vq + s1++ * 32768, 0, SC1);
    }
  }
  if (row == 0) st4c(go, rw, l0, CTL_OFF);
}
DEVI void clear_block(const Params& p, int j) {
  const rsrc_t rw = mkrs(p.ws, END_OFF);
  const u4 e = ld16c(rw, 0, CTL_OFF);
#pragma unroll
  for (int i = 0; i < 16; ++i) st16c(u4{~0u, ~0u, ~0u, ~0u}, rw, threadIdx.x * 16 + i * 1024, (j < 32 ? j : j + 36) * BLK);
  drain();
  __builtin_amdgcn_raw_buffer_store_b64(u2{e.x + 2, ~e.y}, rw, threadIdx.x ? OOB : 0, ARR_OFF + j * 128, SC1);
}
template <int NV> DEVI void warm(const Params& p, int x, int v) {
  const int l = threadIdx.x & 63;
  unsigned d = 0;
  const char *wq = (const char*)p.wq, *wk = (const char*)p.wk, *wv = (const char*)p.wv;
  asm volatile("" : "+s"(wq), "+s"(wk), "+s"(wv));
  for (int n = v; n < 1024; n += NV) {
    const int u = n >> 5, r = (n & 31) * 2 + (l >> 5);
    const char* a = u >= 20 && u < 28 ? (const char*)p.wo + (x << 20) + (n - 640) * 4096 + l * 64
                  : (u < 4 || u >= 28 ? wq : u < 12 ? wk : wv) + (((u + 4) & 7) * 64 + r) * 8192 + x * 2048 + (l & 31) * 64;
    asm volatile("global_load_dword %0, %1, off" : "+v"(d) : "v"(a) : "memory");
  }
  asm volatile("s_waitcnt vmcnt(0)" : "+v"(d) : : "memory");
}
DEVI void warm_code() {
  const char* a; int n; unsigned d = 0;
  asm volatile("s_getpc_b64 %0\n.Lwc:\n\ts_mov_b32 %1, .Lch_end-.Lwc" : "=s"(a), "=s"(n));
  for (int i = (threadIdx.x & 63) * 64; i < n; i += 4096) asm volatile("global_load_dword %0, %1, off" : "+v"(d) : "v"(a + i) : "memory");
  asm volatile("s_waitcnt vmcnt(0)" : "+v"(d) : : "memory");
}
#define NMISS 10
DEVI unsigned probe(rsrc_t rw, int off) {
  const int lane = threadIdx.x & 63;
  drain();
  const unsigned long long t0 = __builtin_amdgcn_s_memtime();
  asm volatile("s_waitcnt lgkmcnt(0)" ::: "memory");
  if (lane < 8) st16c(u4{0, 0, 0, 0}, rw, off + lane * 16, 0);
  drain();
  return __builtin_amdgcn_s_memtime() - t0;
}
DEVI void calibrate(const Params& p, int x, int j, int w, unsigned rt0) {
  const rsrc_t rw = mkrs(p.ws, END_OFF);
  const int l0 = threadIdx.x & 63 ? OOB : 0, at = (CAL2_OFF ^ topo(x)) + j * 128;
  const u4 e = ld16c(rw, 0, CTL_OFF);
  if (w == 0) {
    const unsigned d0 = probe(rw, at ^ (j & 1) << 13), d1 = probe(rw, at ^ (~j & 1) << 13);
    st16c(u4{e.x + 2, ~e.y, j & 1 ? d1 : d0, j & 1 ? d0 : d1}, rw, l0, ARR_OFF + (64 + j) * 128);
  }
  const int hf = get_par(p) << 13;
  unsigned long long m = ~0ull;
  for (int i = w; i < 80 && (unsigned)__builtin_amdgcn_s_memrealtime() - rt0 < TCAL; ++i) {
    const unsigned long long d = probe(rw, at ^ hf ^ (i & 1) << 12 ^ (i & 2) << 13);
    const int sh = 16 * (i & 3);
    if (d < (m >> sh & 0xffff)) m = (m & ~(0xffffull << sh)) | d << sh;
  }
  st16c(u4{e.x + 2, ~e.y, (unsigned)m, (unsigned)(m >> 32)}, rw, l0, ARR_OFF + (128 + 4 * j + w) * 128);
}
DEVI void master(const Params& p) {
  const rsrc_t rw = mkrs(p.ws, END_OFF);
  const int l = threadIdx.x & 63;
  const u4 e = ld16c(rw, 0, CTL_OFF);
  auto cnt = [](bool v) { return __builtin_popcountll(__builtin_amdgcn_ballot_w64(v)); };
  auto miss = [&](u4& v) { const bool n = v.x != e.x + 2 || v.y != ~e.y; if (n) v.z = v.w = ~0u; return n; };
  u4 a, b;
  for (int i = 0; i < (1 << 14); ++i) {
    a = ld16c(rw, l * 128, ARR_OFF); b = ld16c(rw, (l & 31) * 128, ARR_OFF + 8192);
    if (cnt(miss(b)) <= 2 * NMISS && !anyl(miss(a) && l < (int)NCLR)) break;
    __builtin_amdgcn_s_sleep(2); MEMB();
  }
  for (int i = 1; i < 32; i *= 2) { b.z = min(b.z, (unsigned)__shfl_xor(b.z, i)); b.w = min(b.w, (unsigned)__shfl_xor(b.w, i)); }
  unsigned go = ((e.x + 16) & ~15u) | (b.z > b.w) * 4;
  __builtin_amdgcn_raw_buffer_store_b64(u2{go, ~e.y}, rw, l ? OOB : 0, CTL_OFF + 8, SC1);
  for (int i = 0; i < (1 << 14); ++i) {
    a = ld16c(rw, l * 128, ARR_OFF + 16384); b = ld16c(rw, l * 128, ARR_OFF + 24576);
    if (cnt(miss(a)) + cnt(miss(b)) <= 3 * NMISS) break;
    __builtin_amdgcn_s_sleep(8); MEMB();
  }
  u4 m = {min(a.z & 0xffff, b.z & 0xffff), min(a.z >> 16, b.z >> 16), min(a.w & 0xffff, b.w & 0xffff), min(a.w >> 16, b.w >> 16)};
  for (int i = 1; i < 64; i *= 2)
    for (int k = 0; k < 4; ++k) m[k] = min(m[k], (unsigned)__shfl_xor(m[k], i));
  go |= (m.x + m.z > m.y + m.w) * 2 + (m.x + m.y > m.z + m.w) * 8;
  __builtin_amdgcn_raw_buffer_store_b64(u2{go, ~e.y}, rw, l ? OOB : 0, CTL_OFF + 16, SC1);
}

__global__ void __launch_bounds__(256, 1) mega_k(Params p) {
  const unsigned rt0 = __builtin_amdgcn_s_memrealtime();
  int x = blockIdx.x & 7, t = blockIdx.x >> 3;
  if (x < 4 ? t >= SW0 && t < SW0 + SWAP : t >= 32 - SWAP) { t += x < 4 ? 32 - SWAP - SW0 : SW0 + SWAP - 32; x ^= 4; }
  const int w = __builtin_amdgcn_readfirstlane(threadIdx.x >> 6);
  if (w == 2 && t == (x < 4 ? 10 : 0) && (p.pad & 2)) warm_code();
  if (x < 4 && t >= 10) {
    if (w == 0 && (t - 10) * 4 + x < (int)NCLR) clear_block(p, (t - 10) * 4 + x);
    if (w == 1 && t < 31 && (t < SW0 || t >= SW0 + SWAP) && (p.pad & 1)) { pause(rt0, p.pad >> 8); warm<21 - WSW>(p, x, t - (t < SW0 ? 10 : 10 + WSW)); }
    if (t < 31 || x < 3) pause(rt0, p.pairs & 255);
  }
  if (FK > 1 && x < 4 && (t == 8 || t == 9)) { input_sums(p, (x * 2 + t - 8) * 4 + w); pause(rt0, p.pairs >> 8); }
  if (t < 8) {
    if (x >= 4) { calibrate(p, x, (x - 4) * 8 + t, w, rt0); pause(rt0, p.pairs >> 8); }
    chain_role(p, x >> 2, ((x & 3) ^ HX) * 8 + t, (x & 3) ^ RX);
  }
  else if (x >= 4) {
    const int id = HAL ? (HAL == 2 ? 3 - (t >> 3) : (t >> 3) - 1) * 32 + (x - 4) * 8 + (t & 7) : (x - 4) * 24 + t - 8;
    if (id < 64) gemm_role<K_QK>(p, id, t); else gemm_role<K_V>(p, id, t);
  } else if (FK < 2 && t < 10) norm_role(p, (x * 2 + t - 8) * 4 + w);
  else if (t >= 10 && t < 18) gemm_role<K_WO>(p, x * 8 + t - 10, t * 2);
  else if (t >= 10 && t < 22) reduce_role(p, RS ? t & 1 : w >> 1, RS ? (x * 2 + (t - 18 >> 1)) * 4 + w : (x * 4 + t - 18) * 2 + (w & 1));
  else if (t < 26) gemm_role<K_DN>(p, t < 10 ? 8 + x * 2 + t : x * 4 + t - 22, 0);
  else if (t < 31 || x < 2) gemm_role<K_FF>(p, (t - 26) * 4 + x, t);
  else {
    if (x == 3 && w == 0) master(p);
    if (FK < 2) norm_input<true>(p, (x - 2) * 16 + w * 4);
  }
}

extern "C" void kernel_launch(void* const* d_in, const int* in_sizes, int n_in, void* d_out, int out_size, void* d_ws, size_t ws_size, hipStream_t stream) {
  unsigned char* ws = (unsigned char*)(((uintptr_t)d_ws + (2u << 20) - 1) & ~(uintptr_t)((2u << 20) - 1));
  (void)hipFuncSetAttribute((const void*)mega_k, hipFuncAttributeMaxDynamicSharedMemorySize, LDS_BYTES);
  Params p{d_in[0], d_in[1], d_in[2], d_in[3], d_in[4], d_in[5], d_in[8], d_in[9], d_in[10], d_out, ws, PD1, PD2, 0, 0, PLEAD, PD3, PDL, PWARM};
  mega_k<<<256, 256, LDS_BYTES, stream>>>(p);
}
```
